```python
import torch

def _softmax(x, axis=-1):
    return torch.softmax(x, dim=axis)
def _gelu(x):
    return torch.nn.functional.gelu(x, approximate="tanh")
def _silu(x):
    return torch.nn.functional.silu(x)
def _sigmoid(x):
    return torch.sigmoid(x)
def _layernorm(x, axis=-1, eps=1e-5):
    m = x.mean(dim=axis, keepdim=True)
    v = x.var(dim=axis, keepdim=True, unbiased=False)
    return (x - m) / torch.sqrt(v + eps)
def _scatter_add(tgt, idx, src, axis=0):
    return tgt.clone().index_add_(axis, idx, src)
def _segment_sum(src, seg, n):
    out = torch.zeros((n,) + src.shape[1:], dtype=src.dtype)
    return out.index_add_(0, seg, src)
def _pad(x, pw):
    flat = tuple(v for pair in reversed(pw) for v in pair)
    return torch.nn.functional.pad(x, flat)

kernel_name = 'sequential_preLN__linear_attn_block__dense_activation_silu__prompt'

def setup_inputs():

    _w = (lambda a: (lambda t: t.to(torch.bfloat16) if t.is_floating_point() else t)(torch.as_tensor(a)))
    def _A_setup():
        B, S, D = 1, 2048, 640
        return {
            "x": _w((lambda *s: torch.randn(*s, dtype=torch.bfloat16) * 0.05)(B, S, D)),
            "wqkv": _w((lambda *s: torch.randn(*s, dtype=torch.bfloat16) * 0.05)(D, 3 * D)),
            "wo": _w((lambda *s: torch.randn(*s, dtype=torch.bfloat16) * 0.05)(D, D)),
        }
    _A_in = _A_setup()

    def _B_setup():
        B, S, D, F = 1, 2048, 640, 1728
        return {
            "x": _w((lambda *s: torch.randn(*s, dtype=torch.bfloat16) * 0.05)(B, S, D)),
            "w1": _w((lambda *s: torch.randn(*s, dtype=torch.bfloat16) * 0.8)(D, F) * (D ** -0.5)),
            "w2": _w((lambda *s: torch.randn(*s, dtype=torch.bfloat16) * 0.1)(F, D) * (F ** -0.5)),
        }
    _B_in = _B_setup()

    return {
        "A_x": _A_in["x"],
        "A_wqkv": _A_in["wqkv"],
        "A_wo": _A_in["wo"],
        "B_x": _B_in["x"],
        "B_w1": _B_in["w1"],
        "B_w2": _B_in["w2"],
    }

def reference(A_x, A_wqkv, A_wo, B_x, B_w1, B_w2):
    def _A_ref(x, wqkv, wo):
        B, S, D = x.shape
        H = 32; Dh = D // H
        qkv = (lambda x, *d: x.permute(*d))((x @ wqkv).reshape(B, S, 3, H, Dh), 2, 0, 3, 1, 4)
        q, k, v = qkv[0], qkv[1], qkv[2]
        phi = lambda t: torch.where(t > 0, t + 1, torch.exp(t)) + 1e-6
        pq, pk = phi(q), phi(k)
        kv = torch.einsum("bhsd,bhse->bhde", pk, v)
        z = torch.einsum("bhsd->bhd", pk)
        num = torch.einsum("bhqd,bhde->bhqe", pq, kv)
        den = torch.einsum("bhqd,bhd->bhq", pq, z)[..., None] + 1e-6
        o = num / den
        return (lambda x, *d: x.permute(*d))(o, 0, 2, 1, 3).reshape(B, S, D) @ wo

    def _B_ref(x, w1, w2):
        h = torch.einsum("bsd,df->bsf", x, w1)
        h = _silu(h)
        out = torch.einsum("bsf,fd->bsd", h, w2)
        return out

    x = A_x
    h = x + _A_ref(_layernorm(x, axis=-1), A_wqkv, A_wo)
    h2 = h + _B_ref(_layernorm(h, axis=-1), B_w1, B_w2)
    return h2
```

```cpp
#include <hip/hip_runtime.h>
#include <hip/hip_bf16.h>
typedef unsigned short u16;
__device__ __forceinline__ float bf2f(u16 b){ return __uint_as_float(((unsigned)b)<<16); }
__device__ __forceinline__ u16 f2bf(float f){ unsigned u=__float_as_uint(f); u += 0x7fffu + ((u>>16)&1u); return (u16)(u>>16); }
constexpr int S=2048,D=640,F=1728,H=32,DH=20;

__global__ void ln_k(const u16* x, float* y){
  int r=blockIdx.x; __shared__ float red[256];
  float s=0; for(int i=threadIdx.x;i<D;i+=256) s+=bf2f(x[r*D+i]);
  red[threadIdx.x]=s; __syncthreads();
  for(int o=128;o>0;o>>=1){ if(threadIdx.x<o) red[threadIdx.x]+=red[threadIdx.x+o]; __syncthreads(); }
  float m=red[0]/D; __syncthreads();
  float v=0; for(int i=threadIdx.x;i<D;i+=256){ float d=bf2f(x[r*D+i])-m; v+=d*d; }
  red[threadIdx.x]=v; __syncthreads();
  for(int o=128;o>0;o>>=1){ if(threadIdx.x<o) red[threadIdx.x]+=red[threadIdx.x+o]; __syncthreads(); }
  float rs=rsqrtf(red[0]/D+1e-5f);
  for(int i=threadIdx.x;i<D;i+=256) y[r*D+i]=bf2f(f2bf((bf2f(x[r*D+i])-m)*rs));
}
__global__ void gemm_k(const float* A,const u16* W,float* C,int M,int N,int K){
  int n=blockIdx.x*64+threadIdx.x; int m=blockIdx.y*4+threadIdx.y;
  if(n>=N) return;
  float acc=0; for(int k=0;k<K;k++) acc+=A[m*K+k]*bf2f(W[k*N+n]);
  C[m*N+n]=acc;
}
__device__ float phi(float t){ return (t>0? t+1.f : __expf(t)) + 1e-6f; }
__global__ void kv_k(const float* qkv, float* kv){
  int h=blockIdx.x; int t=threadIdx.x; if(t>=DH*21) return; int d=t/21,e=t%21;
  float acc=0; for(int s=0;s<S;s++){ float pk=phi(qkv[s*3*D+D+h*DH+d]); float v=(e<20)? qkv[s*3*D+2*D+h*DH+e]:1.f; acc+=pk*v; }
  kv[h*DH*21+t]=acc;
}
__global__ void o_k(const float* qkv,const float* kv,float* o){
  int s=blockIdx.x; int c=threadIdx.x; if(c>=D) return; int h=c/DH,e=c%DH;
  float num=0,den=0; for(int d=0;d<DH;d++){ float pq=phi(qkv[s*3*D+h*DH+d]); num+=pq*kv[h*DH*21+d*21+e]; den+=pq*kv[h*DH*21+d*21+20]; }
  o[s*D+c]=num/(den+1e-6f);
}
__global__ void res_k(const u16* x,const float* a,u16* h){ int i=blockIdx.x*256+threadIdx.x; h[i]=f2bf(bf2f(x[i])+a[i]); }
__global__ void silu_k(float* f){ int i=blockIdx.x*256+threadIdx.x; float v=f[i]; f[i]=v/(1.f+__expf(-v)); }

extern "C" void kernel_launch(void* const* d_in, const int* in_sizes, int n_in, void* d_out, int out_size, void* d_ws, size_t ws_size, hipStream_t stream) {
  const u16* x=(const u16*)d_in[0]; const u16* wqkv=(const u16*)d_in[1]; const u16* wo=(const u16*)d_in[2];
  const u16* w1=(const u16*)d_in[4]; const u16* w2=(const u16*)d_in[5]; u16* out=(u16*)d_out;
  float* ws=(float*)d_ws;
  float* xn=ws; float* qkv=xn+S*D; float* kv=qkv+S*3*D; float* o=kv+16384; float* a=o+S*D; u16* h=(u16*)(a+S*D); float* f=(float*)(h+S*D); float* y=f+S*F;
  ln_k<<<S,256,0,stream>>>(x,xn);
  gemm_k<<<dim3(3*D/64,S/4),dim3(64,4),0,stream>>>(xn,wqkv,qkv,S,3*D,D);
  kv_k<<<H,448,0,stream>>>(qkv,kv);
  o_k<<<S,640,0,stream>>>(qkv,kv,o);
  gemm_k<<<dim3(D/64,S/4),dim3(64,4),0,stream>>>(o,wo,a,S,D,D);
  res_k<<<S*D/256,256,0,stream>>>(x,a,h);
  ln_k<<<S,256,0,stream>>>(h,xn);
  gemm_k<<<dim3(F/64,S/4),dim3(64,4),0,stream>>>(xn,w1,f,S,F,D);
  silu_k<<<S*F/256,256,0,stream>>>(f);
  gemm_k<<<dim3(D/64,S/4),dim3(64,4),0,stream>>>(f,w2,y,S,D,F);
  res_k<<<S*D/256,256,0,stream>>>(h,y,out);
}
```

```cpp
#include <hip/hip_runtime.h>
#include <hip/hip_bf16.h>
#include <stdint.h>

typedef unsigned short u16;
typedef unsigned long long u64;
typedef unsigned v4u __attribute__((ext_vector_type(4)));
typedef unsigned v2u __attribute__((ext_vector_type(2)));
typedef float f32x4 __attribute__((ext_vector_type(4)));
typedef float f32x2 __attribute__((ext_vector_type(2)));
typedef short bf16x8 __attribute__((ext_vector_type(8)));
typedef short s16x4 __attribute__((ext_vector_type(4)));
typedef __bf16 bfx2 __attribute__((ext_vector_type(2)));
#define GAS __attribute__((address_space(1)))
#define LAS __attribute__((address_space(3)))
#define DI __device__ __forceinline__
#define RLX_AGENT __ATOMIC_RELAXED, __HIP_MEMORY_SCOPE_AGENT
typedef __amdgpu_buffer_rsrc_t rsrc_t;

constexpr int S_ = 2048, D_ = 640, F_ = 1728;
constexpr int KB = 1024;
constexpr int WS_EPOCH = 0;
constexpr int WS_CHK   = 4096;
constexpr int WS_FLAG  = 16384;
constexpr int WS_ST    = 64 * KB;
constexpr int WS_CSQ   = 96 * KB;
constexpr int WS_CS1   = 128 * KB;
constexpr int WS_HST   = 192 * KB;
constexpr int SLAB_STRIDE = 6784;
constexpr int WS_SLAB  = 512 * KB;
constexpr int WS_WOT   = 1536 * KB;
constexpr int WS_W1T   = 2560 * KB;
constexpr int W2_WAVE  = 69120;
constexpr int WS_W2T   = 4864 * KB;
constexpr int WS_PQ    = 7168 * KB;
constexpr int WS_O     = 9728 * KB;
constexpr int WS_H     = 12288 * KB;
constexpr int WS_F     = 14848 * KB;
constexpr int WS_END   = 22016 * KB;
constexpr int WS_DBG   = 32768 * KB;

DI rsrc_t mkrsrc(const void* p, int bytes) { return __builtin_amdgcn_make_buffer_rsrc((void*)p, 0, bytes, 0x00020000); }
DI f32x4 mfma32(v4u a, v4u b, f32x4 c) { return __builtin_amdgcn_mfma_f32_16x16x32_bf16(__builtin_bit_cast(bf16x8, a), __builtin_bit_cast(bf16x8, b), c, 0, 0, 0); }
DI f32x4 mfma16(v2u a, v2u b, f32x4 c) { return mfma32(v4u{a.x, a.y, 0u, 0u}, v4u{b.x, b.y, 0u, 0u}, c); }
DI unsigned pk2(float lo, float hi) { bfx2 v; v[0] = (__bf16)lo; v[1] = (__bf16)hi; return __builtin_bit_cast(unsigned, v); }
DI float asf(unsigned u) { return __builtin_bit_cast(float, u); }
DI unsigned asu(float f) { return __builtin_bit_cast(unsigned, f); }
DI float bflo(unsigned u) { return __builtin_bit_cast(float, u << 16); }
DI float bfhi(unsigned u) { return __builtin_bit_cast(float, u & 0xffff0000u); }
DI v2u trread(LAS char* p) { return __builtin_bit_cast(v2u, __builtin_amdgcn_ds_read_tr16_b64_v4i16((LAS s16x4*)p)); }
DI float phi_f(float t) { return (t > 0.f ? t + 1.f : __expf(t)) + 1e-6f; }
DI float dot1(unsigned u, float c) { return __builtin_amdgcn_fdot2_f32_bf16(__builtin_bit_cast(bfx2, u), __builtin_bit_cast(bfx2, 0x3f803f80u), c, false); }
#define DMA16(rs, ldsp, voff, soff) __builtin_amdgcn_raw_ptr_buffer_load_lds(rs, (LAS void*)(ldsp), 16, voff, soff, 0, 0)
#define WAITV(n) asm volatile("s_waitcnt vmcnt(" #n ")" ::: "memory")
#define BAR() asm volatile("s_waitcnt lgkmcnt(0)\n\ts_barrier" ::: "memory")
#define LD128(rs, off, aux) __builtin_amdgcn_raw_buffer_load_b128(rs, off, 0, aux)
#define LD64(rs, off, aux) __builtin_amdgcn_raw_buffer_load_b64(rs, off, 0, aux)
#define ST128(v, rs, off, aux) __builtin_amdgcn_raw_buffer_store_b128(v, rs, off, 0, aux)
#define ST64(v, rs, off, aux) __builtin_amdgcn_raw_buffer_store_b64(v, rs, off, 0, aux)
constexpr int SC1 = 16;

#define STAMP(k, i) do {} while (0)

DI u64 next_epoch(u64 e) { return e * 6364136223846793005ull + 1442695040888963407ull; }

constexpr int K1_BBLK = 5152;
constexpr int K1_SLOT = 16384 + 4 * K1_BBLK;
constexpr int K1_STAT = 4 * K1_SLOT;
constexpr int K1_LDS  = K1_STAT + 2048;

template <bool ISQ>
DI void k1_gemm(LAS char* L, const u16* x, const u16* wqkv, char* ws, int r, int c, int rot, int w, int lane) {
  const rsrc_t rws = mkrsrc(ws, WS_END);
  if (w >= 8) {
    const int lw = w - 8;
    const rsrc_t rx = mkrsrc(x, S_ * D_ * 2), rw = mkrsrc(wqkv, D_ * 3 * D_ * 2);
    const u64 ep = __hip_atomic_load((GAS u64*)(ws + WS_EPOCH), RLX_AGENT);
    int va[4], vb[5];
#pragma unroll
    for (int n = 0; n < 4; ++n) { const int row = 32 * lw + 8 * n + (lane >> 3); va[n] = (128 * r + row) * 1280 + 16 * ((lane & 7) ^ ((row >> 1) & 7)); }
#pragma unroll
    for (int n = 0; n < 5; ++n) {
      const int pos = 64 * n + lane, row16 = pos / 20, j = pos % 20;
      const int kg = (lw >> 1) * 4 + (lw & 1) + 2 * (row16 >> 3), k = 8 * kg + (row16 & 7);
      const int cb = ISQ ? 320 * c + 16 * j : ((j % 10 < 5 ? 1280 : 2560) + 160 * c + 80 * (j / 10) + 16 * (j % 5));
      vb[n] = k * 3840 + cb;
    }
    auto issue = [&](int cc) {
      int kc = cc + rot; if (kc >= 10) kc -= 10;
      LAS char* sl = L + (cc & 3) * K1_SLOT;
#pragma unroll
      for (int n = 0; n < 4; ++n) DMA16(rx, sl + (4 * lw + n) * 1024, va[n], kc * 128);
#pragma unroll
      for (int n = 0; n < 5; ++n) DMA16(rw, sl + 16384 + lw * K1_BBLK + n * 1024, vb[n], kc * (64 * 3840));
    };
    issue(0); issue(1); issue(2); issue(3);
    WAITV(27); BAR();
#pragma unroll
    for (int cc = 1; cc <= 6; ++cc) { WAITV(18); BAR(); issue(cc + 3); }
    const unsigned tag = (unsigned)next_epoch(ep);
    const int it = lw * 64 + lane;
    int off0, off1;
    {
      const int ci = it < 160 ? it : 159;
      const int gc = ISQ ? 160 * c + ci : ((ci % 80 < 40 ? 640 : 1280) + 80 * c + 40 * (ci / 80) + (ci % 40));
      off0 = WS_ST + (128 * r + (it & 127)) * 16; off1 = WS_CSQ + gc * 16;
    }
    v4u g0, g1;
    asm volatile("buffer_load_dwordx4 %0, %2, %4, 0 offen sc1\n\tbuffer_load_dwordx4 %1, %3, %4, 0 offen sc1" : "=&v"(g0), "=&v"(g1) : "v"(off0), "v"(off1), "s"(rws) : "memory");
    WAITV(20); BAR();
    WAITV(11); BAR();
    asm volatile("s_waitcnt vmcnt(0)" : "+v"(g0), "+v"(g1) :: "memory"); BAR();
    for (int sp = 0; sp < 2000000; ++sp) {
      const bool ok = g0.y == tag && g0.w == tag && g1.y == tag && g1.w == tag;
      if (__all(ok)) break;
      __builtin_amdgcn_s_sleep(2);
      g0 = LD128(rws, off0, SC1); g1 = LD128(rws, off1, SC1);
    }
    LAS float* st = (LAS float*)(L + K1_STAT);
    if (it < 128) { st[it] = asf(g0.x); st[128 + it] = asf(g0.z); }
    if (it < 160) st[256 + it] = asf(g1.x) + asf(g1.z);
    BAR();
    if (!ISQ) { BAR(); }
    return;
  }
  const int wm = w >> 1, wn = w & 1, g = lane >> 4, i16 = lane & 15, q = i16 >> 2, p = lane & 3;
  u64 ep = 0;
  if (w == 0) ep = __hip_atomic_load((GAS u64*)(ws + WS_EPOCH), RLX_AGENT);
  const int aoff0 = (32 * wm + i16) * 128 + 16 * ((0 + g) ^ (i16 >> 1));
  const int aoff1 = (32 * wm + i16) * 128 + 16 * ((4 + g) ^ (i16 >> 1));
  const int boff = 16384 + (g & 1) * K1_BBLK + ((g >> 1) * 8 + q) * 320 + (20 * wn + (ISQ ? 5 * p : p)) * 8;
  f32x4 acc[2][5];
#pragma unroll
  for (int a = 0; a < 2; ++a)
#pragma unroll
    for (int t = 0; t < 5; ++t) acc[a][t] = f32x4{0.f, 0.f, 0.f, 0.f};
  v4u fa[2][2], fb[2][5];
  auto rd = [&](int cc, int ks) {
    LAS char* sl = L + (cc & 3) * K1_SLOT;
#pragma unroll
    for (int a = 0; a < 2; ++a) fa[ks][a] = *(LAS v4u*)(sl + (ks ? aoff1 : aoff0) + a * 2048);
#pragma unroll
    for (int t = 0; t < 5; ++t) {
      const v2u lo = trread(sl + boff + ks * (2 * K1_BBLK) + t * (ISQ ? 8 : 32));
      const v2u hi = trread(sl + boff + ks * (2 * K1_BBLK) + 1280 + t * (ISQ ? 8 : 32));
      fb[ks][t] = v4u{lo.x, lo.y, hi.x, hi.y};
    }
  };
  auto mm = [&](int ks) {
#pragma unroll
    for (int t = 0; t < 5; ++t)
#pragma unroll
      for (int a = 0; a < 2; ++a) acc[a][t] = ISQ ? mfma32(fb[ks][t], fa[ks][a], acc[a][t]) : mfma32(fa[ks][a], fb[ks][t], acc[a][t]);
  };
  if (w == 0) asm volatile("s_waitcnt vmcnt(0)" : "+v"(ep) :: "memory");
  BAR();
  if (w == 0 && lane == 0) __hip_atomic_store((GAS u64*)(ws + WS_CHK) + blockIdx.x, next_epoch(ep), RLX_AGENT);
  rd(0, 0);
#pragma unroll
  for (int cc = 0; cc < 10; ++cc) {
    rd(cc, 1);
    mm(0);
    if (cc < 9) { BAR(); rd(cc + 1, 0); }
    mm(1);
  }
  BAR();
  LAS float* st = (LAS float*)(L + K1_STAT);
  if (ISQ) {
#pragma unroll
    for (int a = 0; a < 2; ++a) {
      const float mean = st[32 * wm + 16 * a + i16], rstd = st[128 + 32 * wm + 16 * a + i16];
      unsigned o[10];
#pragma unroll
      for (int t = 0; t < 5; ++t) {
        const f32x4 cs = *(LAS f32x4*)(st + 256 + 80 * wn + 20 * g + 4 * t);
        float v[4];
#pragma unroll
        for (int e = 0; e < 4; ++e) v[e] = phi_f((acc[a][t][e] - mean * cs[e]) * rstd);
        o[2 * t] = pk2(v[0], v[1]); o[2 * t + 1] = pk2(v[2], v[3]);
      }
      const int base = WS_PQ + ((8 * r + 2 * wm + a) * 8 + 2 * c + wn) * 2560;
      ST128((v4u{o[0], o[1], o[2], o[3]}), rws, base + lane * 16, 0);
      ST128((v4u{o[4], o[5], o[6], o[7]}), rws, base + 1024 + lane * 16, 0);
      ST64((v2u{o[8], o[9]}), rws, base + 2048 + lane * 8, 0);
    }
  } else {
    v4u fr[5];
    const f32x4 mean0 = *(LAS f32x4*)(st + 32 * wm + 4 * g), mean1 = *(LAS f32x4*)(st + 32 * wm + 16 + 4 * g);
    const f32x4 rstd0 = *(LAS f32x4*)(st + 128 + 32 * wm + 4 * g), rstd1 = *(LAS f32x4*)(st + 128 + 32 * wm + 16 + 4 * g);
#pragma unroll
    for (int t = 0; t < 5; ++t) {
      const float cs = st[256 + 80 * wn + 16 * t + i16];
      float v[8];
#pragma unroll
      for (int e = 0; e < 4; ++e) { v[e] = (acc[0][t][e] - mean0[e] * cs) * rstd0[e]; v[4 + e] = (acc[1][t][e] - mean1[e] * cs) * rstd1[e]; }
      if (t < 2) {
#pragma unroll
        for (int e = 0; e < 8; ++e) v[e] = phi_f(v[e]);
      } else if (t == 2) {
#pragma unroll
        for (int e = 0; e < 8; ++e) { const float ph = phi_f(v[e]); v[e] = i16 < 8 ? ph : v[e]; }
      }
      fr[t] = v4u{pk2(v[0], v[1]), pk2(v[2], v[3]), pk2(v[4], v[5]), pk2(v[6], v[7])};
    }
    v4u f2b = fr[2];
    if (i16 < 8) f2b = v4u{0x3f803f80u, 0x3f803f80u, 0x3f803f80u, 0x3f803f80u};
    const f32x4 z4 = f32x4{0.f, 0.f, 0.f, 0.f};
    LAS f32x4* red = (LAS f32x4*)L + ((wn * 4 + wm) * 8) * 64 + lane;
    red[0 * 64] = mfma32(fr[0], f2b, z4);
    red[1 * 64] = mfma32(fr[0], fr[3], z4);
    red[2 * 64] = mfma32(fr[1], f2b, z4);
    red[3 * 64] = mfma32(fr[1], fr[3], z4);
    red[4 * 64] = mfma32(fr[1], fr[4], z4);
    red[5 * 64] = mfma32(fr[2], f2b, z4);
    red[6 * 64] = mfma32(fr[2], fr[3], z4);
    red[7 * 64] = mfma32(fr[2], fr[4], z4);
    BAR();
    const int pr = w;
    const int ta = pr < 2 ? 0 : (pr < 5 ? 1 : 2);
    const int tb = pr < 2 ? pr : (pr < 5 ? pr - 2 : pr - 5);
    const int kidx = 16 * ta + 4 * g, hd = kidx >= 20 ? 1 : 0, d0 = kidx - 20 * hd;
    const int vidx = 16 * tb - 8 + i16;
    const int he = vidx >= 20 ? 1 : 0;
    const bool isz = vidx < 0;
    const int e = isz ? 20 : vidx - 20 * he;
    const bool valid = kidx < 40 && (isz ? i16 == 0 : he == hd);
#pragma unroll
    for (int n = 0; n < 2; ++n) {
      LAS f32x4* src = (LAS f32x4*)L + (n * 32 + pr) * 64 + lane;
      const f32x4 s = (src[0] + src[8 * 64]) + (src[16 * 64] + src[24 * 64]);
      if (valid) ST128(__builtin_bit_cast(v4u, s), rws, WS_SLAB + (c * 16 + r) * SLAB_STRIDE + (((2 * n + hd) * 21 + e) * 20 + d0) * 4, 0);
    }
  }
}

DI void k1_rt(LAS char* L, const u16* x, const u16* wqkv, const u16* wo, const u16* w1, const u16* w2, char* ws, int u, int w, int lane) {
  const rsrc_t rws = mkrsrc(ws, WS_END);
  const int g = lane >> 4, i16 = lane & 15, q = i16 >> 2, p = lane & 3;
  LAS float* csl = (LAS float*)(L + 81920);
  u64 ep = 0;
  if (w < 4) {
    const int kind = u < 27 ? 0 : (u < 37 ? 1 : 2);
    const u16* src = kind == 0 ? w1 : (kind == 1 ? wo : w2);
    const int rs_ = kind == 0 ? F_ * 2 : D_ * 2;
    const rsrc_t rsrc = mkrsrc(src, kind == 1 ? D_ * D_ * 2 : D_ * F_ * 2);
    LAS char* buf = L + w * 20480;
    int strip[5], step[5];
#pragma unroll
    for (int n = 0; n < 5; ++n) {
      if (kind == 0) { strip[n] = u; step[n] = 5 * w + n; }
      else if (kind == 1) { strip[n] = u - 27; step[n] = 5 * w + n; }
      else { const int mu = 20 * (u - 37) + 5 * w + n; step[n] = mu / 10; strip[n] = mu % 10; }
#pragma unroll
      for (int i = 0; i < 4; ++i) {
        const int rho = 8 * i + (lane >> 3), gg = i, hh = (rho >> 2) & 1, qq = rho & 3;
        int row;
        if (kind == 1) {
          const int b = w, s = n;
          row = s < 4 ? 20 * (8 * b + 4 * (s >> 1) + gg) + 8 * (s & 1) + 4 * hh + qq : 20 * (8 * b + 4 * hh + gg) + 16 + qq;
        } else {
          int Ta, Tb;
          if (kind == 0) { const int b = w, s = n; if (s < 4) { Ta = 5 * (2 * b + (s >> 1)) + 2 * (s & 1); Tb = Ta + 1; } else { Ta = 10 * b + 4; Tb = 10 * b + 9; } }
          else {
            const int sg = step[n];
            if (sg < 52) { const int wv = sg / 13, s = sg % 13; if (s < 12) { Ta = 27 * wv + 7 * (s / 3) + 2 * (s % 3); Tb = Ta + 1; } else { Ta = 27 * wv + 6; Tb = 27 * wv + 13; } }
            else { Ta = 27 * (2 * (sg - 52)) + 20; Tb = Ta + 27; }
          }
          row = 16 * (hh ? Tb : Ta) + 4 * gg + qq;
        }
        DMA16(rsrc, buf + n * 4096 + i * 1024, row * rs_ + strip[n] * 128 + (lane & 7) * 16, 0);
      }
    }
    BAR();
    WAITV(0);
    float cs[4] = {0.f, 0.f, 0.f, 0.f};
#pragma unroll
    for (int n = 0; n < 5; ++n) {
#pragma unroll
      for (int j = 0; j < 4; ++j) {
        const v2u lo = trread(buf + n * 4096 + (8 * g + q) * 128 + 32 * j + 8 * p);
        const v2u hi = trread(buf + n * 4096 + (8 * g + 4 + q) * 128 + 32 * j + 8 * p);
        const int nt = 4 * strip[n] + j;
        if (kind == 0) {
          const int nq = nt / 27, nr = nt % 27, mi = nr / 7 > 3 ? 3 : nr / 7, jj = nr - 7 * mi;
          ST128((v4u{lo.x, lo.y, hi.x, hi.y}), rws, WS_W1T + (((4 * nq + mi) * 20 + step[n]) * 7 + jj) * 1024 + lane * 16, 0);
          cs[j] = dot1(lo.x, cs[j]); cs[j] = dot1(lo.y, cs[j]); cs[j] = dot1(hi.x, cs[j]); cs[j] = dot1(hi.y, cs[j]);
        } else if (kind == 1) {
          ST128((v4u{lo.x, lo.y, hi.x, hi.y}), rws, WS_WOT + (((nt / 5) * 20 + step[n]) * 5 + nt % 5) * 1024 + lane * 16, 0);
        } else {
          const int sg = step[n], ct = nt / 5, jj = nt % 5;
          if (sg < 52) ST128((v4u{lo.x, lo.y, hi.x, hi.y}), rws, WS_W2T + (ct * 4 + sg / 13) * W2_WAVE + ((sg % 13) * 5 + jj) * 1024 + lane * 16, 0);
          else {
            const int o = WS_W2T + (ct * 4 + 2 * (sg - 52)) * W2_WAVE + 66560 + jj * 512 + lane * 8;
            ST64(lo, rws, o, 0); ST64(hi, rws, o + W2_WAVE, 0);
          }
        }
      }
    }
    if (kind == 0) {
#pragma unroll
      for (int j = 0; j < 4; ++j) { float s = cs[j]; s += __shfl_xor(s, 16); s += __shfl_xor(s, 32); if (g == 0) csl[w * 64 + 16 * j + i16] = s; }
      BAR();
      if (w == 0) {
        const float s = (csl[lane] + csl[64 + lane]) + (csl[128 + lane] + csl[192 + lane]);
        ((GAS float*)(ws + WS_CS1))[64 * u + lane] = s;
      }
    }
    return;
  } else if (w < 8) {
    const rsrc_t rx = mkrsrc(x, S_ * D_ * 2);
    ep = __hip_atomic_load((GAS u64*)(ws + WS_EPOCH), RLX_AGENT);
    const int row = 32 * u + 8 * (w - 4) + (lane >> 3);
    v4u v[10];
#pragma unroll
    for (int i = 0; i < 10; ++i) v[i] = LD128(rx, row * 1280 + i * 128 + (lane & 7) * 16, 0);
    float s = 0.f;
#pragma unroll
    for (int i = 0; i < 10; ++i) { s = dot1(v[i].x, s); s = dot1(v[i].y, s); s = dot1(v[i].z, s); s = dot1(v[i].w, s); }
    s += __shfl_xor(s, 1); s += __shfl_xor(s, 2); s += __shfl_xor(s, 4);
    const float mean = s * (1.f / D_);
    float m2 = 0.f;
#pragma unroll
    for (int i = 0; i < 10; ++i)
#pragma unroll
      for (int e = 0; e < 4; ++e) { const float a = bflo(v[i][e]) - mean, b = bfhi(v[i][e]) - mean; m2 = fmaf(a, a, m2); m2 = fmaf(b, b, m2); }
    m2 += __shfl_xor(m2, 1); m2 += __shfl_xor(m2, 2); m2 += __shfl_xor(m2, 4);
    const float rstd = 1.f / sqrtf(m2 * (1.f / D_) + 1e-5f);
    const unsigned tag = (unsigned)next_epoch(ep);
    if ((lane & 7) == 0) ST128((v4u{__builtin_bit_cast(unsigned, mean), tag, __builtin_bit_cast(unsigned, rstd), tag}), rws, WS_ST + row * 16, SC1);
  } else {
    ep = __hip_atomic_load((GAS u64*)(ws + WS_EPOCH), RLX_AGENT);
    if (u < 60) {
      const rsrc_t rw = mkrsrc(wqkv, D_ * 3 * D_ * 2);
      const int row0 = 320 * (u & 1) + 80 * (w - 8) + (lane >> 3);
      v4u v[10];
#pragma unroll
      for (int i = 0; i < 10; ++i) v[i] = LD128(rw, (row0 + 8 * i) * 3840 + (u >> 1) * 128 + (lane & 7) * 16, 0);
      float s[8];
#pragma unroll
      for (int e = 0; e < 8; ++e) s[e] = 0.f;
#pragma unroll
      for (int i = 0; i < 10; ++i)
#pragma unroll
        for (int e = 0; e < 4; ++e) { s[2 * e] += bflo(v[i][e]); s[2 * e + 1] += bfhi(v[i][e]); }
#pragma unroll
      for (int e = 0; e < 8; ++e) { s[e] += __shfl_xor(s[e], 8); s[e] += __shfl_xor(s[e], 16); s[e] += __shfl_xor(s[e], 32); }
      if (lane < 8) {
#pragma unroll
        for (int e = 0; e < 8; ++e) csl[256 + (w - 8) * 64 + 8 * lane + e] = s[e];
      }
    }
    asm volatile("" : "+v"(ep));
  }
  BAR();
  if (w == 8) {
    const u64 ne = next_epoch(ep);
    if (u < 60) {
      const float s = (csl[256 + lane] + csl[320 + lane]) + (csl[384 + lane] + csl[448 + lane]);
      ST64((v2u{__builtin_bit_cast(unsigned, s), (unsigned)ne}), rws, WS_CSQ + (64 * (u >> 1) + lane) * 16 + (u & 1) * 8, SC1);
    }
    if (lane == 0) __hip_atomic_store((GAS u64*)(ws + WS_CHK) + blockIdx.x, ne, RLX_AGENT);
    if (u == 63) {
      bool all = false;
      for (int sp = 0; sp < 2000000 && !all; ++sp) {
        bool ok = true;
#pragma unroll
        for (int i = 0; i < 4; ++i) ok &= __hip_atomic_load((GAS u64*)(ws + WS_CHK) + i * 64 + lane, RLX_AGENT) == ne;
        all = __all(ok);
        if (!all) __builtin_amdgcn_s_sleep(8);
      }
      if (lane == 0) __hip_atomic_store((GAS u64*)(ws + WS_EPOCH), ne, RLX_AGENT);
    }
  }
}

__global__ __launch_bounds__(768) void k1(const u16* x, const u16* wqkv, const u16* wo, const u16* w1, const u16* w2, char* ws) {
  extern __shared__ __attribute__((aligned(16))) char smem[];
  LAS char* L = (LAS char*)smem;
  const int lane = threadIdx.x & 63, w = __builtin_amdgcn_readfirstlane(threadIdx.x >> 6);
  const int b = blockIdx.x, xcd = b & 7, idx = b >> 3;
  STAMP(0, 0);
  const int r = 4 * (xcd >> 1) + (idx & 3);
  if (idx < 16) k1_gemm<false>(L, x, wqkv, ws, r, 4 * (xcd & 1) + (idx >> 2), (idx * 3 + (idx >> 2) * 2) % 10, w, lane);
  else if (idx < 24) k1_gemm<true>(L, x, wqkv, ws, r, 2 * (xcd & 1) + ((idx >> 2) & 1), (idx * 3 + (idx >> 2) * 2) % 10, w, lane);
  else k1_rt(L, x, wqkv, wo, w1, w2, ws, xcd * 8 + (idx - 24), w, lane);
  STAMP(0, 1);
}

__global__ __launch_bounds__(256) void k_o(char* ws) {
  __shared__ __attribute__((aligned(16))) char smem[5 * 1152];
  LAS char* L = (LAS char*)smem;
  const int tid = threadIdx.x, lane = tid & 63, w = __builtin_amdgcn_readfirstlane(tid >> 6);
  const int b = blockIdx.x, xcd = b & 7, idx = b >> 3, G = 2 * xcd + (idx >> 4), m = idx & 15, hf = m >> 3, c = m & 7;
  const int g = lane >> 4, i16 = lane & 15;
  const rsrc_t rws = mkrsrc(ws, WS_END);
  STAMP(1, 0);
  const int rt = 8 * G + 4 * hf + w;
  const int pbase = (rt * 8 + c) * 2560;
  f32x4 s0 = f32x4{0.f, 0.f, 0.f, 0.f}, s1 = s0;
  {
    v4u a[16], bq[16];
    const int o0 = WS_SLAB + c * 16 * SLAB_STRIDE + tid * 16;
    const int o1 = tid < 164 ? o0 + 4096 : WS_END;
#pragma unroll
    for (int r = 0; r < 16; ++r) { a[r] = LD128(rws, o0 + r * SLAB_STRIDE, 0); bq[r] = LD128(rws, o1 + r * SLAB_STRIDE, 0); }
#pragma unroll
    for (int r = 0; r < 16; ++r) { s0 += __builtin_bit_cast(f32x4, a[r]); s1 += __builtin_bit_cast(f32x4, bq[r]); }
  }
  const v4u p0 = LD128(rws, WS_PQ + pbase + lane * 16, 0), p1 = LD128(rws, WS_PQ + pbase + 1024 + lane * 16, 0);
  const v2u p2 = LD64(rws, WS_PQ + pbase + 2048 + lane * 8, 0);
  {
    const int i0 = tid, r0 = i0 / 5, h0 = r0 / 21;
    *(LAS v2u*)(L + h0 * 1152 + (r0 - 21 * h0) * 48 + (i0 % 5) * 8) = v2u{pk2(s0[0], s0[1]), pk2(s0[2], s0[3])};
    const int i1 = tid + 256, r1 = i1 / 5, h1 = r1 / 21;
    if (tid < 164) *(LAS v2u*)(L + h1 * 1152 + (r1 - 21 * h1) * 48 + (i1 % 5) * 8) = v2u{pk2(s1[0], s1[1]), pk2(s1[2], s1[3])};
    if (tid < 72) *(LAS v4u*)(L + 4 * 1152 + tid * 16) = v4u{0u, 0u, 0u, 0u};
  }
  __syncthreads();
  const bool act = (i16 >> 2) == g;
  LAS char* kb = L + (act ? g : 4) * 1152;
  f32x4 d[6];
#pragma unroll
  for (int t = 0; t < 6; ++t) {
    LAS char* rp = kb + (t < 5 ? (4 * t + (i16 & 3)) : 20) * 48;
    const v4u a0 = *(LAS v4u*)rp, a1 = *(LAS v4u*)(rp + 16);
    const v2u a2 = *(LAS v2u*)(rp + 32);
    f32x4 acc = f32x4{0.f, 0.f, 0.f, 0.f};
    acc = mfma32(a0, p0, acc); acc = mfma32(a1, p1, acc); acc = mfma16(a2, p2, acc);
    d[t] = acc;
  }
  const float inv = 1.f / (d[5][0] + 1e-6f);
  unsigned o[10];
#pragma unroll
  for (int t = 0; t < 5; ++t) { o[2 * t] = pk2(d[t][0] * inv, d[t][1] * inv); o[2 * t + 1] = pk2(d[t][2] * inv, d[t][3] * inv); }
  ST128((v4u{o[0], o[1], o[2], o[3]}), rws, WS_O + pbase + lane * 16, 0);
  ST128((v4u{o[4], o[5], o[6], o[7]}), rws, WS_O + pbase + 1024 + lane * 16, 0);
  ST64((v2u{o[8], o[9]}), rws, WS_O + pbase + 2048 + lane * 8, 0);
  STAMP(1, 1);
}

__global__ __launch_bounds__(256) void k_wo(const u16* x, char* ws) {
  extern __shared__ __attribute__((aligned(16))) char smem[];
  LAS char* L = (LAS char*)smem;
  const int tid = threadIdx.x, lane = tid & 63, w = __builtin_amdgcn_readfirstlane(tid >> 6);
  const int b = blockIdx.x, xcd = b & 7, idx = b >> 3, G = 2 * xcd + (idx >> 4), m = idx & 15, hf = m >> 3, ct = m & 7;
  const int g = lane >> 4, i16 = lane & 15;
  const rsrc_t rws = mkrsrc(ws, WS_END), rx = mkrsrc(x, S_ * D_ * 2);
  STAMP(2, 0);
  const int rt0 = 8 * G + 4 * hf;
  v4u A[4][5], B[5][5];
  v4u xr[3];
  {
#pragma unroll
    for (int i = 0; i < 3; ++i) { const int pc = 64 * i + lane; xr[i] = LD128(rx, pc < 160 ? (16 * (rt0 + w) + pc / 10) * 1280 + 160 * ct + (pc % 10) * 16 : 0x7fffff00, 0); }
  }
#pragma unroll
  for (int s = 0; s < 5; ++s) {
#pragma unroll
    for (int a = 0; a < 4; ++a) {
      const int base = WS_O + ((rt0 + a) * 8 + 2 * w) * 2560;
      if (s < 4) A[a][s] = LD128(rws, base + (s >> 1) * 2560 + (s & 1) * 1024 + lane * 16, 0);
      else { const v2u lo = LD64(rws, base + 2048 + lane * 8, 0), hi = LD64(rws, base + 2560 + 2048 + lane * 8, 0); A[a][4] = v4u{lo.x, lo.y, hi.x, hi.y}; }
    }
#pragma unroll
    for (int j = 0; j < 5; ++j) B[s][j] = LD128(rws, WS_WOT + (((ct * 4 + w) * 5 + s) * 5 + j) * 1024 + lane * 16, 0);
  }
  __builtin_amdgcn_sched_barrier(0);
#pragma unroll
  for (int i = 0; i < 3; ++i) { const int pc = 64 * i + lane; if (pc < 160) *(LAS v4u*)(L + 61440 + w * 2560 + pc * 16) = xr[i]; }
  f32x4 acc[4][5];
#pragma unroll
  for (int a = 0; a < 4; ++a)
#pragma unroll
    for (int j = 0; j < 5; ++j) acc[a][j] = f32x4{0.f, 0.f, 0.f, 0.f};
#pragma unroll
  for (int s = 0; s < 5; ++s)
#pragma unroll
    for (int j = 0; j < 5; ++j)
#pragma unroll
      for (int a = 0; a < 4; ++a) acc[a][j] = mfma32(B[s][j], A[a][s], acc[a][j]);
  LAS f32x4* red = (LAS f32x4*)L;
  f32x4 own[5];
#pragma unroll
  for (int a = 0; a < 4; ++a) {
    if (a == w) {
#pragma unroll
      for (int j = 0; j < 5; ++j) own[j] = acc[a][j];
    } else {
      const int k = (w - a - 1) & 3;
#pragma unroll
      for (int j = 0; j < 5; ++j) red[((a * 3 + k) * 5 + j) * 64 + lane] = acc[a][j];
    }
  }
  __syncthreads();
  unsigned hp[10];
  float hv[20];
#pragma unroll
  for (int j = 0; j < 5; ++j) {
    const f32x4 s = (own[j] + red[((w * 3 + 0) * 5 + j) * 64 + lane]) + (red[((w * 3 + 1) * 5 + j) * 64 + lane] + red[((w * 3 + 2) * 5 + j) * 64 + lane]);
    const v2u xv = *(LAS v2u*)(L + 61440 + w * 2560 + i16 * 160 + 32 * j + 8 * g);
    hp[2 * j] = pk2(bflo(xv.x) + s[0], bfhi(xv.x) + s[1]); hp[2 * j + 1] = pk2(bflo(xv.y) + s[2], bfhi(xv.y) + s[3]);
    hv[4 * j] = bflo(hp[2 * j]); hv[4 * j + 1] = bfhi(hp[2 * j]); hv[4 * j + 2] = bflo(hp[2 * j + 1]); hv[4 * j + 3] = bfhi(hp[2 * j + 1]);
  }
  const int hb = WS_H + ((rt0 + w) * 8 + ct) * 2560;
  ST128((v4u{hp[0], hp[1], hp[2], hp[3]}), rws, hb + lane * 16, 0);
  ST128((v4u{hp[4], hp[5], hp[6], hp[7]}), rws, hb + 1024 + lane * 16, 0);
  ST64((v2u{hp[8], hp[9]}), rws, hb + 2048 + lane * 8, 0);
  float s = 0.f;
#pragma unroll
  for (int i = 0; i < 20; ++i) s += hv[i];
  s += __shfl_xor(s, 16); s += __shfl_xor(s, 32);
  const float mc = s * (1.f / 80.f);
  float m2 = 0.f;
#pragma unroll
  for (int i = 0; i < 20; ++i) { const float dd = hv[i] - mc; m2 = fmaf(dd, dd, m2); }
  m2 += __shfl_xor(m2, 16); m2 += __shfl_xor(m2, 32);
  if (g == 0) ST64((v2u{__builtin_bit_cast(unsigned, mc), __builtin_bit_cast(unsigned, m2)}), rws, WS_HST + ((16 * (rt0 + w) + i16) * 8 + ct) * 8, 0);
  STAMP(2, 1);
}

__global__ __launch_bounds__(256) void k_f1(char* ws) {
  extern __shared__ __attribute__((aligned(16))) char smem[];
  LAS char* L = (LAS char*)smem;
  const int tid = threadIdx.x, lane = tid & 63, w = __builtin_amdgcn_readfirstlane(tid >> 6);
  const int b = blockIdx.x, xcd = b & 7, idx = b >> 3, G = 2 * xcd + (idx >> 4), m = idx & 15;
  const int g = lane >> 4, i16 = lane & 15;
  const rsrc_t rws = mkrsrc(ws, WS_END);
  STAMP(3, 0);
  const int rt0 = 8 * G + 2 * w;
  const int nt0 = 27 * (m >> 2) + 7 * (m & 3);
  v4u A[2][20];
  v4u hs[2]; f32x4 cs[7];
  const int l16 = lane * 16, l8 = lane * 8;
#pragma unroll
  for (int a = 0; a < 2; ++a) { const int o = WS_HST + (16 * (rt0 + a) + i16) * 64 + g * 16; asm volatile("buffer_load_dwordx4 %0, %1, %2, 0 offen" : "=v"(hs[a]) : "v"(o), "s"(rws) : "memory"); }
#pragma unroll
  for (int j = 0; j < 7; ++j) { const int o = WS_CS1 + (16 * (nt0 + j) + 4 * g) * 4; asm volatile("buffer_load_dwordx4 %0, %1, %2, 0 offen" : "=v"(cs[j]) : "v"(o), "s"(rws) : "memory"); }
  v2u Ah[2][4][2];
#pragma unroll
  for (int bb = 0; bb < 4; ++bb) {
#pragma unroll
    for (int i = 0; i < 9; ++i) {
      const int f = 4 * i + w;
      const int fi = 35 * bb + f;
      const int dst = __builtin_amdgcn_readfirstlane((int)(size_t)L + (f < 35 ? fi : 140) * 1024);
      const int so = __builtin_amdgcn_readfirstlane(WS_W1T + (m * 140 + fi) * 1024);
      asm volatile("s_mov_b32 m0, %0\n\ts_nop 0\n\tbuffer_load_dwordx4 %1, %2, %3 offen lds" :: "s"(dst), "v"(l16), "s"(rws), "s"(so) : "memory");
    }
#pragma unroll
    for (int a = 0; a < 2; ++a) {
      const int base = __builtin_amdgcn_readfirstlane(WS_H + ((rt0 + a) * 8 + 2 * bb) * 2560);
      asm volatile("buffer_load_dwordx4 %0, %1, %2, %3 offen" : "=v"(A[a][5 * bb + 0]) : "v"(l16), "s"(rws), "s"(base) : "memory");
      asm volatile("buffer_load_dwordx4 %0, %1, %2, %3 offen offset:1024" : "=v"(A[a][5 * bb + 1]) : "v"(l16), "s"(rws), "s"(base) : "memory");
      asm volatile("buffer_load_dwordx4 %0, %1, %2, %3 offen offset:2560" : "=v"(A[a][5 * bb + 2]) : "v"(l16), "s"(rws), "s"(base) : "memory");
      asm volatile("buffer_load_dwordx4 %0, %1, %2, %3 offen offset:3584" : "=v"(A[a][5 * bb + 3]) : "v"(l16), "s"(rws), "s"(base) : "memory");
      asm volatile("buffer_load_dwordx2 %0, %1, %2, %3 offen offset:2048" : "=v"(Ah[a][bb][0]) : "v"(l8), "s"(rws), "s"(base) : "memory");
      const int base2 = base + 2560;
      asm volatile("buffer_load_dwordx2 %0, %1, %2, %3 offen offset:2048" : "=v"(Ah[a][bb][1]) : "v"(l8), "s"(rws), "s"(base2) : "memory");
    }
  }
  f32x4 acc[2][7];
  float rstd[2];
#define PIN_CHUNK(bb) asm volatile("" : "+v"(A[0][5*(bb)]), "+v"(A[0][5*(bb)+1]), "+v"(A[0][5*(bb)+2]), "+v"(A[0][5*(bb)+3]), "+v"(A[1][5*(bb)]), "+v"(A[1][5*(bb)+1]), "+v"(A[1][5*(bb)+2]), "+v"(A[1][5*(bb)+3]), \
                                        "+v"(Ah[0][bb][0]), "+v"(Ah[0][bb][1]), "+v"(Ah[1][bb][0]), "+v"(Ah[1][bb][1]))
  WAITV(63);
  asm volatile("" : "+v"(hs[0]), "+v"(hs[1]), "+v"(cs[0]), "+v"(cs[1]), "+v"(cs[2]), "+v"(cs[3]), "+v"(cs[4]), "+v"(cs[5]), "+v"(cs[6]));
#pragma unroll
  for (int a = 0; a < 2; ++a) {
    const float m0 = asf(hs[a].x), q0 = asf(hs[a].y), m1 = asf(hs[a].z), q1 = asf(hs[a].w);
    float s = m0 + m1; s += __shfl_xor(s, 16); s += __shfl_xor(s, 32);
    const float mean = s * 0.125f;
    float v = q0 + q1 + 80.f * ((m0 - mean) * (m0 - mean) + (m1 - mean) * (m1 - mean));
    v += __shfl_xor(v, 16); v += __shfl_xor(v, 32);
    rstd[a] = 1.f / sqrtf(v * (1.f / D_) + 1e-5f);
#pragma unroll
    for (int j = 0; j < 7; ++j) acc[a][j] = cs[j] * (-mean);
  }
#pragma unroll
  for (int bb = 0; bb < 4; ++bb) {
    if (bb == 1) WAITV(42); else if (bb == 2) WAITV(21); else if (bb == 3) WAITV(0);
    BAR();
    if (bb == 0) PIN_CHUNK(0); else if (bb == 1) PIN_CHUNK(1); else if (bb == 2) PIN_CHUNK(2); else PIN_CHUNK(3);
#pragma unroll
    for (int a = 0; a < 2; ++a) A[a][5 * bb + 4] = v4u{Ah[a][bb][0].x, Ah[a][bb][0].y, Ah[a][bb][1].x, Ah[a][bb][1].y};
#pragma unroll
    for (int s = 0; s < 5; ++s) {
#pragma unroll
      for (int j = 0; j < 7; ++j) {
        const v4u bf = *(LAS v4u*)(L + ((5 * bb + s) * 7 + j) * 1024 + l16);
        acc[0][j] = mfma32(bf, A[0][5 * bb + s], acc[0][j]);
        acc[1][j] = mfma32(bf, A[1][5 * bb + s], acc[1][j]);
      }
    }
  }
#pragma unroll
  for (int a = 0; a < 2; ++a) {
    unsigned o[14];
    const float c1 = rstd[a], c2 = rstd[a] * -1.4426950408889634f;
#pragma unroll
    for (int j = 0; j < 7; ++j) {
      float v[4];
#pragma unroll
      for (int e = 0; e < 4; ++e) { const float t = acc[a][j][e] * c1; const float ex = __builtin_amdgcn_exp2f(acc[a][j][e] * c2); v[e] = t * __builtin_amdgcn_rcpf(1.f + ex); }
      o[2 * j] = pk2(v[0], v[1]); o[2 * j + 1] = pk2(v[2], v[3]);
    }
    const int fb = WS_F + ((rt0 + a) * 16 + m) * 3584;
    ST128((v4u{o[0], o[1], o[2], o[3]}), rws, fb + l16, 0);
    ST128((v4u{o[4], o[5], o[6], o[7]}), rws, fb + 1024 + l16, 0);
    ST128((v4u{o[8], o[9], o[10], o[11]}), rws, fb + 2048 + l16, 0);
    if ((m & 3) != 3) ST64((v2u{o[12], o[13]}), rws, fb + 3072 + l8, 0);
  }
  STAMP(3, 1);
}

__global__ __launch_bounds__(256) void k_f2(char* ws, u16* out) {
  extern __shared__ __attribute__((aligned(16))) char smem[];
  LAS char* L = (LAS char*)smem;
  const int tid = threadIdx.x, lane = tid & 63, w = __builtin_amdgcn_readfirstlane(tid >> 6);
  const int b = blockIdx.x, xcd = b & 7, idx = b >> 3, G = 2 * xcd + (idx >> 4), m = idx & 15, hf = m >> 3, ct = m & 7;
  const rsrc_t rws = mkrsrc(ws, WS_END), ro = mkrsrc(out, S_ * D_ * 2);
  STAMP(4, 0);
  const int rt0 = 8 * G + 4 * hf;
  const int l16 = lane * 16, l8 = lane * 8;
  f32x4 acc[4][5];
  {
    const int hb = WS_H + ((rt0 + w) * 8 + ct) * 2560;
    const v4u h0 = LD128(rws, hb + l16, 0), h1 = LD128(rws, hb + 1024 + l16, 0); const v2u h2 = LD64(rws, hb + 2048 + l8, 0);
    const unsigned hp[10] = {h0.x, h0.y, h0.z, h0.w, h1.x, h1.y, h1.z, h1.w, h2.x, h2.y};
#pragma unroll
    for (int a = 0; a < 4; ++a)
#pragma unroll
      for (int j = 0; j < 5; ++j) acc[a][j] = a == w ? f32x4{bflo(hp[2 * j]), bfhi(hp[2 * j]), bflo(hp[2 * j + 1]), bfhi(hp[2 * j + 1])} : f32x4{0.f, 0.f, 0.f, 0.f};
  }
  constexpr int PD = 8;
  v4u A[PD][4], B[PD][5];
  const int wb = WS_W2T + (ct * 4 + w) * W2_WAVE;
  auto ld = [&](int s) {
    const int sl = s % PD;
#pragma unroll
    for (int a = 0; a < 4; ++a) {
      const int base = WS_F + ((rt0 + a) * 16 + 4 * w) * 3584;
      if (s < 12) A[sl][a] = LD128(rws, base + (s / 3) * 3584 + (s % 3) * 1024 + l16, 0);
      else if (s == 12) { const v2u lo = LD64(rws, base + 3072 + l8, 0), hi = LD64(rws, base + 3584 + 3072 + l8, 0); A[sl][a] = v4u{lo.x, lo.y, hi.x, hi.y}; }
      else { const v2u lo = LD64(rws, base + 2 * 3584 + 3072 + l8, 0); A[sl][a] = v4u{lo.x, lo.y, 0u, 0u}; }
    }
#pragma unroll
    for (int j = 0; j < 5; ++j) {
      if (s < 13) B[sl][j] = LD128(rws, wb + (s * 5 + j) * 1024 + l16, 0);
      else { const v2u lo = LD64(rws, wb + 66560 + j * 512 + l8, 0); B[sl][j] = v4u{lo.x, lo.y, 0u, 0u}; }
    }
  };
#pragma unroll
  for (int s = 0; s < PD - 1; ++s) ld(s);
  __builtin_amdgcn_sched_barrier(0);
#pragma unroll
  for (int s = 0; s < 14; ++s) {
    if (s + PD - 1 < 14) ld(s + PD - 1);
    __builtin_amdgcn_sched_barrier(0);
    const int sl = s % PD;
#pragma unroll
    for (int j = 0; j < 5; ++j)
#pragma unroll
      for (int a = 0; a < 4; ++a) {
        if (s < 13) acc[a][j] = mfma32(B[sl][j], A[sl][a], acc[a][j]);
        else acc[a][j] = mfma16(v2u{B[sl][j].x, B[sl][j].y}, v2u{A[sl][a].x, A[sl][a].y}, acc[a][j]);
      }
    __builtin_amdgcn_sched_barrier(0);
  }
  LAS f32x4* red = (LAS f32x4*)L;
#pragma unroll
  for (int a = 0; a < 4; ++a)
#pragma unroll
    for (int j = 0; j < 5; ++j) red[((w * 4 + a) * 5 + j) * 64 + lane] = acc[a][j];
  __syncthreads();
#pragma unroll
  for (int i = 0; i < 3; ++i) {
    const int it = 64 * i + lane;
    if (it < 160) {
      const int tok = it / 10, pc = it % 10, j = pc >> 1, g0 = 2 * (pc & 1);
      const int o = (w * 5 + j) * 64 + g0 * 16 + tok;
      f32x4 lo = (red[o] + red[o + 1280]) + (red[o + 2560] + red[o + 3840]);
      f32x4 hi = (red[o + 16] + red[o + 1296]) + (red[o + 2576] + red[o + 3856]);
      ST128((v4u{pk2(lo[0], lo[1]), pk2(lo[2], lo[3]), pk2(hi[0], hi[1]), pk2(hi[2], hi[3])}), ro, (16 * (rt0 + w) + tok) * 1280 + 160 * ct + pc * 16, 0);
    }
  }
  STAMP(4, 1);
}

extern "C" void kernel_launch(void* const* d_in, const int* in_sizes, int n_in, void* d_out, int out_size, void* d_ws, size_t ws_size, hipStream_t stream) {
  const u16* x = (const u16*)d_in[0]; const u16* wqkv = (const u16*)d_in[1]; const u16* wo = (const u16*)d_in[2];
  const u16* w1 = (const u16*)d_in[4]; const u16* w2 = (const u16*)d_in[5];
  char* ws = (char*)d_ws;
  (void)hipFuncSetAttribute((const void*)k1, hipFuncAttributeMaxDynamicSharedMemorySize, K1_LDS);
  (void)hipFuncSetAttribute((const void*)k_wo, hipFuncAttributeMaxDynamicSharedMemorySize, 72 * 1024);
  (void)hipFuncSetAttribute((const void*)k_f1, hipFuncAttributeMaxDynamicSharedMemorySize, 141 * 1024);
  (void)hipFuncSetAttribute((const void*)k_f2, hipFuncAttributeMaxDynamicSharedMemorySize, 80 * 1024);
  k1<<<256, 768, K1_LDS, stream>>>(x, wqkv, wo, w1, w2, ws);
  k_o<<<256, 256, 0, stream>>>(ws);
  k_wo<<<256, 256, 72 * 1024, stream>>>(x, ws);
  k_f1<<<256, 256, 141 * 1024, stream>>>(ws);
  k_f2<<<256, 256, 80 * 1024, stream>>>(ws, (u16*)d_out);
}
```

```cpp
#include <hip/hip_runtime.h>
#include <stdint.h>
typedef unsigned short u16;

__global__ __launch_bounds__(256) void fused_block(const u16* x, const u16* wqkv, const u16* wo, const u16* w1, const u16* w2, u16* out, char* ws) {
  const int w = __builtin_amdgcn_readfirstlane(threadIdx.x >> 6);
  asm volatile(
    "L_start_%=:\n"
    "s_load_dwordx2 s[16:17], %0, 8\n"
    "s_load_dwordx2 s[4:5], %0, 48\n"
    "s_load_dwordx2 s[20:21], %0, 0\n"
    "s_load_dwordx2 s[22:23], %0, 16\n"
    "s_getpc_b64 s[12:13]\n"
    "L_pc_%=:\n"
    "s_add_u32 s32, s12, T_st_%=@rel32@lo+4\n"
    "s_addc_u32 s33, s13, T_st_%=@rel32@hi+12\n"
    "s_lshl_b32 s31, %1, 2\n"
    "s_add_u32 s31, s31, %2\n"
    "s_mul_i32 s30, s31, 384\n"
    "s_add_u32 s10, s32, s30\n"
    "s_addc_u32 s11, s33, 0\n"
    "v_mbcnt_lo_u32_b32 v3, -1, 0\n"
    "v_mbcnt_hi_u32_b32 v3, -1, v3\n"
    "v_lshlrev_b32 v0, 4, v3\n"
    "s_and_b32 s13, s13, 0xffff\n"
    "s_mov_b32 s14, L_end_%=-L_pc_%=\n"
    "s_mov_b32 s15, 0x20000\n"
    "s_lshr_b32 s31, %1, 3\n"
    "s_lshl_b32 s31, s31, 2\n"
    "s_add_u32 s31, s31, %2\n"
    "s_lshl_b32 s31, s31, 7\n"
    "v_lshlrev_b32 v156, 14, v3\n"
    "buffer_load_dword v150, v156, s[12:15], s31 offen\n"
    "s_mov_b32 s12, s32\n"
    "s_and_b32 s13, s33, 0xffff\n"
    "s_mov_b32 s14, T_end_%=-T_st_%=\n"
    "s_lshl_b32 s31, %1, 10\n"
    "s_add_u32 s31, s31, T_bt_%=-T_st_%=\n"
    "s_lshl_b32 s32, %2, 12\n"
    "s_add_u32 s32, s32, T_lt_%=-T_st_%=\n"
    "v_and_b32 v157, 0x180, v0\n"
    "buffer_load_dword v151, v157, s[12:15], s30 offen\n"
    "buffer_load_dwordx4 v[96:99], v0, s[12:15], s31 offen\n"
    "buffer_load_dwordx4 v[80:83], v0, s[12:15], s32 offen offset:0\n"
    "buffer_load_dwordx4 v[84:87], v0, s[12:15], s32 offen offset:1024\n"
    "buffer_load_dwordx4 v[88:91], v0, s[12:15], s32 offen offset:2048\n"
    "buffer_load_dwordx4 v[92:95], v0, s[12:15], s32 offen offset:3072\n"
    "v_and_b32 v4, 15, v3\n"
    "v_lshrrev_b32 v5, 4, v3\n"
    "v_mov_b32 v108, 0\n"
    "s_mul_i32 s31, %1, 205\n"
    "s_lshr_b32 s31, s31, 11\n"
    "s_mul_i32 s32, s31, 10\n"
    "s_sub_u32 s32, %1, s32\n"
    "s_lshr_b32 s28, s31, 1\n"
    "s_and_b32 s33, s31, 1\n"
    "s_mul_i32 s33, s33, 10\n"
    "s_add_u32 s33, s33, 10\n"
    "s_lshl_b32 s31, s31, 1\n"
    "s_sub_u32 s31, s31, 40\n"
    "s_cmp_lt_u32 %1, 200\n"
    "s_cselect_b32 s28, s28, s31\n"
    "s_cselect_b32 s33, s33, 0\n"
    "s_add_u32 s32, s32, s33\n"
    "s_lshl2_add_u32 s28, s28, %2\n"
    "s_mul_i32 s28, s28, 61440\n"
    "s_lshl_b32 s32, s32, 7\n"
    "s_add_u32 s28, s28, s32\n"
    "s_bfe_u32 s31, %1, 0x20001\n"
    "s_lshr_b32 s32, %1, 6\n"
    "s_lshl2_add_u32 s31, s31, s32\n"
    "s_lshl2_add_u32 s31, s31, %2\n"
    "s_mul_i32 s25, s31, 40960\n"
    "s_add_u32 s26, s25, 1280\n"
    "s_bfe_u32 s31, %1, 196611\n"
    "s_mul_i32 s31, s31, 160\n"
    "s_and_b32 s31, s31, 0x780\n"
    "s_add_u32 s24, s25, s31\n"
    "s_mul_i32 s27, %2, 20480\n"
    "s_add_u32 s27, s27, 71680\n"
    "v_lshrrev_b32 v156, 3, v3\n"
    "v_and_b32 v157, 7, v3\n"
    "v_mul_u32_u24 v158, 3840, v156\n"
    "v_lshl_add_u32 v109, v157, 4, v158\n"
    "v_mul_u32_u24 v158, 1280, v156\n"
    "v_lshl_add_u32 v170, v157, 4, v158\n"
    "v_xor_b32 v157, v157, v5\n"
    "v_lshl_add_u32 v136, v157, 4, v158\n"
    "v_xor_b32 v157, 64, v136\n"
    "v_add_u32 v137, 9216, v157\n"
    "v_add_u32 v138, 18432, v136\n"
    "v_add_u32 v139, 27648, v157\n"
    "s_mov_b32 s6, 21741568\n"
    "s_mov_b32 s7, 0x20000\n"
    "s_mov_b32 s18, 2457600\n"
    "s_mov_b32 s19, 0x20000\n"
    "s_mul_i32 s31, %2, 5120\n"
    "s_add_u32 s31, s31, 40960\n"
    "s_mov_b32 m0, s31\n"
    "v_lshrrev_b32 v157, 2, v4\n"
    "v_lshlrev_b32 v157, 7, v157\n"
    "v_lshl_add_u32 v157, v5, 5, v157\n"
    "v_add_u32 v157, s31, v157\n"
    "v_and_b32 v156, 3, v3\n"
    "v_lshl_add_u32 v162, v156, 3, v157\n"
    "v_and_b32 v156, 1, v3\n"
    "v_lshl_add_u32 v157, v156, 4, v157\n"
    "v_bfe_u32 v156, v3, 1, 1\n"
    "v_lshl_add_u32 v163, v156, 3, v157\n"
    "v_subrev_u32 v163, 40960, v163\n"
    "v_add_u32 v1, 0x10000, v0\n"
    "v_add_u32 v2, 0x20000, v0\n"
    "v_lshlrev_b32 v6, 26, v3\n"
    "v_mov_b32 v116, -1\n"
    "v_mov_b32 v112, 0x3f803f80\n"
    "v_mov_b32 v117, -1\n"
    "v_mov_b32 v113, 0x3f803f80\n"
    "v_mov_b32 v118, -1\n"
    "v_mov_b32 v114, 0x3f803f80\n"
    "v_mov_b32 v119, -1\n"
    "v_mov_b32 v115, 0x3f803f80\n"
    "s_lshl_b32 s31, %2, 2\n"
    "v_add_u32 v164, s31, v2\n"
    "s_and_b32 s31, %2, 1\n"
    "s_lshl_b32 s31, s31, 10\n"
    "v_add_u32 v165, s31, v2\n"
    "v_lshlrev_b32 v156, 10, v5\n"
    "v_lshl_add_u32 v156, v4, 3, v156\n"
    "v_add_u32 v167, 153600, v156\n"
    "s_mul_i32 s31, %2, 2048\n"
    "s_add_u32 s31, s31, 65536\n"
    "v_mov_b32 v156, 32\n"
    "v_mad_u32_u24 v168, v3, v156, s31\n"
    "v_mov_b32 v172, 0x3fb8aa3b\n"
    "v_mov_b32 v174, 0xb5c1a25e\n"
    "v_mov_b32 v176, 0x358637bd\n"
    "v_mul_u32_u24 v11, 1280, v4\n"
    "v_lshl_add_u32 v10, v5, 4, v11\n"
    "v_and_b32 v156, 1, v5\n"
    "v_lshl_add_u32 v11, v156, 4, v11\n"
    "v_lshrrev_b32 v156, 1, v5\n"
    "v_lshl_add_u32 v11, v156, 3, v11\n"
    "v_lshrrev_b32 v156, 1, v4\n"
    "v_xor_b32 v156, v156, v5\n"
    "v_lshlrev_b32 v156, 4, v156\n"
    "v_lshl_add_u32 v156, v4, 7, v156\n"
    "s_waitcnt lgkmcnt(0)\n"
    "s_and_b32 s17, s17, 0xffff\n"
    "buffer_load_dwordx4 v109, s[16:19], s28 offen lds\n"
    "s_add_u32 s31, s28, 29696\n"
    "buffer_load_dwordx4 v109, s[16:19], s31 offen offset:1024 lds\n"
    "s_add_u32 s31, s28, 243712\n"
    "s_add_u32 s32, s28, 273408\n"
    "s_cmp_lt_u32 %1, 200\n"
    "s_cbranch_scc1 skip_1_%=\n"
    "buffer_load_dwordx4 v109, s[16:19], s31 offen offset:2048 lds\n"
    "buffer_load_dwordx4 v109, s[16:19], s32 offen offset:3072 lds\n"
    "skip_1_%=:\n"
    "s_and_b32 s5, s5, 0xffff\n"
    "buffer_load_dword v108, v108, s[4:7], 0 offen sc1\n"
    "s_mov_b32 s12, s20\n"
    "s_and_b32 s13, s21, 0xffff\n"
    "s_mov_b32 s14, 2621440\n"
    "s_mov_b32 s30, 200000\n"
    "s_load_dwordx16 s[36:51], s[10:11], 0\n"
    "s_load_dwordx16 s[52:67], s[10:11], 64\n"
    "s_load_dwordx16 s[68:83], s[10:11], 128\n"
    "s_load_dwordx16 s[84:99], s[10:11], 192\n"
    "s_load_dword s20, s[10:11], 256\n"
    "s_load_dword s21, s[10:11], 320\n"
    "v_add_u32 v110, s27, v156\n"
    "v_xor_b32 v111, 64, v110\n"
    "s_lshl_b32 s31, %2, 10\n"
    "v_add_u32 v166, s31, v0\n"
    "s_sleep 6\n"
    "s_add_u32 m0, s27, 0\n"
    "s_nop 0\n"
    "buffer_load_dwordx4 v136, s[12:15], s24 offen offset:0 lds\n"
    "buffer_load_dwordx4 v137, s[12:15], s24 offen offset:1024 lds\n"
    "buffer_load_dwordx4 v138, s[12:15], s24 offen offset:2048 lds\n"
    "buffer_load_dwordx4 v139, s[12:15], s24 offen offset:3072 lds\n"
    "s_load_dwordx2 s[34:35], %0, 40\n"
    "s_lshl2_add_u32 s31, %1, %2\n"
    "s_mul_i32 s31, s31, 2560\n"
    "v_mov_b32 v157, s31\n"
    "s_waitcnt lgkmcnt(0)\n"
    "s_waitcnt vmcnt(13)\n"
    "global_load_dword v150, v157, s[34:35]\n"
    "s_lshl_b32 s31, %2, 1\n"
    "s_waitcnt vmcnt(12)\n"
    "v_mad_u32_u24 v184, v97, s31, v96\n"
    "v_add_u32 v185, v184, v97\n"
    "v_lshl_add_u32 v186, v97, 3, v184\n"
    "v_add_u32 v187, v186, v97\n"
    "s_lshl_b32 s31, %2, 9\n"
    "v_add_u32 v98, s31, v98\n"
    "s_waitcnt vmcnt(5)\n"
    "ds_read_b64_tr_b16 v[188:189], v162 offset:0\n"
    "ds_read_b64_tr_b16 v[190:191], v162 offset:512\n"
    "ds_read_b64_tr_b16 v[192:193], v162 offset:1024\n"
    "ds_read_b64_tr_b16 v[194:195], v162 offset:1536\n"
    "ds_read_b64_tr_b16 v[196:197], v162 offset:2048\n"
    "ds_read_b64_tr_b16 v[198:199], v162 offset:2560\n"
    "ds_read_b64_tr_b16 v[200:201], v162 offset:3072\n"
    "ds_read_b64_tr_b16 v[202:203], v162 offset:3584\n"
    "s_waitcnt lgkmcnt(6)\n"
    "buffer_store_dwordx4 v[188:191], v184, s[4:7], 0 offen sc1\n"
    "s_waitcnt lgkmcnt(0)\n"
    "s_cmp_lt_u32 %1, 200\n"
    "s_cbranch_scc1 skip_2_%=\n"
    "buffer_store_dwordx4 v[196:199], v186, s[4:7], 0 offen sc1\n"
    "buffer_store_dwordx4 v[200:203], v187, s[4:7], 0 offen sc1\n"
    "skip_2_%=:\n"
    "buffer_store_dwordx4 v[192:195], v185, s[4:7], 0 offen sc1\n"
    "buffer_store_dwordx4 v[116:119], v0, s[4:7], s37 offen sc1\n"
    "v_dot2_f32_bf16 v204, v188, v112, 0\n"
    "v_dot2_f32_bf16 v205, v192, v112, 0\n"
    "v_dot2_f32_bf16 v206, v196, v112, 0\n"
    "v_dot2_f32_bf16 v207, v200, v112, 0\n"
    "v_dot2_f32_bf16 v204, v189, v112, v204\n"
    "v_dot2_f32_bf16 v205, v193, v112, v205\n"
    "v_dot2_f32_bf16 v206, v197, v112, v206\n"
    "v_dot2_f32_bf16 v207, v201, v112, v207\n"
    "v_dot2_f32_bf16 v204, v190, v112, v204\n"
    "v_dot2_f32_bf16 v205, v194, v112, v205\n"
    "v_dot2_f32_bf16 v206, v198, v112, v206\n"
    "v_dot2_f32_bf16 v207, v202, v112, v207\n"
    "v_dot2_f32_bf16 v204, v191, v112, v204\n"
    "v_dot2_f32_bf16 v205, v195, v112, v205\n"
    "v_dot2_f32_bf16 v206, v199, v112, v206\n"
    "v_dot2_f32_bf16 v207, v203, v112, v207\n"
    "v_readfirstlane_b32 s29, v108\n"
    "s_add_u32 s29, s29, 1\n"
    "s_nop 0\n"
    "v_mov_b32 v9, s29\n"
    "v_add_f32 v204, v204, v205\n"
    "v_add_f32 v206, v206, v207\n"
    "ds_write_b32 v164, v204 offset:28672\n"
    "ds_write_b32 v164, v206 offset:29696\n"
    "s_mov_b32 s16, s10\n"
    "s_and_b32 s17, s11, 0xffff\n"
    "s_add_u32 s47, s47, T_ct_%=-T_st_%=-392832\n"
    "buffer_load_dwordx4 v[100:103], v0, s[16:19], s47 offen offset:0\n"
    "buffer_load_dwordx4 v[104:107], v0, s[16:19], s47 offen offset:1024\n"
    "s_add_u32 s67, s67, T_ct_%=-T_st_%=-392832\n"
    "s_add_u32 m0, s82, 45056\n"
    "buffer_load_dword v171, v0, s[16:19], s67 offen\n"
    "s_mov_b32 s16, s22\n"
    "s_and_b32 s17, s23, 0xffff\n"
    "s_mov_b32 s18, 819200\n"
    "buffer_load_dwordx4 v170, s[16:19], s65 offen lds\n"
    "s_load_dwordx2 s[16:17], %0, s46\n"
    "s_mov_b32 s18, 2211840\n"
    "s_mov_b32 s22, 0x00ff00ff\n"
    "s_mov_b32 s23, 0x00ff00ff\n"
    "v_cndmask_b32 v177, 0, v176, s[22:23]\n"
    "s_add_u32 m0, s27, 4096\n"
    "s_add_u32 s24, s24, 128\n"
    "s_cmp_eq_u32 s24, s26\n"
    "s_cselect_b32 s24, s25, s24\n"
    "buffer_load_dwordx4 v136, s[12:15], s24 offen offset:0 lds\n"
    "buffer_load_dwordx4 v137, s[12:15], s24 offen offset:1024 lds\n"
    "buffer_load_dwordx4 v138, s[12:15], s24 offen offset:2048 lds\n"
    "buffer_load_dwordx4 v139, s[12:15], s24 offen offset:3072 lds\n"
    "s_waitcnt vmcnt(12)\n"
    "ds_read_b128 a[0:3], v110 offset:0\n"
    "ds_read_b128 a[80:83], v110 offset:2048\n"
    "ds_read_b128 a[4:7], v111 offset:0\n"
    "ds_read_b128 a[84:87], v111 offset:2048\n"
    "s_add_u32 m0, s27, 8192\n"
    "s_add_u32 s24, s24, 128\n"
    "s_cmp_eq_u32 s24, s26\n"
    "s_cselect_b32 s24, s25, s24\n"
    "buffer_load_dwordx4 v136, s[12:15], s24 offen offset:0 lds\n"
    "buffer_load_dwordx4 v137, s[12:15], s24 offen offset:1024 lds\n"
    "buffer_load_dwordx4 v138, s[12:15], s24 offen offset:2048 lds\n"
    "buffer_load_dwordx4 v139, s[12:15], s24 offen offset:3072 lds\n"
    "s_waitcnt vmcnt(12)\n"
    "s_waitcnt lgkmcnt(0)\n"
    "s_barrier\n"
    "s_and_b32 s17, s17, 0xffff\n"
    "ds_read_b128 v[204:207], v165 offset:28672\n"
    "v_lshlrev_b32 v165, 3, v3\n"
    "s_waitcnt lgkmcnt(0)\n"
    "v_add_f32 v204, v204, v205\n"
    "v_add_f32 v206, v206, v207\n"
    "v_add_f32 v8, v204, v206\n"
    "buffer_store_dwordx2 v[8:9], v165, s[4:7], s36 offen sc1\n"
    "s_add_u32 m0, s27, 12288\n"
    "s_add_u32 s24, s24, 128\n"
    "s_cmp_eq_u32 s24, s26\n"
    "s_cselect_b32 s24, s25, s24\n"
    "buffer_load_dwordx4 v136, s[12:15], s24 offen offset:0 lds\n"
    "buffer_load_dwordx4 v137, s[12:15], s24 offen offset:1024 lds\n"
    "buffer_load_dwordx4 v138, s[12:15], s24 offen offset:2048 lds\n"
    "buffer_load_dwordx4 v139, s[12:15], s24 offen offset:3072 lds\n"
    "s_cmp_lt_u32 %2, 2\n"
    "s_cselect_b32 s20, s29, 0\n"
    "s_waitcnt vmcnt(9)\n"
    "ds_read_b128 a[8:11], v110 offset:4096\n"
    "ds_read_b128 a[88:91], v110 offset:6144\n"
    "ds_read_b128 a[12:15], v111 offset:4096\n"
    "ds_read_b128 a[92:95], v111 offset:6144\n"
    "s_add_u32 m0, s27, 16384\n"
    "s_add_u32 s24, s24, 128\n"
    "s_cmp_eq_u32 s24, s26\n"
    "s_cselect_b32 s24, s25, s24\n"
    "buffer_load_dwordx4 v136, s[12:15], s24 offen offset:0 lds\n"
    "buffer_load_dwordx4 v137, s[12:15], s24 offen offset:1024 lds\n"
    "buffer_load_dwordx4 v138, s[12:15], s24 offen offset:2048 lds\n"
    "buffer_load_dwordx4 v139, s[12:15], s24 offen offset:3072 lds\n"
    "v_mfma_f32_16x16x32_bf16 v[120:123], a[0:3], a[0:3], 0\n"
    "v_mfma_f32_16x16x32_bf16 v[128:131], v[112:115], a[0:3], 0\n"
    "v_mfma_f32_16x16x32_bf16 v[124:127], a[80:83], a[80:83], 0\n"
    "v_mfma_f32_16x16x32_bf16 v[132:135], v[112:115], a[80:83], 0\n"
    "v_mfma_f32_16x16x32_bf16 v[120:123], a[4:7], a[4:7], v[120:123]\n"
    "v_mfma_f32_16x16x32_bf16 v[128:131], v[112:115], a[4:7], v[128:131]\n"
    "v_mfma_f32_16x16x32_bf16 v[124:127], a[84:87], a[84:87], v[124:127]\n"
    "v_mfma_f32_16x16x32_bf16 v[132:135], v[112:115], a[84:87], v[132:135]\n"
    "s_waitcnt vmcnt(9)\n"
    "ds_read_b128 a[16:19], v110 offset:8192\n"
    "ds_read_b128 a[96:99], v110 offset:10240\n"
    "ds_read_b128 a[20:23], v111 offset:8192\n"
    "ds_read_b128 a[100:103], v111 offset:10240\n"
    "s_add_u32 m0, s27, 0\n"
    "s_add_u32 s24, s24, 128\n"
    "s_cmp_eq_u32 s24, s26\n"
    "s_cselect_b32 s24, s25, s24\n"
    "buffer_load_dwordx4 v136, s[12:15], s24 offen offset:0 lds\n"
    "buffer_load_dwordx4 v137, s[12:15], s24 offen offset:1024 lds\n"
    "buffer_load_dwordx4 v138, s[12:15], s24 offen offset:2048 lds\n"
    "buffer_load_dwordx4 v139, s[12:15], s24 offen offset:3072 lds\n"
    "ds_read_b64_tr_b16 v[180:181], v163 offset:45056\n"
    "ds_read_b64_tr_b16 v[182:183], v163 offset:45568\n"
    "s_waitcnt lgkmcnt(9)\n"
    "v_mfma_f32_16x16x32_bf16 v[120:123], a[8:11], a[8:11], v[120:123]\n"
    "v_mfma_f32_16x16x32_bf16 v[128:131], v[112:115], a[8:11], v[128:131]\n"
    "s_waitcnt lgkmcnt(8)\n"
    "v_mfma_f32_16x16x32_bf16 v[124:127], a[88:91], a[88:91], v[124:127]\n"
    "v_mfma_f32_16x16x32_bf16 v[132:135], v[112:115], a[88:91], v[132:135]\n"
    "s_waitcnt lgkmcnt(7)\n"
    "v_mfma_f32_16x16x32_bf16 v[120:123], a[12:15], a[12:15], v[120:123]\n"
    "v_mfma_f32_16x16x32_bf16 v[128:131], v[112:115], a[12:15], v[128:131]\n"
    "s_waitcnt lgkmcnt(6)\n"
    "v_mfma_f32_16x16x32_bf16 v[124:127], a[92:95], a[92:95], v[124:127]\n"
    "v_mfma_f32_16x16x32_bf16 v[132:135], v[112:115], a[92:95], v[132:135]\n"
    "s_waitcnt vmcnt(8)\n"
    "ds_read_b128 a[24:27], v110 offset:12288\n"
    "ds_read_b128 a[104:107], v110 offset:14336\n"
    "ds_read_b128 a[28:31], v111 offset:12288\n"
    "ds_read_b128 a[108:111], v111 offset:14336\n"
    "s_add_u32 m0, s27, 4096\n"
    "s_add_u32 s24, s24, 128\n"
    "s_cmp_eq_u32 s24, s26\n"
    "s_cselect_b32 s24, s25, s24\n"
    "buffer_load_dwordx4 v136, s[12:15], s24 offen offset:0 lds\n"
    "buffer_load_dwordx4 v137, s[12:15], s24 offen offset:1024 lds\n"
    "buffer_load_dwordx4 v138, s[12:15], s24 offen offset:2048 lds\n"
    "buffer_load_dwordx4 v139, s[12:15], s24 offen offset:3072 lds\n"
    "s_waitcnt lgkmcnt(4)\n"
    "buffer_store_dwordx4 v[180:183], v171, s[4:7], s66 offen sc1\n"
    "v_mfma_f32_16x16x32_bf16 v[120:123], a[16:19], a[16:19], v[120:123]\n"
    "v_mfma_f32_16x16x32_bf16 v[128:131], v[112:115], a[16:19], v[128:131]\n"
    "v_mfma_f32_16x16x32_bf16 v[124:127], a[96:99], a[96:99], v[124:127]\n"
    "v_mfma_f32_16x16x32_bf16 v[132:135], v[112:115], a[96:99], v[132:135]\n"
    "v_mfma_f32_16x16x32_bf16 v[120:123], a[20:23], a[20:23], v[120:123]\n"
    "v_mfma_f32_16x16x32_bf16 v[128:131], v[112:115], a[20:23], v[128:131]\n"
    "v_mfma_f32_16x16x32_bf16 v[124:127], a[100:103], a[100:103], v[124:127]\n"
    "v_mfma_f32_16x16x32_bf16 v[132:135], v[112:115], a[100:103], v[132:135]\n"
    "buffer_store_dwordx4 v[116:119], v0, s[4:7], s40 offen offset:0 sc1\n"
    "s_waitcnt vmcnt(10)\n"
    "ds_read_b128 a[32:35], v110 offset:16384\n"
    "ds_read_b128 a[112:115], v110 offset:18432\n"
    "ds_read_b128 a[36:39], v111 offset:16384\n"
    "ds_read_b128 a[116:119], v111 offset:18432\n"
    "s_add_u32 m0, s27, 8192\n"
    "s_add_u32 s24, s24, 128\n"
    "s_cmp_eq_u32 s24, s26\n"
    "s_cselect_b32 s24, s25, s24\n"
    "buffer_load_dwordx4 v136, s[12:15], s24 offen offset:0 lds\n"
    "buffer_load_dwordx4 v137, s[12:15], s24 offen offset:1024 lds\n"
    "buffer_load_dwordx4 v138, s[12:15], s24 offen offset:2048 lds\n"
    "buffer_load_dwordx4 v139, s[12:15], s24 offen offset:3072 lds\n"
    "s_waitcnt lgkmcnt(7)\n"
    "v_mfma_f32_16x16x32_bf16 v[120:123], a[24:27], a[24:27], v[120:123]\n"
    "v_mfma_f32_16x16x32_bf16 v[128:131], v[112:115], a[24:27], v[128:131]\n"
    "s_waitcnt lgkmcnt(6)\n"
    "v_mfma_f32_16x16x32_bf16 v[124:127], a[104:107], a[104:107], v[124:127]\n"
    "v_mfma_f32_16x16x32_bf16 v[132:135], v[112:115], a[104:107], v[132:135]\n"
    "s_waitcnt lgkmcnt(5)\n"
    "v_mfma_f32_16x16x32_bf16 v[120:123], a[28:31], a[28:31], v[120:123]\n"
    "v_mfma_f32_16x16x32_bf16 v[128:131], v[112:115], a[28:31], v[128:131]\n"
    "s_waitcnt lgkmcnt(4)\n"
    "v_mfma_f32_16x16x32_bf16 v[124:127], a[108:111], a[108:111], v[124:127]\n"
    "v_mfma_f32_16x16x32_bf16 v[132:135], v[112:115], a[108:111], v[132:135]\n"
    "buffer_store_dwordx4 v[116:119], v0, s[4:7], s40 offen offset:1024 sc1\n"
    "s_waitcnt vmcnt(11)\n"
    "ds_read_b128 a[40:43], v110 offset:0\n"
    "ds_read_b128 a[120:123], v110 offset:2048\n"
    "ds_read_b128 a[44:47], v111 offset:0\n"
    "ds_read_b128 a[124:127], v111 offset:2048\n"
    "s_add_u32 m0, s27, 12288\n"
    "s_add_u32 s24, s24, 128\n"
    "s_cmp_eq_u32 s24, s26\n"
    "s_cselect_b32 s24, s25, s24\n"
    "buffer_load_dwordx4 v136, s[12:15], s24 offen offset:0 lds\n"
    "buffer_load_dwordx4 v137, s[12:15], s24 offen offset:1024 lds\n"
    "buffer_load_dwordx4 v138, s[12:15], s24 offen offset:2048 lds\n"
    "buffer_load_dwordx4 v139, s[12:15], s24 offen offset:3072 lds\n"
    "buffer_load_dwordx4 v[184:187], v98, s[4:7], 0 offen sc1\n"
    "buffer_load_dwordx4 v[188:191], v98, s[4:7], 0 offen offset:2048 sc1\n"
    "buffer_load_dwordx4 v[192:195], v98, s[4:7], s45 offen sc1\n"
    "s_waitcnt lgkmcnt(7)\n"
    "v_mfma_f32_16x16x32_bf16 v[120:123], a[32:35], a[32:35], v[120:123]\n"
    "v_mfma_f32_16x16x32_bf16 v[128:131], v[112:115], a[32:35], v[128:131]\n"
    "s_waitcnt lgkmcnt(6)\n"
    "v_mfma_f32_16x16x32_bf16 v[124:127], a[112:115], a[112:115], v[124:127]\n"
    "v_mfma_f32_16x16x32_bf16 v[132:135], v[112:115], a[112:115], v[132:135]\n"
    "s_waitcnt lgkmcnt(5)\n"
    "v_mfma_f32_16x16x32_bf16 v[120:123], a[36:39], a[36:39], v[120:123]\n"
    "v_mfma_f32_16x16x32_bf16 v[128:131], v[112:115], a[36:39], v[128:131]\n"
    "s_waitcnt lgkmcnt(4)\n"
    "v_mfma_f32_16x16x32_bf16 v[124:127], a[116:119], a[116:119], v[124:127]\n"
    "v_mfma_f32_16x16x32_bf16 v[132:135], v[112:115], a[116:119], v[132:135]\n"
    "buffer_store_dwordx4 v[116:119], v0, s[4:7], s41 offen offset:0 sc1\n"
    "s_waitcnt vmcnt(15)\n"
    "ds_read_b128 a[48:51], v110 offset:4096\n"
    "ds_read_b128 a[128:131], v110 offset:6144\n"
    "ds_read_b128 a[52:55], v111 offset:4096\n"
    "ds_read_b128 a[132:135], v111 offset:6144\n"
    "s_add_u32 m0, s27, 16384\n"
    "s_add_u32 s24, s24, 128\n"
    "s_cmp_eq_u32 s24, s26\n"
    "s_cselect_b32 s24, s25, s24\n"
    "buffer_load_dwordx4 v136, s[12:15], s24 offen offset:0 lds\n"
    "buffer_load_dwordx4 v137, s[12:15], s24 offen offset:1024 lds\n"
    "buffer_load_dwordx4 v138, s[12:15], s24 offen offset:2048 lds\n"
    "buffer_load_dwordx4 v139, s[12:15], s24 offen offset:3072 lds\n"
    "s_waitcnt lgkmcnt(7)\n"
    "v_mfma_f32_16x16x32_bf16 v[120:123], a[40:43], a[40:43], v[120:123]\n"
    "v_mfma_f32_16x16x32_bf16 v[128:131], v[112:115], a[40:43], v[128:131]\n"
    "s_waitcnt lgkmcnt(6)\n"
    "v_mfma_f32_16x16x32_bf16 v[124:127], a[120:123], a[120:123], v[124:127]\n"
    "v_mfma_f32_16x16x32_bf16 v[132:135], v[112:115], a[120:123], v[132:135]\n"
    "s_waitcnt lgkmcnt(5)\n"
    "v_mfma_f32_16x16x32_bf16 v[120:123], a[44:47], a[44:47], v[120:123]\n"
    "v_mfma_f32_16x16x32_bf16 v[128:131], v[112:115], a[44:47], v[128:131]\n"
    "s_waitcnt lgkmcnt(4)\n"
    "v_mfma_f32_16x16x32_bf16 v[124:127], a[124:127], a[124:127], v[124:127]\n"
    "v_mfma_f32_16x16x32_bf16 v[132:135], v[112:115], a[124:127], v[132:135]\n"
    "buffer_store_dwordx4 v[116:119], v0, s[4:7], s42 offen offset:0 sc1\n"
    "s_waitcnt vmcnt(14)\n"
    "ds_read_b128 a[56:59], v110 offset:8192\n"
    "ds_read_b128 a[136:139], v110 offset:10240\n"
    "ds_read_b128 a[60:63], v111 offset:8192\n"
    "ds_read_b128 a[140:143], v111 offset:10240\n"
    "s_waitcnt lgkmcnt(7)\n"
    "v_mfma_f32_16x16x32_bf16 v[120:123], a[48:51], a[48:51], v[120:123]\n"
    "v_mfma_f32_16x16x32_bf16 v[128:131], v[112:115], a[48:51], v[128:131]\n"
    "s_waitcnt lgkmcnt(6)\n"
    "v_mfma_f32_16x16x32_bf16 v[124:127], a[128:131], a[128:131], v[124:127]\n"
    "v_mfma_f32_16x16x32_bf16 v[132:135], v[112:115], a[128:131], v[132:135]\n"
    "s_waitcnt lgkmcnt(5)\n"
    "v_mfma_f32_16x16x32_bf16 v[120:123], a[52:55], a[52:55], v[120:123]\n"
    "v_mfma_f32_16x16x32_bf16 v[128:131], v[112:115], a[52:55], v[128:131]\n"
    "s_waitcnt lgkmcnt(4)\n"
    "v_mfma_f32_16x16x32_bf16 v[124:127], a[132:135], a[132:135], v[124:127]\n"
    "v_mfma_f32_16x16x32_bf16 v[132:135], v[112:115], a[132:135], v[132:135]\n"
    "buffer_store_dwordx4 v[116:119], v0, s[4:7], s42 offen offset:1024 sc1\n"
    "s_waitcnt vmcnt(10)\n"
    "ds_read_b128 a[64:67], v110 offset:12288\n"
    "ds_read_b128 a[144:147], v110 offset:14336\n"
    "ds_read_b128 a[68:71], v111 offset:12288\n"
    "ds_read_b128 a[148:151], v111 offset:14336\n"
    "s_waitcnt vmcnt(9)\n"
    "v_xor_b32 v196, s29, v185\n"
    "v_xor_b32 v197, s29, v187\n"
    "s_waitcnt vmcnt(8)\n"
    "v_xor_b32 v198, s29, v189\n"
    "v_xor_b32 v199, s29, v191\n"
    "v_or3_b32 v196, v196, v197, v198\n"
    "s_waitcnt vmcnt(7)\n"
    "v_xor_b32 v197, s20, v193\n"
    "v_xor_b32 v198, s20, v195\n"
    "v_or3_b32 v196, v196, v197, v198\n"
    "v_or_b32 v196, v196, v199\n"
    "v_cmp_ne_u32 vcc, 0, v196\n"
    "s_cbranch_vccnz g2s_3_%=\n"
    "g2r_4_%=:\n"
    "s_barrier\n"
    "s_add_u32 m0, s82, 0\n"
    "s_nop 0\n"
    "buffer_load_dwordx4 v0, s[4:7], s52 offen offset:0 sc1 lds\n"
    "buffer_load_dwordx4 v0, s[4:7], s53 offen offset:1024 sc1 lds\n"
    "buffer_load_dwordx4 v0, s[4:7], s54 offen offset:2048 sc1 lds\n"
    "buffer_load_dwordx4 v0, s[4:7], s55 offen offset:3072 sc1 lds\n"
    "s_add_u32 m0, m0, 4096\n"
    "s_nop 0\n"
    "buffer_load_dwordx4 v0, s[4:7], s56 offen offset:0 sc1 lds\n"
    "s_waitcnt lgkmcnt(7)\n"
    "v_mfma_f32_16x16x32_bf16 v[120:123], a[56:59], a[56:59], v[120:123]\n"
    "v_add_f32 v184, v184, v188\n"
    "v_add_f32 v184, v184, v192\n"
    "v_mfma_f32_16x16x32_bf16 v[128:131], v[112:115], a[56:59], v[128:131]\n"
    "v_cvt_pk_bf16_f32 v185, v184, v184\n"
    "v_lshlrev_b32 v185, 16, v185\n"
    "s_waitcnt lgkmcnt(6)\n"
    "v_mfma_f32_16x16x32_bf16 v[124:127], a[136:139], a[136:139], v[124:127]\n"
    "v_sub_f32 v185, v184, v185\n"
    "v_cvt_pk_bf16_f32 v196, v184, v185\n"
    "v_mfma_f32_16x16x32_bf16 v[132:135], v[112:115], a[136:139], v[132:135]\n"
    "v_mov_b32 v197, v196\n"
    "v_add_f32 v186, v186, v190\n"
    "s_waitcnt lgkmcnt(5)\n"
    "v_mfma_f32_16x16x32_bf16 v[120:123], a[60:63], a[60:63], v[120:123]\n"
    "v_add_f32 v186, v186, v194\n"
    "v_cvt_pk_bf16_f32 v187, v186, v186\n"
    "v_mfma_f32_16x16x32_bf16 v[128:131], v[112:115], a[60:63], v[128:131]\n"
    "v_lshlrev_b32 v187, 16, v187\n"
    "v_sub_f32 v187, v186, v187\n"
    "s_waitcnt lgkmcnt(4)\n"
    "v_mfma_f32_16x16x32_bf16 v[124:127], a[140:143], a[140:143], v[124:127]\n"
    "v_cvt_pk_bf16_f32 v198, v186, v187\n"
    "v_mov_b32 v199, v198\n"
    "v_mfma_f32_16x16x32_bf16 v[132:135], v[112:115], a[140:143], v[132:135]\n"
    "v_add_u32 v188, 153600, v166\n"
    "ds_write_b128 v188, v[196:199]\n"
    "buffer_store_dwordx4 v[116:119], v0, s[4:7], s42 offen offset:2048 sc1\n"
    "s_waitcnt vmcnt(8)\n"
    "ds_read_b128 a[72:75], v110 offset:16384\n"
    "ds_read_b128 a[152:155], v110 offset:18432\n"
    "ds_read_b128 a[76:79], v111 offset:16384\n"
    "ds_read_b128 a[156:159], v111 offset:18432\n"
    "s_waitcnt lgkmcnt(8)\n"
    "v_mfma_f32_16x16x32_bf16 v[120:123], a[64:67], a[64:67], v[120:123]\n"
    "v_add_u32 v169, 20480, v0\n"
    "s_add_u32 m0, s82, 20480\n"
    "v_mfma_f32_16x16x32_bf16 v[128:131], v[112:115], a[64:67], v[128:131]\n"
    "buffer_load_dwordx4 v169, s[4:7], s52 offen offset:0 sc1 lds\n"
    "s_waitcnt lgkmcnt(7)\n"
    "v_mfma_f32_16x16x32_bf16 v[124:127], a[144:147], a[144:147], v[124:127]\n"
    "buffer_load_dwordx4 v169, s[4:7], s53 offen offset:1024 sc1 lds\n"
    "v_mfma_f32_16x16x32_bf16 v[132:135], v[112:115], a[144:147], v[132:135]\n"
    "s_waitcnt lgkmcnt(6)\n"
    "v_mfma_f32_16x16x32_bf16 v[120:123], a[68:71], a[68:71], v[120:123]\n"
    "buffer_load_dwordx4 v169, s[4:7], s54 offen offset:2048 sc1 lds\n"
    "v_mfma_f32_16x16x32_bf16 v[128:131], v[112:115], a[68:71], v[128:131]\n"
    "buffer_load_dwordx4 v169, s[4:7], s55 offen offset:3072 sc1 lds\n"
    "s_add_u32 m0, m0, 4096\n"
    "s_waitcnt lgkmcnt(5)\n"
    "v_mfma_f32_16x16x32_bf16 v[124:127], a[148:151], a[148:151], v[124:127]\n"
    "buffer_load_dwordx4 v169, s[4:7], s56 offen offset:0 sc1 lds\n"
    "v_mfma_f32_16x16x32_bf16 v[132:135], v[112:115], a[148:151], v[132:135]\n"
    "buffer_store_dwordx4 v[116:119], v0, s[4:7], s42 offen offset:3072 sc1\n"
    "s_add_u32 s42, s42, 4194304\n"
    "s_waitcnt lgkmcnt(3)\n"
    "v_mfma_f32_16x16x32_bf16 v[120:123], a[72:75], a[72:75], v[120:123]\n"
    "v_mfma_f32_16x16x32_bf16 v[128:131], v[112:115], a[72:75], v[128:131]\n"
    "s_waitcnt lgkmcnt(2)\n"
    "v_mfma_f32_16x16x32_bf16 v[124:127], a[152:155], a[152:155], v[124:127]\n"
    "v_mfma_f32_16x16x32_bf16 v[132:135], v[112:115], a[152:155], v[132:135]\n"
    "s_waitcnt lgkmcnt(1)\n"
    "v_mfma_f32_16x16x32_bf16 v[120:123], a[76:79], a[76:79], v[120:123]\n"
    "v_mfma_f32_16x16x32_bf16 v[128:131], v[112:115], a[76:79], v[128:131]\n"
    "s_waitcnt lgkmcnt(0)\n"
    "v_mfma_f32_16x16x32_bf16 v[124:127], a[156:159], a[156:159], v[124:127]\n"
    "v_mfma_f32_16x16x32_bf16 v[132:135], v[112:115], a[156:159], v[132:135]\n"
    "s_mov_b32 s20, 0x22222222\n"
    "s_mov_b32 s21, 0x22222222\n"
    "s_mov_b32 s24, 0x44444444\n"
    "s_mov_b32 s25, 0x44444444\n"
    "s_mov_b32 s26, 0x88888888\n"
    "s_mov_b32 s27, 0x88888888\n"
    "s_lshl_b32 s31, %2, 8\n"
    "s_add_u32 s31, s31, 157696\n"
    "v_lshl_add_u32 v160, v4, 2, s31\n"
    "v_lshl_add_u32 v161, v5, 4, s31\n"
    "s_mov_b32 s32, 0x3acccccd\n"
    "s_mov_b32 s33, 0x3727c5ac\n"
    "v_cndmask_b32 v156, v120, v121, s[20:21]\n"
    "v_cndmask_b32 v156, v156, v122, s[24:25]\n"
    "v_cndmask_b32 v156, v156, v123, s[26:27]\n"
    "v_mul_f32 v128, -v128, s32\n"
    "v_mul_f32 v157, v128, v128\n"
    "v_fma_f32 v156, v156, s32, -v157\n"
    "v_max_f32 v156, 0, v156\n"
    "v_add_f32 v156, s33, v156\n"
    "v_rsq_f32 v157, v156\n"
    "v_cndmask_b32 v158, v124, v125, s[20:21]\n"
    "v_cndmask_b32 v158, v158, v126, s[24:25]\n"
    "v_cndmask_b32 v158, v158, v127, s[26:27]\n"
    "v_mul_f32 v132, -v132, s32\n"
    "v_mul_f32 v159, v132, v132\n"
    "v_fma_f32 v158, v158, s32, -v159\n"
    "v_max_f32 v158, 0, v158\n"
    "v_add_f32 v158, s33, v158\n"
    "v_rsq_f32 v159, v158\n"
    "s_mov_b32 exec_lo, 0x00f0000f\n"
    "s_mov_b32 exec_hi, 0xf0000f00\n"
    "ds_write_b32 v160, v157 offset:0\n"
    "ds_write_b32 v160, v159 offset:64\n"
    "s_mov_b64 exec, -1\n"
    "ds_read_b128 v[140:143], v161 offset:0\n"
    "ds_read_b32 v148, v160 offset:0\n"
    "ds_read_b128 v[144:147], v161 offset:64\n"
    "ds_read_b32 v149, v160 offset:64\n"
    "v_cvt_pk_bf16_f32 v152, v128, v128\n"
    "v_lshlrev_b32 v129, 16, v152\n"
    "v_sub_f32 v129, v128, v129\n"
    "v_cvt_pk_bf16_f32 v153, v129, v129\n"
    "v_cvt_pk_bf16_f32 v154, v132, v132\n"
    "v_lshlrev_b32 v133, 16, v154\n"
    "v_sub_f32 v133, v132, v133\n"
    "v_cvt_pk_bf16_f32 v155, v133, v133\n"
    "v_mov_b32 v128, 0\n"
    "v_mov_b32 v129, 0\n"
    "v_mov_b32 v130, 0\n"
    "v_mov_b32 v131, 0\n"
    "v_add_u32 v132, 159744, v166\n"
    "ds_write_b128 v132, v[128:131]\n"
    "v_lshlrev_b32 v204, 2, v5\n"
    "v_cmp_eq_u32 vcc, v204, v4\n"
    "s_nop 1\n"
    "v_cndmask_b32 v108, 0, v112, vcc\n"
    "v_cndmask_b32 v109, 0, v112, vcc\n"
    "v_cndmask_b32 v110, 0, v112, vcc\n"
    "v_cndmask_b32 v111, 0, v112, vcc\n"
    "s_waitcnt vmcnt(7)\n"
    "s_waitcnt lgkmcnt(0)\n"
    "s_barrier\n"
    "ds_read_b128 a[160:163], v0 offset:0\n"
    "ds_read_b128 a[164:167], v0 offset:1024\n"
    "ds_read_b128 a[168:171], v0 offset:2048\n"
    "ds_read_b128 a[172:175], v0 offset:3072\n"
    "ds_read_b128 a[176:179], v0 offset:4096\n"
    "ds_read_b128 a[180:183], v0 offset:5120\n"
    "ds_read_b128 a[184:187], v0 offset:6144\n"
    "ds_read_b128 a[188:191], v0 offset:7168\n"
    "v_add_u32 v240, 4096, v0\n"
    "s_waitcnt lgkmcnt(4)\n"
    "v_mfma_f32_16x16x32_bf16 v[16:19], a[0:3], a[160:163], 0\n"
    "v_add_u32 v169, 40960, v0\n"
    "s_add_u32 m0, s82, 40960\n"
    "v_mfma_f32_16x16x32_bf16 v[20:23], a[80:83], a[160:163], 0\n"
    "ds_read_b128 a[192:195], v0 offset:8192\n"
    "ds_read_b64 a[240:241], v167 offset:320\n"
    "v_mfma_f32_16x16x32_bf16 v[16:19], a[4:7], a[164:167], v[16:19]\n"
    "ds_read_b64 a[242:243], v167 offset:448\n"
    "ds_read_b64 a[244:245], v167 offset:576\n"
    "v_mfma_f32_16x16x32_bf16 v[20:23], a[84:87], a[164:167], v[20:23]\n"
    "ds_read_b128 a[196:199], v0 offset:9216\n"
    "buffer_load_dwordx4 v169, s[4:7], s52 offen offset:0 sc1 lds\n"
    "v_mfma_f32_16x16x32_bf16 v[16:19], a[8:11], a[168:171], v[16:19]\n"
    "ds_read_b64 a[246:247], v167 offset:704\n"
    "ds_read_b64 a[248:249], v167 offset:832\n"
    "v_mfma_f32_16x16x32_bf16 v[20:23], a[88:91], a[168:171], v[20:23]\n"
    "ds_read_b128 a[160:163], v0 offset:10240\n"
    "ds_read_b64 a[250:251], v167 offset:0\n"
    "v_mfma_f32_16x16x32_bf16 v[16:19], a[12:15], a[172:175], v[16:19]\n"
    "ds_read_b64 a[252:253], v167 offset:128\n"
    "ds_read_b64 a[254:255], v167 offset:256\n"
    "v_mfma_f32_16x16x32_bf16 v[20:23], a[92:95], a[172:175], v[20:23]\n"
    "ds_read_b128 a[164:167], v0 offset:11264\n"
    "buffer_load_dwordx4 v169, s[4:7], s53 offen offset:1024 sc1 lds\n"
    "s_waitcnt lgkmcnt(12)\n"
    "v_mfma_f32_16x16x32_bf16 v[16:19], a[16:19], a[176:179], v[16:19]\n"
    "v_mfma_f32_16x16x32_bf16 v[20:23], a[96:99], a[176:179], v[20:23]\n"
    "ds_read_b128 a[168:171], v0 offset:12288\n"
    "v_mfma_f32_16x16x32_bf16 v[16:19], a[20:23], a[180:183], v[16:19]\n"
    "v_mfma_f32_16x16x32_bf16 v[20:23], a[100:103], a[180:183], v[20:23]\n"
    "ds_read_b128 a[172:175], v0 offset:13312\n"
    "buffer_load_dwordx4 v169, s[4:7], s54 offen offset:2048 sc1 lds\n"
    "v_mfma_f32_16x16x32_bf16 v[16:19], a[24:27], a[184:187], v[16:19]\n"
    "v_mfma_f32_16x16x32_bf16 v[20:23], a[104:107], a[184:187], v[20:23]\n"
    "ds_read_b128 a[176:179], v0 offset:14336\n"
    "v_mfma_f32_16x16x32_bf16 v[16:19], a[28:31], a[188:191], v[16:19]\n"
    "buffer_load_dwordx4 v169, s[4:7], s55 offen offset:3072 sc1 lds\n"
    "s_add_u32 m0, m0, 4096\n"
    "v_mfma_f32_16x16x32_bf16 v[20:23], a[108:111], a[188:191], v[20:23]\n"
    "ds_read_b128 a[180:183], v0 offset:15360\n"
    "s_waitcnt lgkmcnt(12)\n"
    "v_mfma_f32_16x16x32_bf16 v[16:19], a[32:35], a[192:195], v[16:19]\n"
    "v_mfma_f32_16x16x32_bf16 v[20:23], a[112:115], a[192:195], v[20:23]\n"
    "ds_read_b128 a[184:187], v0 offset:16384\n"
    "s_waitcnt lgkmcnt(9)\n"
    "v_mfma_f32_16x16x32_bf16 v[16:19], a[36:39], a[196:199], v[16:19]\n"
    "buffer_load_dwordx4 v169, s[4:7], s56 offen offset:0 sc1 lds\n"
    "v_mfma_f32_16x16x32_bf16 v[20:23], a[116:119], a[196:199], v[20:23]\n"
    "ds_read_b128 a[188:191], v0 offset:17408\n"
    "v_mfma_f32_16x16x32_bf16 v[16:19], a[40:43], a[160:163], v[16:19]\n"
    "v_mfma_f32_16x16x32_bf16 v[20:23], a[120:123], a[160:163], v[20:23]\n"
    "ds_read_b128 a[192:195], v0 offset:18432\n"
    "s_waitcnt lgkmcnt(4)\n"
    "v_mfma_f32_16x16x32_bf16 v[16:19], a[44:47], a[164:167], v[16:19]\n"
    "v_add_u32 v169, 61440, v0\n"
    "s_add_u32 m0, s82, 61440\n"
    "v_mfma_f32_16x16x32_bf16 v[20:23], a[124:127], a[164:167], v[20:23]\n"
    "ds_read_b128 a[196:199], v0 offset:19456\n"
    "v_mfma_f32_16x16x32_bf16 v[16:19], a[48:51], a[168:171], v[16:19]\n"
    "v_mfma_f32_16x16x32_bf16 v[20:23], a[128:131], a[168:171], v[20:23]\n"
    "s_waitcnt vmcnt(5)\n"
    "s_barrier\n"
    "buffer_store_dword v9, v6, s[4:7], s38 offen offset:0 sc1\n"
    "ds_read_b128 a[160:163], v0 offset:20480\n"
    "buffer_load_dwordx4 v169, s[4:7], s52 offen offset:0 sc1 lds\n"
    "v_mfma_f32_16x16x32_bf16 v[16:19], a[52:55], a[172:175], v[16:19]\n"
    "v_mfma_f32_16x16x32_bf16 v[20:23], a[132:135], a[172:175], v[20:23]\n"
    "ds_read_b128 a[164:167], v0 offset:21504\n"
    "v_mfma_f32_16x16x32_bf16 v[16:19], a[56:59], a[176:179], v[16:19]\n"
    "v_mfma_f32_16x16x16_bf16 v[24:27], v[152:153], a[242:243], 0\n"
    "v_mfma_f32_16x16x32_bf16 v[20:23], a[136:139], a[176:179], v[20:23]\n"
    "v_mfma_f32_16x16x16_bf16 v[28:31], v[154:155], a[242:243], 0\n"
    "ds_read_b128 a[168:171], v0 offset:22528\n"
    "buffer_load_dwordx4 v169, s[4:7], s53 offen offset:1024 sc1 lds\n"
    "s_waitcnt lgkmcnt(4)\n"
    "v_mfma_f32_16x16x32_bf16 v[16:19], a[60:63], a[180:183], v[16:19]\n"
    "v_mfma_f32_16x16x32_bf16 v[20:23], a[140:143], a[180:183], v[20:23]\n"
    "ds_read_b128 a[172:175], v0 offset:23552\n"
    "v_mfma_f32_16x16x32_bf16 v[16:19], a[64:67], a[184:187], v[16:19]\n"
    "v_mfma_f32_16x16x32_bf16 v[20:23], a[144:147], a[184:187], v[20:23]\n"
    "ds_read_b128 a[176:179], v0 offset:24576\n"
    "buffer_load_dwordx4 v169, s[4:7], s54 offen offset:2048 sc1 lds\n"
    "v_mfma_f32_16x16x32_bf16 v[16:19], a[68:71], a[188:191], v[16:19]\n"
    "v_mfma_f32_16x16x32_bf16 v[20:23], a[148:151], a[188:191], v[20:23]\n"
    "ds_read_b128 a[180:183], v0 offset:25600\n"
    "v_mfma_f32_16x16x32_bf16 v[16:19], a[72:75], a[192:195], v[16:19]\n"
    "buffer_load_dwordx4 v169, s[4:7], s55 offen offset:3072 sc1 lds\n"
    "s_add_u32 m0, m0, 4096\n"
    "v_mfma_f32_16x16x32_bf16 v[20:23], a[152:155], a[192:195], v[20:23]\n"
    "ds_read_b128 a[184:187], v0 offset:26624\n"
    "s_waitcnt lgkmcnt(4)\n"
    "v_mfma_f32_16x16x32_bf16 v[16:19], a[76:79], a[196:199], v[16:19]\n"
    "v_mfma_f32_16x16x32_bf16 v[20:23], a[156:159], a[196:199], v[20:23]\n"
    "ds_read_b128 a[188:191], v0 offset:27648\n"
    "v_mfma_f32_16x16x32_bf16 v[24:27], a[0:3], a[160:163], v[24:27]\n"
    "buffer_load_dwordx4 v169, s[4:7], s56 offen offset:0 sc1 lds\n"
    "v_mfma_f32_16x16x32_bf16 v[28:31], a[80:83], a[160:163], v[28:31]\n"
    "ds_read_b128 a[192:195], v0 offset:28672\n"
    "v_mfma_f32_16x16x32_bf16 v[24:27], a[4:7], a[164:167], v[24:27]\n"
    "v_mfma_f32_16x16x32_bf16 v[28:31], a[84:87], a[164:167], v[28:31]\n"
    "ds_read_b128 a[196:199], v0 offset:29696\n"
    "v_mfma_f32_16x16x32_bf16 v[24:27], a[8:11], a[168:171], v[24:27]\n"
    "v_mfma_f32_16x16x16_bf16 v[16:19], v[152:153], a[240:241], v[16:19]\n"
    "v_add_u32 v169, 81920, v0\n"
    "s_add_u32 m0, s82, 81920\n"
    "v_mfma_f32_16x16x32_bf16 v[28:31], a[88:91], a[168:171], v[28:31]\n"
    "v_mfma_f32_16x16x16_bf16 v[20:23], v[154:155], a[240:241], v[20:23]\n"
    "ds_read_b128 a[160:163], v0 offset:30720\n"
    "s_waitcnt lgkmcnt(4)\n"
    "v_mfma_f32_16x16x32_bf16 v[24:27], a[12:15], a[172:175], v[24:27]\n"
    "v_mfma_f32_16x16x32_bf16 v[28:31], a[92:95], a[172:175], v[28:31]\n"
    "ds_read_b128 a[164:167], v0 offset:31744\n"
    "buffer_load_dwordx4 v169, s[4:7], s52 offen offset:0 sc1 lds\n"
    "v_mfma_f32_16x16x32_bf16 v[24:27], a[16:19], a[176:179], v[24:27]\n"
    "v_fma_f32 v16, v16, v140, v176\n"
    "v_fma_f32 v17, v17, v141, v176\n"
    "v_mfma_f32_16x16x32_bf16 v[28:31], a[96:99], a[176:179], v[28:31]\n"
    "ds_read_b128 a[168:171], v0 offset:32768\n"
    "v_fma_f32 v18, v18, v142, v176\n"
    "v_mfma_f32_16x16x32_bf16 v[24:27], a[20:23], a[180:183], v[24:27]\n"
    "v_fma_f32 v19, v19, v143, v176\n"
    "v_fma_f32 v20, v20, v144, v176\n"
    "v_mfma_f32_16x16x32_bf16 v[28:31], a[100:103], a[180:183], v[28:31]\n"
    "ds_read_b128 a[172:175], v0 offset:33792\n"
    "buffer_load_dwordx4 v169, s[4:7], s53 offen offset:1024 sc1 lds\n"
    "v_mfma_f32_16x16x32_bf16 v[24:27], a[24:27], a[184:187], v[24:27]\n"
    "v_fma_f32 v21, v21, v145, v176\n"
    "v_fma_f32 v22, v22, v146, v176\n"
    "v_mfma_f32_16x16x32_bf16 v[28:31], a[104:107], a[184:187], v[28:31]\n"
    "ds_read_b128 a[176:179], v0 offset:34816\n"
    "v_fma_f32 v23, v23, v147, v176\n"
    "s_waitcnt lgkmcnt(4)\n"
    "v_mfma_f32_16x16x32_bf16 v[24:27], a[28:31], a[188:191], v[24:27]\n"
    "v_fma_f32 v120, v16, v172, v174\n"
    "v_fma_f32 v121, v17, v172, v174\n"
    "v_mfma_f32_16x16x32_bf16 v[28:31], a[108:111], a[188:191], v[28:31]\n"
    "ds_read_b128 a[180:183], v0 offset:35840\n"
    "buffer_load_dwordx4 v169, s[4:7], s54 offen offset:2048 sc1 lds\n"
    "v_mfma_f32_16x16x32_bf16 v[24:27], a[32:35], a[192:195], v[24:27]\n"
    "v_fma_f32 v122, v18, v172, v174\n"
    "v_fma_f32 v123, v19, v172, v174\n"
    "v_mfma_f32_16x16x32_bf16 v[28:31], a[112:115], a[192:195], v[28:31]\n"
    "ds_read_b128 a[184:187], v0 offset:36864\n"
    "v_fma_f32 v124, v20, v172, v174\n"
    "v_mfma_f32_16x16x32_bf16 v[24:27], a[36:39], a[196:199], v[24:27]\n"
    "buffer_load_dwordx4 v169, s[4:7], s55 offen offset:3072 sc1 lds\n"
    "s_add_u32 m0, m0, 4096\n"
    "v_mfma_f32_16x16x32_bf16 v[28:31], a[116:119], a[196:199], v[28:31]\n"
    "ds_read_b128 a[188:191], v0 offset:37888\n"
    "v_fma_f32 v125, v21, v172, v174\n"
    "v_mfma_f32_16x16x32_bf16 v[24:27], a[40:43], a[160:163], v[24:27]\n"
    "v_fma_f32 v126, v22, v172, v174\n"
    "v_fma_f32 v127, v23, v172, v174\n"
    "v_mfma_f32_16x16x32_bf16 v[28:31], a[120:123], a[160:163], v[28:31]\n"
    "ds_read_b128 a[192:195], v0 offset:38912\n"
    "s_waitcnt lgkmcnt(4)\n"
    "v_mfma_f32_16x16x32_bf16 v[24:27], a[44:47], a[164:167], v[24:27]\n"
    "buffer_load_dwordx4 v169, s[4:7], s56 offen offset:0 sc1 lds\n"
    "v_mfma_f32_16x16x32_bf16 v[28:31], a[124:127], a[164:167], v[28:31]\n"
    "ds_read_b128 a[196:199], v0 offset:39936\n"
    "v_mfma_f32_16x16x32_bf16 v[24:27], a[48:51], a[168:171], v[24:27]\n"
    "v_exp_f32 v120, v120 clamp\n"
    "v_max_f32 v16, v176, v16\n"
    "v_mfma_f32_16x16x32_bf16 v[28:31], a[128:131], a[168:171], v[28:31]\n"
    "s_waitcnt vmcnt(11)\n"
    "s_barrier\n"
    "ds_read_b128 a[160:163], v0 offset:40960\n"
    "v_exp_f32 v121, v121 clamp\n"
    "v_mfma_f32_16x16x32_bf16 v[24:27], a[52:55], a[172:175], v[24:27]\n"
    "s_add_u32 m0, s82, 0\n"
    "v_max_f32 v17, v176, v17\n"
    "v_mfma_f32_16x16x32_bf16 v[28:31], a[132:135], a[172:175], v[28:31]\n"
    "ds_read_b128 a[164:167], v0 offset:41984\n"
    "buffer_load_dwordx4 v99, s[16:19], s60 offen lds\n"
    "v_exp_f32 v122, v122 clamp\n"
    "v_mfma_f32_16x16x32_bf16 v[24:27], a[56:59], a[176:179], v[24:27]\n"
    "v_mfma_f32_16x16x16_bf16 v[32:35], v[152:153], a[244:245], 0\n"
    "v_max_f32 v18, v176, v18\n"
    "v_mfma_f32_16x16x32_bf16 v[28:31], a[136:139], a[176:179], v[28:31]\n"
    "v_mfma_f32_16x16x16_bf16 v[36:39], v[154:155], a[244:245], 0\n"
    "ds_read_b128 a[168:171], v0 offset:43008\n"
    "v_exp_f32 v123, v123 clamp\n"
    "v_max_f32 v19, v176, v19\n"
    "s_waitcnt lgkmcnt(4)\n"
    "v_mfma_f32_16x16x32_bf16 v[24:27], a[60:63], a[180:183], v[24:27]\n"
    "v_exp_f32 v124, v124 clamp\n"
    "v_mfma_f32_16x16x32_bf16 v[28:31], a[140:143], a[180:183], v[28:31]\n"
    "ds_read_b128 a[172:175], v0 offset:44032\n"
    "s_add_u32 m0, s82, 1024\n"
    "v_max_f32 v20, v176, v20\n"
    "v_mfma_f32_16x16x32_bf16 v[24:27], a[64:67], a[184:187], v[24:27]\n"
    "buffer_load_dwordx4 v99, s[16:19], s61 offen lds\n"
    "v_exp_f32 v125, v125 clamp\n"
    "v_mfma_f32_16x16x32_bf16 v[28:31], a[144:147], a[184:187], v[28:31]\n"
    "ds_read_b128 a[176:179], v0 offset:45056\n"
    "v_max_f32 v21, v176, v21\n"
    "v_exp_f32 v126, v126 clamp\n"
    "v_mfma_f32_16x16x32_bf16 v[24:27], a[68:71], a[188:191], v[24:27]\n"
    "v_max_f32 v22, v176, v22\n"
    "v_mfma_f32_16x16x32_bf16 v[28:31], a[148:151], a[188:191], v[28:31]\n"
    "ds_read_b128 a[180:183], v0 offset:46080\n"
    "v_exp_f32 v127, v127 clamp\n"
    "v_mfma_f32_16x16x32_bf16 v[24:27], a[72:75], a[192:195], v[24:27]\n"
    "s_add_u32 m0, s82, 2048\n"
    "v_max_f32 v23, v176, v23\n"
    "v_mfma_f32_16x16x32_bf16 v[28:31], a[152:155], a[192:195], v[28:31]\n"
    "ds_read_b128 a[184:187], v0 offset:47104\n"
    "buffer_load_dwordx4 v99, s[16:19], s62 offen lds\n"
    "v_add_f32 v16, v16, v120\n"
    "s_waitcnt lgkmcnt(4)\n"
    "v_mfma_f32_16x16x32_bf16 v[24:27], a[76:79], a[196:199], v[24:27]\n"
    "v_add_f32 v17, v17, v121\n"
    "v_add_f32 v18, v18, v122\n"
    "v_mfma_f32_16x16x32_bf16 v[28:31], a[156:159], a[196:199], v[28:31]\n"
    "ds_read_b128 a[188:191], v0 offset:48128\n"
    "v_add_f32 v19, v19, v123\n"
    "v_mfma_f32_16x16x32_bf16 v[32:35], a[0:3], a[160:163], v[32:35]\n"
    "v_add_f32 v20, v20, v124\n"
    "v_add_f32 v21, v21, v125\n"
    "v_mfma_f32_16x16x32_bf16 v[36:39], a[80:83], a[160:163], v[36:39]\n"
    "ds_read_b128 a[192:195], v0 offset:49152\n"
    "s_add_u32 m0, s82, 3072\n"
    "v_mfma_f32_16x16x32_bf16 v[32:35], a[4:7], a[164:167], v[32:35]\n"
    "buffer_load_dwordx4 v99, s[16:19], s63 offen lds\n"
    "v_add_f32 v22, v22, v126\n"
    "v_add_f32 v23, v23, v127\n"
    "v_mfma_f32_16x16x32_bf16 v[36:39], a[84:87], a[164:167], v[36:39]\n"
    "ds_read_b128 a[196:199], v0 offset:50176\n"
    "v_cvt_pk_bf16_f32 v180, v16, v17\n"
    "v_cvt_pk_bf16_f32 v181, v18, v19\n"
    "v_cvt_pk_bf16_f32 v182, v20, v21\n"
    "v_cvt_pk_bf16_f32 v183, v22, v23\n"
    "v_mfma_f32_16x16x32_bf16 v[32:35], a[8:11], a[168:171], v[32:35]\n"
    "v_fma_f32 v24, v24, v140, v176\n"
    "v_fma_f32 v25, v25, v141, v176\n"
    "v_mfma_f32_16x16x32_bf16 v[36:39], a[88:91], a[168:171], v[36:39]\n"
    "ds_read_b128 a[160:163], v0 offset:51200\n"
    "v_fma_f32 v26, v26, v142, v176\n"
    "s_waitcnt lgkmcnt(4)\n"
    "v_mfma_f32_16x16x32_bf16 v[32:35], a[12:15], a[172:175], v[32:35]\n"
    "s_add_u32 m0, s82, 4096\n"
    "v_fma_f32 v27, v27, v143, v176\n"
    "v_mfma_f32_16x16x32_bf16 v[36:39], a[92:95], a[172:175], v[36:39]\n"
    "ds_read_b128 a[164:167], v0 offset:52224\n"
    "buffer_load_dwordx4 v99, s[16:19], s64 offen lds\n"
    "v_mfma_f32_16x16x32_bf16 v[32:35], a[16:19], a[176:179], v[32:35]\n"
    "v_add_u32 v169, 102400, v0\n"
    "s_add_u32 m0, s82, 102400\n"
    "v_mfma_f32_16x16x32_bf16 v[36:39], a[96:99], a[176:179], v[36:39]\n"
    "ds_read_b128 a[168:171], v0 offset:53248\n"
    "v_fma_f32 v28, v28, v144, v176\n"
    "v_mfma_f32_16x16x32_bf16 v[32:35], a[20:23], a[180:183], v[32:35]\n"
    "v_fma_f32 v29, v29, v145, v176\n"
    "v_fma_f32 v30, v30, v146, v176\n"
    "v_mfma_f32_16x16x32_bf16 v[36:39], a[100:103], a[180:183], v[36:39]\n"
    "ds_read_b128 a[172:175], v0 offset:54272\n"
    "buffer_load_dwordx4 v169, s[4:7], s52 offen offset:0 sc1 lds\n"
    "v_mfma_f32_16x16x32_bf16 v[32:35], a[24:27], a[184:187], v[32:35]\n"
    "v_fma_f32 v31, v31, v147, v176\n"
    "v_fma_f32 v120, v24, v172, v174\n"
    "v_mfma_f32_16x16x32_bf16 v[36:39], a[104:107], a[184:187], v[36:39]\n"
    "ds_read_b128 a[176:179], v0 offset:55296\n"
    "v_fma_f32 v121, v25, v172, v174\n"
    "s_waitcnt lgkmcnt(4)\n"
    "v_mfma_f32_16x16x32_bf16 v[32:35], a[28:31], a[188:191], v[32:35]\n"
    "v_fma_f32 v122, v26, v172, v174\n"
    "v_fma_f32 v123, v27, v172, v174\n"
    "v_mfma_f32_16x16x32_bf16 v[36:39], a[108:111], a[188:191], v[36:39]\n"
    "ds_read_b128 a[180:183], v0 offset:56320\n"
    "buffer_load_dwordx4 v169, s[4:7], s53 offen offset:1024 sc1 lds\n"
    "v_mfma_f32_16x16x32_bf16 v[32:35], a[32:35], a[192:195], v[32:35]\n"
    "v_fma_f32 v124, v28, v172, v174\n"
    "v_fma_f32 v125, v29, v172, v174\n"
    "v_mfma_f32_16x16x32_bf16 v[36:39], a[112:115], a[192:195], v[36:39]\n"
    "ds_read_b128 a[184:187], v0 offset:57344\n"
    "v_fma_f32 v126, v30, v172, v174\n"
    "v_mfma_f32_16x16x32_bf16 v[32:35], a[36:39], a[196:199], v[32:35]\n"
    "v_fma_f32 v127, v31, v172, v174\n"
    "v_mfma_f32_16x16x32_bf16 v[36:39], a[116:119], a[196:199], v[36:39]\n"
    "ds_read_b128 a[188:191], v0 offset:58368\n"
    "buffer_load_dwordx4 v169, s[4:7], s54 offen offset:2048 sc1 lds\n"
    "v_mfma_f32_16x16x32_bf16 v[32:35], a[40:43], a[160:163], v[32:35]\n"
    "v_exp_f32 v120, v120 clamp\n"
    "v_mfma_f32_16x16x32_bf16 v[36:39], a[120:123], a[160:163], v[36:39]\n"
    "ds_read_b128 a[192:195], v0 offset:59392\n"
    "v_max_f32 v24, v176, v24\n"
    "s_waitcnt lgkmcnt(4)\n"
    "v_mfma_f32_16x16x32_bf16 v[32:35], a[44:47], a[164:167], v[32:35]\n"
    "buffer_load_dwordx4 v169, s[4:7], s55 offen offset:3072 sc1 lds\n"
    "s_add_u32 m0, m0, 4096\n"
    "v_exp_f32 v121, v121 clamp\n"
    "v_max_f32 v25, v176, v25\n"
    "v_mfma_f32_16x16x32_bf16 v[36:39], a[124:127], a[164:167], v[36:39]\n"
    "ds_read_b128 a[196:199], v0 offset:60416\n"
    "v_exp_f32 v122, v122 clamp\n"
    "v_mfma_f32_16x16x32_bf16 v[32:35], a[48:51], a[168:171], v[32:35]\n"
    "v_max_f32 v26, v176, v26\n"
    "v_mfma_f32_16x16x32_bf16 v[36:39], a[128:131], a[168:171], v[36:39]\n"
    "s_waitcnt vmcnt(14)\n"
    "s_barrier\n"
    "ds_read_b128 a[160:163], v0 offset:61440\n"
    "v_exp_f32 v123, v123 clamp\n"
    "v_mfma_f32_16x16x32_bf16 v[32:35], a[52:55], a[172:175], v[32:35]\n"
    "buffer_load_dwordx4 v169, s[4:7], s56 offen offset:0 sc1 lds\n"
    "v_max_f32 v27, v176, v27\n"
    "v_mfma_f32_16x16x32_bf16 v[36:39], a[132:135], a[172:175], v[36:39]\n"
    "ds_read_b128 a[164:167], v0 offset:62464\n"
    "v_exp_f32 v124, v124 clamp\n"
    "v_mfma_f32_16x16x32_bf16 v[32:35], a[56:59], a[176:179], v[32:35]\n"
    "v_mfma_f32_16x16x16_bf16 v[40:43], v[152:153], a[246:247], 0\n"
    "v_max_f32 v28, v176, v28\n"
    "v_exp_f32 v125, v125 clamp\n"
    "v_mfma_f32_16x16x32_bf16 v[36:39], a[136:139], a[176:179], v[36:39]\n"
    "v_mfma_f32_16x16x16_bf16 v[44:47], v[154:155], a[246:247], 0\n"
    "ds_read_b128 a[168:171], v0 offset:63488\n"
    "v_max_f32 v29, v176, v29\n"
    "s_waitcnt lgkmcnt(4)\n"
    "v_mfma_f32_16x16x32_bf16 v[32:35], a[60:63], a[180:183], v[32:35]\n"
    "s_add_u32 m0, s49, 143360\n"
    "v_exp_f32 v126, v126 clamp\n"
    "v_mfma_f32_16x16x32_bf16 v[36:39], a[140:143], a[180:183], v[36:39]\n"
    "ds_read_b128 a[172:175], v0 offset:64512\n"
    "v_max_f32 v30, v176, v30\n"
    "v_mfma_f32_16x16x32_bf16 v[32:35], a[64:67], a[184:187], v[32:35]\n"
    "buffer_load_dwordx4 v0, s[4:7], s57 offen sc1 lds\n"
    "v_exp_f32 v127, v127 clamp\n"
    "v_mfma_f32_16x16x32_bf16 v[36:39], a[144:147], a[184:187], v[36:39]\n"
    "ds_read_b128 a[176:179], v1 offset:0\n"
    "v_max_f32 v31, v176, v31\n"
    "v_add_f32 v24, v24, v120\n"
    "v_mfma_f32_16x16x32_bf16 v[32:35], a[68:71], a[188:191], v[32:35]\n"
    "s_add_u32 m0, s49, 147456\n"
    "v_add_f32 v25, v25, v121\n"
    "v_mfma_f32_16x16x32_bf16 v[36:39], a[148:151], a[188:191], v[36:39]\n"
    "ds_read_b128 a[180:183], v1 offset:1024\n"
    "v_add_f32 v26, v26, v122\n"
    "v_mfma_f32_16x16x32_bf16 v[32:35], a[72:75], a[192:195], v[32:35]\n"
    "buffer_load_dwordx4 v0, s[4:7], s58 offen sc1 lds\n"
    "v_add_f32 v27, v27, v123\n"
    "v_mfma_f32_16x16x32_bf16 v[36:39], a[152:155], a[192:195], v[36:39]\n"
    "ds_read_b128 a[184:187], v1 offset:2048\n"
    "v_add_f32 v28, v28, v124\n"
    "s_waitcnt lgkmcnt(4)\n"
    "v_mfma_f32_16x16x32_bf16 v[32:35], a[76:79], a[196:199], v[32:35]\n"
    "s_add_u32 m0, s49, 151552\n"
    "v_add_f32 v29, v29, v125\n"
    "v_add_f32 v30, v30, v126\n"
    "v_mfma_f32_16x16x32_bf16 v[36:39], a[156:159], a[196:199], v[36:39]\n"
    "ds_read_b128 a[188:191], v1 offset:3072\n"
    "v_add_f32 v31, v31, v127\n"
    "v_mfma_f32_16x16x32_bf16 v[40:43], a[0:3], a[160:163], v[40:43]\n"
    "buffer_load_dwordx4 v0, s[4:7], s59 offen sc1 lds\n"
    "buffer_load_dwordx4 v[244:247], v10, s[12:15], s74 offen\n"
    "v_cvt_pk_bf16_f32 v184, v24, v25\n"
    "v_cvt_pk_bf16_f32 v185, v26, v27\n"
    "v_cvt_pk_bf16_f32 v186, v28, v29\n"
    "v_cvt_pk_bf16_f32 v187, v30, v31\n"
    "v_mfma_f32_16x16x32_bf16 v[44:47], a[80:83], a[160:163], v[44:47]\n"
    "ds_read_b128 a[192:195], v1 offset:4096\n"
    "ds_write_b128 v166, v[180:183] offset:20480\n"
    "ds_write_b128 v166, v[184:187] offset:24576\n"
    "v_mfma_f32_16x16x32_bf16 v[40:43], a[4:7], a[164:167], v[40:43]\n"
    "v_fma_f32 v32, v32, v140, v177\n"
    "v_fma_f32 v33, v33, v141, v177\n"
    "v_mfma_f32_16x16x32_bf16 v[44:47], a[84:87], a[164:167], v[44:47]\n"
    "ds_read_b128 a[196:199], v1 offset:5120\n"
    "v_add_u32 v169, 122880, v0\n"
    "s_add_u32 m0, s82, 122880\n"
    "buffer_load_dwordx4 v[248:251], v10, s[12:15], s74 offen offset:64\n"
    "v_mfma_f32_16x16x32_bf16 v[40:43], a[8:11], a[168:171], v[40:43]\n"
    "v_fma_f32 v34, v34, v142, v177\n"
    "v_fma_f32 v35, v35, v143, v177\n"
    "v_mfma_f32_16x16x32_bf16 v[44:47], a[88:91], a[168:171], v[44:47]\n"
    "ds_read_b128 a[160:163], v1 offset:6144\n"
    "v_fma_f32 v36, v36, v144, v177\n"
    "s_waitcnt lgkmcnt(6)\n"
    "v_mfma_f32_16x16x32_bf16 v[40:43], a[12:15], a[172:175], v[40:43]\n"
    "buffer_load_dwordx4 v169, s[4:7], s52 offen offset:0 sc1 lds\n"
    "buffer_load_dwordx2 v[252:253], v11, s[12:15], s74 offen offset:128\n"
    "v_mfma_f32_16x16x32_bf16 v[44:47], a[92:95], a[172:175], v[44:47]\n"
    "ds_read_b128 a[164:167], v1 offset:7168\n"
    "v_fma_f32 v37, v37, v145, v177\n"
    "v_mfma_f32_16x16x32_bf16 v[40:43], a[16:19], a[176:179], v[40:43]\n"
    "v_fma_f32 v38, v38, v146, v177\n"
    "v_fma_f32 v39, v39, v147, v177\n"
    "v_mfma_f32_16x16x32_bf16 v[44:47], a[96:99], a[176:179], v[44:47]\n"
    "ds_read_b128 a[168:171], v1 offset:8192\n"
    "v_cvt_pk_bf16_f32 v200, v32, v33\n"
    "v_mfma_f32_16x16x32_bf16 v[40:43], a[20:23], a[180:183], v[40:43]\n"
    "buffer_load_dwordx4 v169, s[4:7], s53 offen offset:1024 sc1 lds\n"
    "s_waitcnt vmcnt(17)\n"
    "v_mfma_f32_16x16x32_bf16 v[44:47], a[100:103], a[180:183], v[44:47]\n"
    "ds_read_b128 a[172:175], v1 offset:9216\n"
    "ds_read_b64_tr_b16 a[200:201], v163 offset:0\n"
    "v_mfma_f32_16x16x32_bf16 v[40:43], a[24:27], a[184:187], v[40:43]\n"
    "ds_read_b64_tr_b16 a[202:203], v163 offset:512\n"
    "v_cvt_pk_bf16_f32 v201, v34, v35\n"
    "v_cvt_pk_bf16_f32 v202, v36, v37\n"
    "v_mfma_f32_16x16x32_bf16 v[44:47], a[104:107], a[184:187], v[44:47]\n"
    "ds_read_b128 a[176:179], v1 offset:10240\n"
    "v_cvt_pk_bf16_f32 v203, v38, v39\n"
    "s_waitcnt lgkmcnt(8)\n"
    "v_mfma_f32_16x16x32_bf16 v[40:43], a[28:31], a[188:191], v[40:43]\n"
    "buffer_load_dwordx4 v169, s[4:7], s54 offen offset:2048 sc1 lds\n"
    "v_cndmask_b32 v200, v200, v112, s[22:23]\n"
    "v_mfma_f32_16x16x32_bf16 v[44:47], a[108:111], a[188:191], v[44:47]\n"
    "ds_read_b128 a[180:183], v1 offset:11264\n"
    "buffer_load_dword v7, v168, s[4:7], 0 offen sc1\n"
    "v_cndmask_b32 v201, v201, v112, s[22:23]\n"
    "v_cndmask_b32 v202, v202, v112, s[22:23]\n"
    "v_mfma_f32_16x16x32_bf16 v[40:43], a[32:35], a[192:195], v[40:43]\n"
    "v_cndmask_b32 v203, v203, v112, s[22:23]\n"
    "ds_write_b128 v166, v[200:203] offset:36864\n"
    "v_mfma_f32_16x16x32_bf16 v[44:47], a[112:115], a[192:195], v[44:47]\n"
    "ds_read_b128 a[184:187], v1 offset:12288\n"
    "buffer_load_dwordx4 v169, s[4:7], s55 offen offset:3072 sc1 lds\n"
    "s_add_u32 m0, m0, 4096\n"
    "s_waitcnt vmcnt(19)\n"
    "s_waitcnt lgkmcnt(7)\n"
    "v_mfma_f32_16x16x32_bf16 v[40:43], a[36:39], a[196:199], v[40:43]\n"
    "ds_read_b64_tr_b16 a[204:205], v163 offset:1024\n"
    "v_fma_f32 v120, v32, v172, v174\n"
    "v_mfma_f32_16x16x32_bf16 v[44:47], a[116:119], a[196:199], v[44:47]\n"
    "ds_read_b128 a[188:191], v1 offset:13312\n"
    "ds_read_b64_tr_b16 a[206:207], v163 offset:1536\n"
    "v_fma_f32 v121, v33, v172, v174\n"
    "v_fma_f32 v122, v34, v172, v174\n"
    "v_mfma_f32_16x16x32_bf16 v[40:43], a[40:43], a[160:163], v[40:43]\n"
    "v_fma_f32 v123, v35, v172, v174\n"
    "v_fma_f32 v124, v36, v172, v174\n"
    "v_mfma_f32_16x16x32_bf16 v[44:47], a[120:123], a[160:163], v[44:47]\n"
    "ds_read_b128 a[192:195], v1 offset:14336\n"
    "buffer_load_dwordx4 v169, s[4:7], s56 offen offset:0 sc1 lds\n"
    "v_mfma_f32_16x16x32_bf16 v[40:43], a[44:47], a[164:167], v[40:43]\n"
    "s_waitcnt lgkmcnt(5)\n"
    "buffer_store_dwordx4 a[200:203], v100, s[4:7], s68 offen sc1\n"
    "v_fma_f32 v125, v37, v172, v174\n"
    "v_fma_f32 v126, v38, v172, v174\n"
    "v_mfma_f32_16x16x32_bf16 v[44:47], a[124:127], a[164:167], v[44:47]\n"
    "ds_read_b128 a[196:199], v1 offset:15360\n"
    "v_fma_f32 v127, v39, v172, v174\n"
    "v_mfma_f32_16x16x32_bf16 v[40:43], a[48:51], a[168:171], v[40:43]\n"
    "v_mfma_f32_16x16x32_bf16 v[204:207], v[108:111], a[200:203], 0\n"
    "s_waitcnt vmcnt(20)\n"
    "v_exp_f32 v120, v120 clamp\n"
    "v_mfma_f32_16x16x32_bf16 v[44:47], a[128:131], a[168:171], v[44:47]\n"
    "s_barrier\n"
    "ds_read_b128 a[160:163], v1 offset:16384\n"
    "ds_read_b64_tr_b16 a[208:209], v163 offset:2048\n"
    "v_max_f32 v32, v176, v32\n"
    "v_mfma_f32_16x16x32_bf16 v[40:43], a[52:55], a[172:175], v[40:43]\n"
    "ds_read_b64_tr_b16 a[210:211], v163 offset:2560\n"
    "v_exp_f32 v121, v121 clamp\n"
    "v_max_f32 v33, v176, v33\n"
    "v_mfma_f32_16x16x32_bf16 v[44:47], a[132:135], a[172:175], v[44:47]\n"
    "ds_read_b128 a[164:167], v1 offset:17408\n"
    "v_exp_f32 v122, v122 clamp\n"
    "v_mfma_f32_16x16x32_bf16 v[40:43], a[56:59], a[176:179], v[40:43]\n"
    "v_mfma_f32_16x16x16_bf16 v[48:51], v[152:153], a[248:249], 0\n"
    "v_max_f32 v34, v176, v34\n"
    "v_mfma_f32_16x16x32_bf16 v[44:47], a[136:139], a[176:179], v[44:47]\n"
    "v_mfma_f32_16x16x16_bf16 v[52:55], v[154:155], a[248:249], 0\n"
    "ds_read_b128 a[168:171], v1 offset:18432\n"
    "s_waitcnt lgkmcnt(6)\n"
    "buffer_store_dwordx4 a[204:207], v101, s[4:7], s69 offen sc1\n"
    "v_exp_f32 v123, v123 clamp\n"
    "v_max_f32 v35, v176, v35\n"
    "v_mfma_f32_16x16x32_bf16 v[40:43], a[60:63], a[180:183], v[40:43]\n"
    "v_exp_f32 v124, v124 clamp\n"
    "v_mfma_f32_16x16x32_bf16 v[44:47], a[140:143], a[180:183], v[44:47]\n"
    "ds_read_b128 a[172:175], v1 offset:19456\n"
    "v_mfma_f32_16x16x32_bf16 v[204:207], v[108:111], a[204:207], v[204:207]\n"
    "s_waitcnt vmcnt(20)\n"
    "v_max_f32 v36, v176, v36\n"
    "v_mfma_f32_16x16x32_bf16 v[40:43], a[64:67], a[184:187], v[40:43]\n"
    "ds_read_b64_tr_b16 a[212:213], v163 offset:3072\n"
    "v_exp_f32 v125, v125 clamp\n"
    "v_mfma_f32_16x16x32_bf16 v[44:47], a[144:147], a[184:187], v[44:47]\n"
    "ds_read_b128 a[176:179], v1 offset:20480\n"
    "ds_read_b64_tr_b16 a[214:215], v163 offset:3584\n"
    "v_max_f32 v37, v176, v37\n"
    "v_exp_f32 v126, v126 clamp\n"
    "v_mfma_f32_16x16x32_bf16 v[40:43], a[68:71], a[188:191], v[40:43]\n"
    "v_max_f32 v38, v176, v38\n"
    "v_mfma_f32_16x16x32_bf16 v[44:47], a[148:151], a[188:191], v[44:47]\n"
    "ds_read_b128 a[180:183], v1 offset:21504\n"
    "v_exp_f32 v127, v127 clamp\n"
    "v_mfma_f32_16x16x32_bf16 v[40:43], a[72:75], a[192:195], v[40:43]\n"
    "s_waitcnt lgkmcnt(5)\n"
    "buffer_store_dwordx4 a[208:211], v102, s[4:7], s70 offen sc1\n"
    "v_max_f32 v39, v176, v39\n"
    "v_add_f32 v32, v32, v120\n"
    "v_mfma_f32_16x16x32_bf16 v[44:47], a[152:155], a[192:195], v[44:47]\n"
    "ds_read_b128 a[184:187], v1 offset:22528\n"
    "v_add_f32 v33, v33, v121\n"
    "v_mfma_f32_16x16x32_bf16 v[40:43], a[76:79], a[196:199], v[40:43]\n"
    "v_mfma_f32_16x16x32_bf16 v[204:207], v[108:111], a[208:211], v[204:207]\n"
    "s_waitcnt vmcnt(20)\n"
    "v_add_f32 v34, v34, v122\n"
    "v_mfma_f32_16x16x32_bf16 v[44:47], a[156:159], a[196:199], v[44:47]\n"
    "ds_read_b128 a[188:191], v1 offset:23552\n"
    "ds_read_b64_tr_b16 a[216:217], v163 offset:4096\n"
    "v_add_f32 v35, v35, v123\n"
    "v_mfma_f32_16x16x32_bf16 v[48:51], a[0:3], a[160:163], v[48:51]\n"
    "ds_read_b64_tr_b16 a[218:219], v163 offset:4608\n"
    "v_add_f32 v36, v36, v124\n"
    "v_add_f32 v37, v37, v125\n"
    "v_mfma_f32_16x16x32_bf16 v[52:55], a[80:83], a[160:163], v[52:55]\n"
    "ds_read_b128 a[192:195], v1 offset:24576\n"
    "v_add_f32 v38, v38, v126\n"
    "v_mfma_f32_16x16x32_bf16 v[48:51], a[4:7], a[164:167], v[48:51]\n"
    "v_add_f32 v39, v39, v127\n"
    "v_cvt_pk_bf16_f32 v188, v32, v33\n"
    "v_mfma_f32_16x16x32_bf16 v[52:55], a[84:87], a[164:167], v[52:55]\n"
    "ds_read_b128 a[196:199], v1 offset:25600\n"
    "s_waitcnt lgkmcnt(4)\n"
    "buffer_store_dwordx4 a[212:215], v103, s[4:7], s71 offen sc1\n"
    "v_cvt_pk_bf16_f32 v189, v34, v35\n"
    "v_cvt_pk_bf16_f32 v190, v36, v37\n"
    "v_cvt_pk_bf16_f32 v191, v38, v39\n"
    "ds_write_b128 v166, v[188:191] offset:28672\n"
    "v_mfma_f32_16x16x32_bf16 v[48:51], a[8:11], a[168:171], v[48:51]\n"
    "v_mul_f32 v40, v40, v140\n"
    "v_mul_f32 v41, v41, v141\n"
    "v_mfma_f32_16x16x32_bf16 v[52:55], a[88:91], a[168:171], v[52:55]\n"
    "ds_read_b128 a[160:163], v1 offset:26624\n"
    "v_mfma_f32_16x16x32_bf16 v[204:207], v[108:111], a[212:215], v[204:207]\n"
    "v_mul_f32 v42, v42, v142\n"
    "v_mul_f32 v43, v43, v143\n"
    "v_mfma_f32_16x16x32_bf16 v[48:51], a[12:15], a[172:175], v[48:51]\n"
    "v_mul_f32 v44, v44, v144\n"
    "v_mul_f32 v45, v45, v145\n"
    "v_mfma_f32_16x16x32_bf16 v[52:55], a[92:95], a[172:175], v[52:55]\n"
    "ds_read_b128 a[164:167], v1 offset:27648\n"
    "v_mul_f32 v46, v46, v146\n"
    "v_mfma_f32_16x16x32_bf16 v[48:51], a[16:19], a[176:179], v[48:51]\n"
    "v_mul_f32 v47, v47, v147\n"
    "v_cvt_pk_bf16_f32 v192, v40, v41\n"
    "v_mfma_f32_16x16x32_bf16 v[52:55], a[96:99], a[176:179], v[52:55]\n"
    "ds_read_b128 a[168:171], v1 offset:28672\n"
    "v_cvt_pk_bf16_f32 v193, v42, v43\n"
    "v_mfma_f32_16x16x32_bf16 v[48:51], a[20:23], a[180:183], v[48:51]\n"
    "s_waitcnt lgkmcnt(3)\n"
    "buffer_store_dwordx4 a[216:219], v104, s[4:7], s72 offen sc1\n"
    "v_cvt_pk_bf16_f32 v194, v44, v45\n"
    "v_mfma_f32_16x16x32_bf16 v[52:55], a[100:103], a[180:183], v[52:55]\n"
    "ds_read_b128 a[172:175], v1 offset:29696\n"
    "v_cvt_pk_bf16_f32 v195, v46, v47\n"
    "v_mfma_f32_16x16x32_bf16 v[48:51], a[24:27], a[184:187], v[48:51]\n"
    "v_mfma_f32_16x16x32_bf16 v[204:207], v[108:111], a[216:219], v[204:207]\n"
    "ds_write_b128 v166, v[192:195] offset:32768\n"
    "v_mfma_f32_16x16x32_bf16 v[52:55], a[104:107], a[184:187], v[52:55]\n"
    "ds_read_b128 a[176:179], v1 offset:30720\n"
    "v_mfma_f32_16x16x32_bf16 v[48:51], a[28:31], a[188:191], v[48:51]\n"
    "s_waitcnt vmcnt(7)\n"
    "v_cmp_ne_u32 vcc, s29, v7\n"
    "s_cbranch_vccnz pfws_5_%=\n"
    "pfwr_6_%=:\n"
    "v_mfma_f32_16x16x32_bf16 v[52:55], a[108:111], a[188:191], v[52:55]\n"
    "ds_read_b128 a[180:183], v1 offset:31744\n"
    "ds_write_b32 v164, v204 offset:27648\n"
    "v_mfma_f32_16x16x32_bf16 v[48:51], a[32:35], a[192:195], v[48:51]\n"
    "s_waitcnt lgkmcnt(0)\n"
    "s_barrier\n"
    "v_mfma_f32_16x16x32_bf16 v[52:55], a[112:115], a[192:195], v[52:55]\n"
    "ds_read_b128 a[184:187], v1 offset:32768\n"
    "ds_read_b128 v[16:19], v92 offset:20480\n"
    "v_mfma_f32_16x16x32_bf16 v[48:51], a[36:39], a[196:199], v[48:51]\n"
    "ds_read_b128 v[204:207], v2 offset:27648\n"
    "s_add_u32 m0, s82, 0\n"
    "v_mfma_f32_16x16x32_bf16 v[52:55], a[116:119], a[196:199], v[52:55]\n"
    "ds_read_b128 a[188:191], v1 offset:33792\n"
    "s_waitcnt lgkmcnt(1)\n"
    "v_add_f32 v204, v204, v205\n"
    "v_mfma_f32_16x16x32_bf16 v[48:51], a[40:43], a[160:163], v[48:51]\n"
    "buffer_load_dwordx4 v0, s[4:7], s79 offen offset:0 sc1 lds\n"
    "ds_read_b128 v[32:35], v93 offset:20480\n"
    "v_mfma_f32_16x16x32_bf16 v[52:55], a[120:123], a[160:163], v[52:55]\n"
    "ds_read_b128 a[192:195], v1 offset:34816\n"
    "v_add_f32 v206, v206, v207\n"
    "ds_read_b128 v[20:23], v92 offset:21504\n"
    "v_mfma_f32_16x16x32_bf16 v[48:51], a[44:47], a[164:167], v[48:51]\n"
    "ds_read_b128 v[36:39], v93 offset:21504\n"
    "ds_read_b128 v[24:27], v92 offset:22528\n"
    "v_mfma_f32_16x16x32_bf16 v[52:55], a[124:127], a[164:167], v[52:55]\n"
    "ds_read_b128 a[196:199], v1 offset:35840\n"
    "v_add_f32 v206, v204, v206\n"
    "buffer_load_dwordx4 v0, s[4:7], s79 offen offset:1024 sc1 lds\n"
    "v_mfma_f32_16x16x32_bf16 v[48:51], a[48:51], a[168:171], v[48:51]\n"
    "ds_read_b128 v[180:183], v93 offset:22528\n"
    "ds_read_b128 v[28:31], v92 offset:23552\n"
    "v_mfma_f32_16x16x32_bf16 v[52:55], a[128:131], a[168:171], v[52:55]\n"
    "v_cvt_pk_bf16_f32 v205, v206, v206\n"
    "ds_read_b128 v[184:187], v93 offset:23552\n"
    "v_mfma_f32_16x16x32_bf16 v[48:51], a[52:55], a[172:175], v[48:51]\n"
    "buffer_load_dwordx4 v0, s[4:7], s79 offen offset:2048 sc1 lds\n"
    "ds_read_b128 v[188:191], v94 offset:20480\n"
    "v_mfma_f32_16x16x32_bf16 v[52:55], a[132:135], a[172:175], v[52:55]\n"
    "v_lshlrev_b32 v205, 16, v205\n"
    "ds_read_b128 v[200:203], v94 offset:21504\n"
    "ds_read_b128 v[40:43], v94 offset:22528\n"
    "ds_read_b128 v[44:47], v94 offset:23552\n"
    "v_mfma_f32_16x16x32_bf16 v[48:51], a[56:59], a[176:179], v[48:51]\n"
    "v_mfma_f32_16x16x16_bf16 v[56:59], a[250:251], v[152:153], 0\n"
    "v_sub_f32 v205, v206, v205\n"
    "buffer_load_dwordx4 v0, s[4:7], s79 offen offset:3072 sc1 lds\n"
    "s_add_u32 m0, m0, 4096\n"
    "v_mfma_f32_16x16x32_bf16 v[52:55], a[136:139], a[176:179], v[52:55]\n"
    "v_mfma_f32_16x16x16_bf16 v[68:71], a[250:251], v[154:155], 0\n"
    "v_mfma_f32_16x16x32_bf16 v[48:51], a[60:63], a[180:183], v[48:51]\n"
    "v_cvt_pk_bf16_f32 v204, v206, v205\n"
    "v_mfma_f32_16x16x32_bf16 v[52:55], a[140:143], a[180:183], v[52:55]\n"
    "buffer_load_dwordx4 v240, s[4:7], s79 offen sc1 lds\n"
    "v_mfma_f32_16x16x32_bf16 v[48:51], a[64:67], a[184:187], v[48:51]\n"
    "v_mov_b32 v205, v204\n"
    "v_mfma_f32_16x16x32_bf16 v[52:55], a[144:147], a[184:187], v[52:55]\n"
    "s_waitcnt lgkmcnt(9)\n"
    "v_mfma_f32_16x16x32_bf16 v[120:123], v[16:19], v[32:35], 0\n"
    "v_mfma_f32_16x16x32_bf16 v[120:123], v[20:23], v[36:39], v[120:123]\n"
    "s_waitcnt lgkmcnt(5)\n"
    "v_mfma_f32_16x16x32_bf16 v[120:123], v[24:27], v[180:183], v[120:123]\n"
    "s_waitcnt lgkmcnt(3)\n"
    "v_mfma_f32_16x16x32_bf16 v[120:123], v[28:31], v[184:187], v[120:123]\n"
    "v_mfma_f32_16x16x32_bf16 v[48:51], a[68:71], a[188:191], v[48:51]\n"
    "buffer_store_dwordx4 v[204:207], v100, s[4:7], s50 offen sc1\n"
    "v_mfma_f32_16x16x32_bf16 v[52:55], a[148:151], a[188:191], v[52:55]\n"
    "v_mfma_f32_16x16x32_bf16 v[48:51], a[72:75], a[192:195], v[48:51]\n"
    "v_mfma_f32_16x16x32_bf16 v[52:55], a[152:155], a[192:195], v[52:55]\n"
    "v_mfma_f32_16x16x32_bf16 v[48:51], a[76:79], a[196:199], v[48:51]\n"
    "v_mfma_f32_16x16x32_bf16 v[52:55], a[156:159], a[196:199], v[52:55]\n"
    "s_nop 0\n"
    "v_cvt_pk_bf16_f32 v120, v120, v121\n"
    "v_cvt_pk_bf16_f32 v121, v122, v123\n"
    "buffer_store_dwordx2 v[120:121], v82, s[4:7], s48 offen sc1\n"
    "s_nop 2\n"
    "v_mul_f32 v48, v48, v140\n"
    "v_mul_f32 v49, v49, v141\n"
    "v_mul_f32 v50, v50, v142\n"
    "v_mul_f32 v51, v51, v143\n"
    "v_mul_f32 v52, v52, v144\n"
    "v_mul_f32 v53, v53, v145\n"
    "v_mul_f32 v54, v54, v146\n"
    "v_mul_f32 v55, v55, v147\n"
    "v_cvt_pk_bf16_f32 v196, v48, v49\n"
    "v_cvt_pk_bf16_f32 v197, v50, v51\n"
    "v_cvt_pk_bf16_f32 v198, v52, v53\n"
    "v_cvt_pk_bf16_f32 v199, v54, v55\n"
    "ds_write_b128 v166, v[196:199] offset:40960\n"
    "s_waitcnt lgkmcnt(0)\n"
    "s_barrier\n"
    "ds_read_b128 v[16:19], v95 offset:20480\n"
    "ds_read_b128 v[20:23], v95 offset:21504\n"
    "ds_read_b128 v[24:27], v95 offset:22528\n"
    "ds_read_b128 v[28:31], v95 offset:23552\n"
    "ds_read_b128 a[160:163], v1 offset:36864\n"
    "ds_read_b128 a[164:167], v1 offset:37888\n"
    "ds_read_b128 a[168:171], v1 offset:38912\n"
    "ds_read_b128 a[172:175], v1 offset:39936\n"
    "ds_read_b128 a[176:179], v1 offset:40960\n"
    "ds_read_b128 a[180:183], v1 offset:41984\n"
    "ds_read_b128 a[184:187], v1 offset:43008\n"
    "ds_read_b128 a[188:191], v1 offset:44032\n"
    "s_waitcnt lgkmcnt(8)\n"
    "v_mfma_f32_16x16x32_bf16 v[124:127], v[188:191], v[16:19], 0\n"
    "v_mfma_f32_16x16x32_bf16 v[124:127], v[200:203], v[20:23], v[124:127]\n"
    "v_mfma_f32_16x16x32_bf16 v[124:127], v[40:43], v[24:27], v[124:127]\n"
    "v_mfma_f32_16x16x32_bf16 v[124:127], v[44:47], v[28:31], v[124:127]\n"
    "s_nop 7\n"
    "v_cvt_pk_bf16_f32 v124, v124, v125\n"
    "v_cvt_pk_bf16_f32 v125, v126, v127\n"
    "buffer_store_dwordx2 v[124:125], v83, s[4:7], s48 offen sc1\n"
    "s_waitcnt lgkmcnt(4)\n"
    "v_mfma_f32_16x16x32_bf16 v[56:59], a[160:163], a[0:3], v[56:59]\n"
    "v_mfma_f32_16x16x32_bf16 v[68:71], a[160:163], a[80:83], v[68:71]\n"
    "ds_read_b128 a[192:195], v1 offset:45056\n"
    "v_mfma_f32_16x16x32_bf16 v[56:59], a[164:167], a[4:7], v[56:59]\n"
    "s_add_u32 m0, s82, 61440\n"
    "v_mfma_f32_16x16x32_bf16 v[68:71], a[164:167], a[84:87], v[68:71]\n"
    "ds_read_b128 a[196:199], v1 offset:46080\n"
    "v_mfma_f32_16x16x32_bf16 v[56:59], a[168:171], a[8:11], v[56:59]\n"
    "v_mfma_f32_16x16x32_bf16 v[68:71], a[168:171], a[88:91], v[68:71]\n"
    "ds_read_b128 a[160:163], v1 offset:47104\n"
    "v_add_u32 v241, s99, v0\n"
    "v_mfma_f32_16x16x32_bf16 v[56:59], a[172:175], a[12:15], v[56:59]\n"
    "buffer_load_dwordx4 v0, s[4:7], s77 offen offset:0 sc1 lds\n"
    "s_sub_u32 s20, s98, 61440\n"
    "v_mfma_f32_16x16x32_bf16 v[68:71], a[172:175], a[92:95], v[68:71]\n"
    "ds_read_b128 a[164:167], v1 offset:48128\n"
    "v_and_b32 v111, 16, v0\n"
    "s_waitcnt lgkmcnt(4)\n"
    "v_mfma_f32_16x16x32_bf16 v[56:59], a[176:179], a[16:19], v[56:59]\n"
    "buffer_load_dwordx4 v0, s[4:7], s77 offen offset:1024 sc1 lds\n"
    "v_lshrrev_b32 v110, 16, v112\n"
    "v_mfma_f32_16x16x32_bf16 v[68:71], a[176:179], a[96:99], v[68:71]\n"
    "ds_read_b128 a[168:171], v1 offset:49152\n"
    "v_lshlrev_b32 v111, v111, v110\n"
    "v_mfma_f32_16x16x32_bf16 v[56:59], a[180:183], a[20:23], v[56:59]\n"
    "s_mov_b32 s22, 0x00300003\n"
    "s_mov_b32 s23, 0x30000300\n"
    "v_mfma_f32_16x16x32_bf16 v[68:71], a[180:183], a[100:103], v[68:71]\n"
    "ds_read_b128 a[172:175], v1 offset:50176\n"
    "buffer_load_dwordx4 v0, s[4:7], s77 offen offset:2048 sc1 lds\n"
    "v_mfma_f32_16x16x32_bf16 v[56:59], a[184:187], a[24:27], v[56:59]\n"
    "v_cndmask_b32 v108, 0, v111, s[22:23]\n"
    "v_cndmask_b32 v110, 0, v111, s[22:23]\n"
    "v_mfma_f32_16x16x32_bf16 v[68:71], a[184:187], a[104:107], v[68:71]\n"
    "ds_read_b128 a[176:179], v1 offset:51200\n"
    "s_lshl_b64 s[22:23], s[22:23], 2\n"
    "v_mfma_f32_16x16x32_bf16 v[56:59], a[188:191], a[28:31], v[56:59]\n"
    "buffer_load_dwordx4 v0, s[4:7], s77 offen offset:3072 sc1 lds\n"
    "s_add_u32 m0, m0, 4096\n"
    "v_mfma_f32_16x16x32_bf16 v[68:71], a[188:191], a[108:111], v[68:71]\n"
    "ds_read_b128 a[180:183], v1 offset:52224\n"
    "v_cndmask_b32 v109, 0, v111, s[22:23]\n"
    "s_waitcnt lgkmcnt(4)\n"
    "v_mfma_f32_16x16x32_bf16 v[56:59], a[192:195], a[32:35], v[56:59]\n"
    "v_cndmask_b32 v111, 0, v111, s[22:23]\n"
    "v_mov_b32 v120, v176\n"
    "v_mfma_f32_16x16x32_bf16 v[68:71], a[192:195], a[112:115], v[68:71]\n"
    "ds_read_b128 a[184:187], v1 offset:53248\n"
    "buffer_load_dwordx4 v240, s[4:7], s77 offen sc1 lds\n"
    "v_mfma_f32_16x16x32_bf16 v[56:59], a[196:199], a[36:39], v[56:59]\n"
    "v_mov_b32 v121, v176\n"
    "v_mov_b32 v122, v176\n"
    "v_mfma_f32_16x16x32_bf16 v[68:71], a[196:199], a[116:119], v[68:71]\n"
    "ds_read_b128 a[188:191], v1 offset:54272\n"
    "v_mov_b32 v123, v176\n"
    "v_mfma_f32_16x16x32_bf16 v[56:59], a[160:163], a[40:43], v[56:59]\n"
    "v_mfma_f32_16x16x32_bf16 v[68:71], a[160:163], a[120:123], v[68:71]\n"
    "ds_read_b128 a[192:195], v1 offset:55296\n"
    "s_add_u32 m0, s82, 81920\n"
    "v_mfma_f32_16x16x32_bf16 v[56:59], a[164:167], a[44:47], v[56:59]\n"
    "v_mfma_f32_16x16x32_bf16 v[68:71], a[164:167], a[124:127], v[68:71]\n"
    "ds_read_b128 a[196:199], v1 offset:56320\n"
    "s_waitcnt lgkmcnt(4)\n"
    "v_mfma_f32_16x16x32_bf16 v[56:59], a[168:171], a[48:51], v[56:59]\n"
    "buffer_load_dwordx4 v0, s[4:7], s78 offen offset:0 sc1 lds\n"
    "v_mfma_f32_16x16x32_bf16 v[68:71], a[168:171], a[128:131], v[68:71]\n"
    "ds_read_b128 a[160:163], v91 offset:0\n"
    "v_mfma_f32_16x16x32_bf16 v[56:59], a[172:175], a[52:55], v[56:59]\n"
    "v_mfma_f32_16x16x32_bf16 v[68:71], a[172:175], a[132:135], v[68:71]\n"
    "ds_read_b128 a[164:167], v91 offset:512\n"
    "buffer_load_dwordx4 v0, s[4:7], s78 offen offset:1024 sc1 lds\n"
    "v_mfma_f32_16x16x32_bf16 v[56:59], a[176:179], a[56:59], v[56:59]\n"
    "v_mfma_f32_16x16x16_bf16 v[64:67], a[254:255], v[152:153], 0\n"
    "v_mfma_f32_16x16x32_bf16 v[68:71], a[176:179], a[136:139], v[68:71]\n"
    "v_mfma_f32_16x16x16_bf16 v[76:79], a[254:255], v[154:155], 0\n"
    "ds_read_b128 a[168:171], v91 offset:1024\n"
    "v_mfma_f32_16x16x32_bf16 v[56:59], a[180:183], a[60:63], v[56:59]\n"
    "buffer_load_dwordx4 v0, s[4:7], s78 offen offset:2048 sc1 lds\n"
    "v_mfma_f32_16x16x32_bf16 v[68:71], a[180:183], a[140:143], v[68:71]\n"
    "ds_read_b128 a[172:175], v91 offset:1536\n"
    "s_waitcnt lgkmcnt(4)\n"
    "v_mfma_f32_16x16x32_bf16 v[56:59], a[184:187], a[64:67], v[56:59]\n"
    "v_mfma_f32_16x16x32_bf16 v[68:71], a[184:187], a[144:147], v[68:71]\n"
    "ds_read_b128 a[176:179], v91 offset:2048\n"
    "v_mfma_f32_16x16x32_bf16 v[56:59], a[188:191], a[68:71], v[56:59]\n"
    "buffer_load_dwordx4 v0, s[4:7], s78 offen offset:3072 sc1 lds\n"
    "s_add_u32 m0, m0, 4096\n"
    "v_mfma_f32_16x16x32_bf16 v[68:71], a[188:191], a[148:151], v[68:71]\n"
    "ds_read_b128 a[180:183], v91 offset:2560\n"
    "v_mfma_f32_16x16x32_bf16 v[56:59], a[192:195], a[72:75], v[56:59]\n"
    "buffer_load_dwordx4 v240, s[4:7], s78 offen sc1 lds\n"
    "v_mfma_f32_16x16x32_bf16 v[68:71], a[192:195], a[152:155], v[68:71]\n"
    "ds_read_b128 a[184:187], v91 offset:3072\n"
    "v_mfma_f32_16x16x32_bf16 v[56:59], a[196:199], a[76:79], v[56:59]\n"
    "v_mfma_f32_16x16x32_bf16 v[68:71], a[196:199], a[156:159], v[68:71]\n"
    "ds_read_b128 a[188:191], v91 offset:3584\n"
    "s_waitcnt lgkmcnt(4)\n"
    "v_mfma_f32_16x16x32_bf16 v[64:67], a[160:163], a[0:3], v[64:67]\n"
    "s_waitcnt vmcnt(12)\n"
    "s_barrier\n"
    "buffer_store_dword v9, v6, s[4:7], s39 offen offset:0 sc1\n"
    "v_mfma_f32_16x16x32_bf16 v[76:79], a[160:163], a[80:83], v[76:79]\n"
    "ds_read_b128 a[192:195], v91 offset:4096\n"
    "s_add_u32 m0, s82, 20480\n"
    "v_mfma_f32_16x16x32_bf16 v[64:67], a[164:167], a[4:7], v[64:67]\n"
    "v_fma_f32 v56, v56, v148, v176\n"
    "v_fma_f32 v57, v57, v148, v176\n"
    "v_mfma_f32_16x16x32_bf16 v[76:79], a[164:167], a[84:87], v[76:79]\n"
    "ds_read_b128 a[196:199], v91 offset:4608\n"
    "v_fma_f32 v58, v58, v148, v176\n"
    "v_mfma_f32_16x16x32_bf16 v[64:67], a[168:171], a[8:11], v[64:67]\n"
    "v_fma_f32 v59, v59, v148, v176\n"
    "v_fma_f32 v68, v68, v149, v176\n"
    "v_mfma_f32_16x16x32_bf16 v[76:79], a[168:171], a[88:91], v[76:79]\n"
    "ds_read_b128 a[160:163], v91 offset:5120\n"
    "buffer_load_dwordx4 v0, s[4:7], s80 offen offset:0 sc1 lds\n"
    "v_mfma_f32_16x16x32_bf16 v[64:67], a[172:175], a[12:15], v[64:67]\n"
    "v_fma_f32 v69, v69, v149, v176\n"
    "v_fma_f32 v70, v70, v149, v176\n"
    "v_mfma_f32_16x16x32_bf16 v[76:79], a[172:175], a[92:95], v[76:79]\n"
    "ds_read_b128 a[164:167], v91 offset:5632\n"
    "v_fma_f32 v71, v71, v149, v176\n"
    "s_waitcnt lgkmcnt(4)\n"
    "v_mfma_f32_16x16x32_bf16 v[64:67], a[176:179], a[16:19], v[64:67]\n"
    "v_fma_f32 v128, v56, v172, v174\n"
    "v_fma_f32 v129, v57, v172, v174\n"
    "v_mfma_f32_16x16x32_bf16 v[76:79], a[176:179], a[96:99], v[76:79]\n"
    "ds_read_b128 a[168:171], v91 offset:6144\n"
    "buffer_load_dwordx4 v0, s[4:7], s80 offen offset:1024 sc1 lds\n"
    "v_mfma_f32_16x16x32_bf16 v[64:67], a[180:183], a[20:23], v[64:67]\n"
    "v_fma_f32 v130, v58, v172, v174\n"
    "v_fma_f32 v131, v59, v172, v174\n"
    "v_mfma_f32_16x16x32_bf16 v[76:79], a[180:183], a[100:103], v[76:79]\n"
    "ds_read_b128 a[172:175], v91 offset:6656\n"
    "v_fma_f32 v132, v68, v172, v174\n"
    "v_mfma_f32_16x16x32_bf16 v[64:67], a[184:187], a[24:27], v[64:67]\n"
    "v_fma_f32 v133, v69, v172, v174\n"
    "v_fma_f32 v134, v70, v172, v174\n"
    "v_mfma_f32_16x16x32_bf16 v[76:79], a[184:187], a[104:107], v[76:79]\n"
    "ds_read_b128 a[176:179], v91 offset:7168\n"
    "buffer_load_dwordx4 v0, s[4:7], s80 offen offset:2048 sc1 lds\n"
    "v_mfma_f32_16x16x32_bf16 v[64:67], a[188:191], a[28:31], v[64:67]\n"
    "v_fma_f32 v135, v71, v172, v174\n"
    "v_mfma_f32_16x16x32_bf16 v[76:79], a[188:191], a[108:111], v[76:79]\n"
    "ds_read_b128 a[180:183], v91 offset:7680\n"
    "s_waitcnt lgkmcnt(4)\n"
    "v_mfma_f32_16x16x32_bf16 v[64:67], a[192:195], a[32:35], v[64:67]\n"
    "v_exp_f32 v128, v128 clamp\n"
    "v_mfma_f32_16x16x32_bf16 v[76:79], a[192:195], a[112:115], v[76:79]\n"
    "ds_read_b128 a[184:187], v91 offset:8192\n"
    "v_max_f32 v56, v176, v56\n"
    "v_mfma_f32_16x16x32_bf16 v[64:67], a[196:199], a[36:39], v[64:67]\n"
    "buffer_load_dwordx4 v0, s[4:7], s80 offen offset:3072 sc1 lds\n"
    "s_add_u32 m0, m0, 4096\n"
    "v_mfma_f32_16x16x32_bf16 v[76:79], a[196:199], a[116:119], v[76:79]\n"
    "ds_read_b128 a[188:191], v91 offset:8704\n"
    "v_mfma_f32_16x16x32_bf16 v[64:67], a[160:163], a[40:43], v[64:67]\n"
    "v_exp_f32 v129, v129 clamp\n"
    "v_mfma_f32_16x16x32_bf16 v[76:79], a[160:163], a[120:123], v[76:79]\n"
    "ds_read_b128 a[192:195], v91 offset:9216\n"
    "v_max_f32 v57, v176, v57\n"
    "v_mfma_f32_16x16x32_bf16 v[64:67], a[164:167], a[44:47], v[64:67]\n"
    "v_exp_f32 v130, v130 clamp\n"
    "v_mfma_f32_16x16x32_bf16 v[76:79], a[164:167], a[124:127], v[76:79]\n"
    "ds_read_b128 a[196:199], v91 offset:9728\n"
    "v_max_f32 v58, v176, v58\n"
    "s_waitcnt lgkmcnt(4)\n"
    "v_mfma_f32_16x16x32_bf16 v[64:67], a[168:171], a[48:51], v[64:67]\n"
    "buffer_load_dwordx4 v240, s[4:7], s80 offen sc1 lds\n"
    "v_mfma_f32_16x16x32_bf16 v[76:79], a[168:171], a[128:131], v[76:79]\n"
    "s_barrier\n"
    "ds_read_b128 a[160:163], v1 offset:57344\n"
    "v_exp_f32 v131, v131 clamp\n"
    "v_max_f32 v59, v176, v59\n"
    "v_mfma_f32_16x16x32_bf16 v[64:67], a[172:175], a[52:55], v[64:67]\n"
    "v_exp_f32 v132, v132 clamp\n"
    "v_mfma_f32_16x16x32_bf16 v[76:79], a[172:175], a[132:135], v[76:79]\n"
    "ds_read_b128 a[164:167], v1 offset:58368\n"
    "v_max_f32 v68, v176, v68\n"
    "v_mfma_f32_16x16x32_bf16 v[64:67], a[176:179], a[56:59], v[64:67]\n"
    "v_mfma_f32_16x16x16_bf16 v[60:63], a[252:253], v[152:153], 0\n"
    "v_exp_f32 v133, v133 clamp\n"
    "v_mfma_f32_16x16x32_bf16 v[76:79], a[176:179], a[136:139], v[76:79]\n"
    "v_mfma_f32_16x16x16_bf16 v[72:75], a[252:253], v[154:155], 0\n"
    "ds_read_b128 a[168:171], v1 offset:59392\n"
    "v_max_f32 v69, v176, v69\n"
    "buffer_load_dwordx2 v[16:17], v81, s[4:7], s73 offen offset:0 sc1\n"
    "v_mfma_f32_16x16x32_bf16 v[64:67], a[180:183], a[60:63], v[64:67]\n"
    "v_exp_f32 v134, v134 clamp\n"
    "buffer_load_dwordx2 v[18:19], v81, s[4:7], s73 offen offset:2048 sc1\n"
    "v_mfma_f32_16x16x32_bf16 v[76:79], a[180:183], a[140:143], v[76:79]\n"
    "ds_read_b128 a[172:175], v1 offset:60416\n"
    "v_max_f32 v70, v176, v70\n"
    "s_waitcnt lgkmcnt(4)\n"
    "v_mfma_f32_16x16x32_bf16 v[64:67], a[184:187], a[64:67], v[64:67]\n"
    "v_exp_f32 v135, v135 clamp\n"
    "s_add_u32 s33, s73, 4096\n"
    "buffer_load_dwordx2 v[20:21], v81, s[4:7], s33 offen offset:0 sc1\n"
    "v_mfma_f32_16x16x32_bf16 v[76:79], a[184:187], a[144:147], v[76:79]\n"
    "ds_read_b128 a[176:179], v1 offset:61440\n"
    "v_max_f32 v71, v176, v71\n"
    "buffer_load_dwordx2 v[22:23], v81, s[4:7], s33 offen offset:2048 sc1\n"
    "v_mfma_f32_16x16x32_bf16 v[64:67], a[188:191], a[68:71], v[64:67]\n"
    "v_add_f32 v56, v56, v128\n"
    "v_add_f32 v57, v57, v129\n"
    "v_mfma_f32_16x16x32_bf16 v[76:79], a[188:191], a[148:151], v[76:79]\n"
    "ds_read_b128 a[180:183], v1 offset:62464\n"
    "v_add_f32 v58, v58, v130\n"
    "s_add_u32 s33, s73, 8192\n"
    "buffer_load_dwordx2 v[24:25], v81, s[4:7], s33 offen offset:0 sc1\n"
    "v_mfma_f32_16x16x32_bf16 v[64:67], a[192:195], a[72:75], v[64:67]\n"
    "v_add_f32 v59, v59, v131\n"
    "buffer_load_dwordx2 v[26:27], v81, s[4:7], s33 offen offset:2048 sc1\n"
    "v_mfma_f32_16x16x32_bf16 v[76:79], a[192:195], a[152:155], v[76:79]\n"
    "ds_read_b128 a[184:187], v1 offset:63488\n"
    "v_add_f32 v68, v68, v132\n"
    "v_mfma_f32_16x16x32_bf16 v[64:67], a[196:199], a[76:79], v[64:67]\n"
    "v_add_f32 v69, v69, v133\n"
    "s_add_u32 s33, s73, 12288\n"
    "buffer_load_dwordx2 v[28:29], v81, s[4:7], s33 offen offset:0 sc1\n"
    "v_mfma_f32_16x16x32_bf16 v[76:79], a[196:199], a[156:159], v[76:79]\n"
    "ds_read_b128 a[188:191], v1 offset:64512\n"
    "v_add_f32 v70, v70, v134\n"
    "buffer_load_dwordx2 v[30:31], v81, s[4:7], s33 offen offset:2048 sc1\n"
    "s_waitcnt lgkmcnt(4)\n"
    "v_mfma_f32_16x16x32_bf16 v[60:63], a[160:163], a[0:3], v[60:63]\n"
    "v_add_f32 v71, v71, v135\n"
    "v_cvt_pk_bf16_f32 v92, v56, v57\n"
    "v_mfma_f32_16x16x32_bf16 v[72:75], a[160:163], a[80:83], v[72:75]\n"
    "ds_read_b128 a[192:195], v2 offset:0\n"
    "v_cvt_pk_bf16_f32 v93, v58, v59\n"
    "v_cvt_pk_bf16_f32 v96, v68, v69\n"
    "v_cvt_pk_bf16_f32 v97, v70, v71\n"
    "s_add_u32 s33, s73, 16384\n"
    "buffer_load_dwordx2 v[32:33], v81, s[4:7], s33 offen offset:0 sc1\n"
    "v_mfma_f32_16x16x32_bf16 v[60:63], a[164:167], a[4:7], v[60:63]\n"
    "buffer_load_dwordx2 v[34:35], v81, s[4:7], s33 offen offset:2048 sc1\n"
    "v_fma_f32 v64, v64, v148, v176\n"
    "v_mfma_f32_16x16x32_bf16 v[72:75], a[164:167], a[84:87], v[72:75]\n"
    "ds_read_b128 a[196:199], v2 offset:1024\n"
    "v_fma_f32 v65, v65, v148, v176\n"
    "v_mfma_f32_16x16x32_bf16 v[60:63], a[168:171], a[8:11], v[60:63]\n"
    "s_add_u32 s33, s73, 20480\n"
    "buffer_load_dwordx2 v[36:37], v81, s[4:7], s33 offen offset:0 sc1\n"
    "v_mfma_f32_16x16x32_bf16 v[72:75], a[168:171], a[88:91], v[72:75]\n"
    "ds_read_b128 a[160:163], v2 offset:2048\n"
    "buffer_load_dwordx2 v[38:39], v81, s[4:7], s33 offen offset:2048 sc1\n"
    "v_mfma_f32_16x16x32_bf16 v[60:63], a[172:175], a[12:15], v[60:63]\n"
    "v_fma_f32 v66, v66, v148, v176\n"
    "v_fma_f32 v67, v67, v148, v176\n"
    "v_mfma_f32_16x16x32_bf16 v[72:75], a[172:175], a[92:95], v[72:75]\n"
    "ds_read_b128 a[164:167], v2 offset:3072\n"
    "s_add_u32 s33, s73, 24576\n"
    "buffer_load_dwordx2 v[40:41], v81, s[4:7], s33 offen offset:0 sc1\n"
    "s_waitcnt lgkmcnt(4)\n"
    "v_mfma_f32_16x16x32_bf16 v[60:63], a[176:179], a[16:19], v[60:63]\n"
    "buffer_load_dwordx2 v[42:43], v81, s[4:7], s33 offen offset:2048 sc1\n"
    "v_fma_f32 v76, v76, v149, v176\n"
    "v_mfma_f32_16x16x32_bf16 v[72:75], a[176:179], a[96:99], v[72:75]\n"
    "ds_read_b128 a[168:171], v2 offset:4096\n"
    "s_add_u32 s33, s73, 28672\n"
    "buffer_load_dwordx2 v[44:45], v81, s[4:7], s33 offen offset:0 sc1\n"
    "buffer_load_dwordx2 v[46:47], v81, s[4:7], s33 offen offset:2048 sc1\n"
    "v_fma_f32 v77, v77, v149, v176\n"
    "v_mfma_f32_16x16x32_bf16 v[60:63], a[180:183], a[20:23], v[60:63]\n"
    "v_fma_f32 v78, v78, v149, v176\n"
    "v_fma_f32 v79, v79, v149, v176\n"
    "v_mfma_f32_16x16x32_bf16 v[72:75], a[180:183], a[100:103], v[72:75]\n"
    "ds_read_b128 a[172:175], v2 offset:5120\n"
    "v_fma_f32 v128, v64, v172, v174\n"
    "v_mfma_f32_16x16x32_bf16 v[60:63], a[184:187], a[24:27], v[60:63]\n"
    "v_fma_f32 v129, v65, v172, v174\n"
    "s_add_u32 m0, s82, 40960\n"
    "v_mfma_f32_16x16x32_bf16 v[72:75], a[184:187], a[104:107], v[72:75]\n"
    "ds_read_b128 a[176:179], v2 offset:6144\n"
    "v_fma_f32 v130, v66, v172, v174\n"
    "v_mfma_f32_16x16x32_bf16 v[60:63], a[188:191], a[28:31], v[60:63]\n"
    "v_fma_f32 v131, v67, v172, v174\n"
    "v_fma_f32 v132, v76, v172, v174\n"
    "v_mfma_f32_16x16x32_bf16 v[72:75], a[188:191], a[108:111], v[72:75]\n"
    "ds_read_b128 a[180:183], v2 offset:7168\n"
    "v_fma_f32 v133, v77, v172, v174\n"
    "s_waitcnt lgkmcnt(4)\n"
    "v_mfma_f32_16x16x32_bf16 v[60:63], a[192:195], a[32:35], v[60:63]\n"
    "buffer_load_dwordx4 v0, s[4:7], s81 offen offset:0 sc1 lds\n"
    "v_fma_f32 v134, v78, v172, v174\n"
    "v_mfma_f32_16x16x32_bf16 v[72:75], a[192:195], a[112:115], v[72:75]\n"
    "ds_read_b128 a[184:187], v2 offset:8192\n"
    "v_fma_f32 v135, v79, v172, v174\n"
    "v_mfma_f32_16x16x32_bf16 v[60:63], a[196:199], a[36:39], v[60:63]\n"
    "v_exp_f32 v128, v128 clamp\n"
    "v_mfma_f32_16x16x32_bf16 v[72:75], a[196:199], a[116:119], v[72:75]\n"
    "ds_read_b128 a[188:191], v2 offset:9216\n"
    "v_max_f32 v64, v176, v64\n"
    "v_mfma_f32_16x16x32_bf16 v[60:63], a[160:163], a[40:43], v[60:63]\n"
    "v_exp_f32 v129, v129 clamp\n"
    "v_mfma_f32_16x16x32_bf16 v[72:75], a[160:163], a[120:123], v[72:75]\n"
    "ds_read_b128 a[192:195], v2 offset:10240\n"
    "buffer_load_dwordx4 v0, s[4:7], s81 offen offset:1024 sc1 lds\n"
    "v_max_f32 v65, v176, v65\n"
    "v_mfma_f32_16x16x32_bf16 v[60:63], a[164:167], a[44:47], v[60:63]\n"
    "v_exp_f32 v130, v130 clamp\n"
    "v_mfma_f32_16x16x32_bf16 v[72:75], a[164:167], a[124:127], v[72:75]\n"
    "ds_read_b128 a[196:199], v2 offset:11264\n"
    "buffer_store_dwordx4 v[116:119], v0, s[4:7], s42 offen offset:0 sc1\n"
    "v_max_f32 v66, v176, v66\n"
    "s_waitcnt lgkmcnt(4)\n"
    "v_mfma_f32_16x16x32_bf16 v[60:63], a[168:171], a[48:51], v[60:63]\n"
    "v_exp_f32 v131, v131 clamp\n"
    "v_max_f32 v67, v176, v67\n"
    "v_mfma_f32_16x16x32_bf16 v[72:75], a[168:171], a[128:131], v[72:75]\n"
    "v_exp_f32 v132, v132 clamp\n"
    "v_mfma_f32_16x16x32_bf16 v[60:63], a[172:175], a[52:55], v[60:63]\n"
    "buffer_load_dwordx4 v0, s[4:7], s81 offen offset:2048 sc1 lds\n"
    "buffer_store_dwordx4 v[116:119], v0, s[4:7], s42 offen offset:1024 sc1\n"
    "v_max_f32 v76, v176, v76\n"
    "v_mfma_f32_16x16x32_bf16 v[72:75], a[172:175], a[132:135], v[72:75]\n"
    "v_exp_f32 v133, v133 clamp\n"
    "v_mfma_f32_16x16x32_bf16 v[60:63], a[176:179], a[56:59], v[60:63]\n"
    "v_max_f32 v77, v176, v77\n"
    "buffer_store_dwordx4 v[116:119], v0, s[4:7], s42 offen offset:2048 sc1\n"
    "v_mfma_f32_16x16x32_bf16 v[72:75], a[176:179], a[136:139], v[72:75]\n"
    "v_exp_f32 v134, v134 clamp\n"
    "v_mfma_f32_16x16x32_bf16 v[60:63], a[180:183], a[60:63], v[60:63]\n"
    "buffer_load_dwordx4 v0, s[4:7], s81 offen offset:3072 sc1 lds\n"
    "s_add_u32 m0, m0, 4096\n"
    "v_max_f32 v78, v176, v78\n"
    "v_exp_f32 v135, v135 clamp\n"
    "v_mfma_f32_16x16x32_bf16 v[72:75], a[180:183], a[140:143], v[72:75]\n"
    "v_max_f32 v79, v176, v79\n"
    "buffer_store_dwordx4 v[116:119], v0, s[4:7], s42 offen offset:3072 sc1\n"
    "s_waitcnt lgkmcnt(3)\n"
    "v_mfma_f32_16x16x32_bf16 v[60:63], a[184:187], a[64:67], v[60:63]\n"
    "v_add_f32 v64, v64, v128\n"
    "v_add_f32 v65, v65, v129\n"
    "v_mfma_f32_16x16x32_bf16 v[72:75], a[184:187], a[144:147], v[72:75]\n"
    "buffer_load_dwordx4 v240, s[4:7], s81 offen sc1 lds\n"
    "v_add_f32 v66, v66, v130\n"
    "s_waitcnt lgkmcnt(2)\n"
    "v_mfma_f32_16x16x32_bf16 v[60:63], a[188:191], a[68:71], v[60:63]\n"
    "buffer_store_dwordx4 v[116:119], v0, s[4:7], s43 offen offset:0 sc1\n"
    "v_add_f32 v67, v67, v131\n"
    "v_mfma_f32_16x16x32_bf16 v[72:75], a[188:191], a[148:151], v[72:75]\n"
    "v_add_f32 v76, v76, v132\n"
    "v_add_f32 v77, v77, v133\n"
    "s_waitcnt lgkmcnt(1)\n"
    "v_mfma_f32_16x16x32_bf16 v[60:63], a[192:195], a[72:75], v[60:63]\n"
    "v_add_f32 v78, v78, v134\n"
    "buffer_store_dwordx4 v[116:119], v0, s[4:7], s43 offen offset:1024 sc1\n"
    "v_mfma_f32_16x16x32_bf16 v[72:75], a[192:195], a[152:155], v[72:75]\n"
    "v_add_f32 v79, v79, v135\n"
    "v_cvt_pk_bf16_f32 v136, v64, v65\n"
    "s_waitcnt lgkmcnt(0)\n"
    "v_mfma_f32_16x16x32_bf16 v[60:63], a[196:199], a[76:79], v[60:63]\n"
    "v_cvt_pk_bf16_f32 v137, v66, v67\n"
    "v_cvt_pk_bf16_f32 v138, v76, v77\n"
    "v_mfma_f32_16x16x32_bf16 v[72:75], a[196:199], a[156:159], v[72:75]\n"
    "v_cvt_pk_bf16_f32 v139, v78, v79\n"
    "s_load_dwordx16 s[36:51], s[10:11], 256\n"
    "s_load_dwordx16 s[52:67], s[10:11], 320\n"
    "s_load_dwordx2 s[12:13], %0, 40\n"
    "s_nop 0\n"
    "s_waitcnt vmcnt(25)\n"
    "v_mfma_f32_16x16x32_bf16 v[48:51], v[108:111], v[16:19], 0\n"
    "v_fma_f32 v60, v60, v148, v176\n"
    "v_fma_f32 v61, v61, v148, v176\n"
    "s_waitcnt vmcnt(23)\n"
    "v_mfma_f32_16x16x32_bf16 v[48:51], v[108:111], v[20:23], v[48:51]\n"
    "v_fma_f32 v62, v62, v148, v176\n"
    "v_fma_f32 v63, v63, v148, v176\n"
    "s_waitcnt vmcnt(21)\n"
    "v_mfma_f32_16x16x32_bf16 v[48:51], v[108:111], v[24:27], v[48:51]\n"
    "v_fma_f32 v72, v72, v149, v176\n"
    "v_fma_f32 v73, v73, v149, v176\n"
    "s_waitcnt vmcnt(19)\n"
    "v_mfma_f32_16x16x32_bf16 v[48:51], v[108:111], v[28:31], v[48:51]\n"
    "v_fma_f32 v74, v74, v149, v176\n"
    "s_waitcnt vmcnt(17)\n"
    "v_mfma_f32_16x16x32_bf16 v[48:51], v[108:111], v[32:35], v[48:51]\n"
    "v_fma_f32 v75, v75, v149, v176\n"
    "v_fma_f32 v128, v60, v172, v174\n"
    "s_waitcnt vmcnt(15)\n"
    "v_mfma_f32_16x16x32_bf16 v[48:51], v[108:111], v[36:39], v[48:51]\n"
    "v_fma_f32 v129, v61, v172, v174\n"
    "v_fma_f32 v130, v62, v172, v174\n"
    "s_waitcnt vmcnt(13)\n"
    "v_mfma_f32_16x16x32_bf16 v[48:51], v[108:111], v[40:43], v[48:51]\n"
    "v_fma_f32 v131, v63, v172, v174\n"
    "v_fma_f32 v132, v72, v172, v174\n"
    "s_waitcnt vmcnt(11)\n"
    "v_mfma_f32_16x16x32_bf16 v[48:51], v[108:111], v[44:47], v[48:51]\n"
    "v_fma_f32 v133, v73, v172, v174\n"
    "v_fma_f32 v134, v74, v172, v174\n"
    "v_fma_f32 v135, v75, v172, v174\n"
    "v_exp_f32 v128, v128 clamp\n"
    "v_max_f32 v60, v176, v60\n"
    "v_exp_f32 v129, v129 clamp\n"
    "v_max_f32 v61, v176, v61\n"
    "s_nop 0\n"
    "v_cmp_u_f32 vcc, v48, v48\n"
    "s_cbranch_vccnz slabs_9_%=\n"
    "slabr_10_%=:\n"
    "v_cvt_pk_bf16_f32 v48, v48, v49\n"
    "v_cvt_pk_bf16_f32 v49, v50, v51\n"
    "ds_write_b64 v80, v[48:49]\n"
    "s_mov_b32 s32, 32768\n"
    "buffer_load_dwordx4 v[220:223], v0, s[4:7], s32 offen sc1\n"
    "v_exp_f32 v130, v130 clamp\n"
    "v_max_f32 v62, v176, v62\n"
    "v_exp_f32 v131, v131 clamp\n"
    "v_max_f32 v63, v176, v63\n"
    "v_exp_f32 v132, v132 clamp\n"
    "v_max_f32 v72, v176, v72\n"
    "v_exp_f32 v133, v133 clamp\n"
    "v_max_f32 v73, v176, v73\n"
    "s_waitcnt lgkmcnt(0)\n"
    "s_barrier\n"
    "s_lshl_b32 s21, %2, 12\n"
    "ds_read_b64 v[52:53], v89 offset:64\n"
    "ds_read_b64 v[44:45], v85 offset:64\n"
    "ds_read_b64 v[46:47], v85 offset:448\n"
    "ds_read_b64 v[20:21], v87 offset:64\n"
    "ds_read_b64 v[22:23], v87 offset:448\n"
    "ds_read_b128 v[40:43], v88 offset:0\n"
    "ds_read_b128 v[24:27], v84 offset:0\n"
    "ds_read_b128 v[28:31], v84 offset:384\n"
    "ds_read_b128 v[32:35], v86 offset:0\n"
    "ds_read_b128 v[36:39], v86 offset:384\n"
    "v_exp_f32 v134, v134 clamp\n"
    "v_max_f32 v74, v176, v74\n"
    "v_exp_f32 v135, v135 clamp\n"
    "v_max_f32 v75, v176, v75\n"
    "v_add_f32 v60, v60, v128\n"
    "v_add_f32 v61, v61, v129\n"
    "s_waitcnt lgkmcnt(6)\n"
    "v_mfma_f32_16x16x16_bf16 v[196:199], v[52:53], v[136:137], v[120:123]\n"
    "v_add_f32 v62, v62, v130\n"
    "v_add_f32 v63, v63, v131\n"
    "v_mfma_f32_16x16x16_bf16 v[180:183], v[44:45], v[136:137], 0\n"
    "v_add_f32 v72, v72, v132\n"
    "v_mfma_f32_16x16x16_bf16 v[184:187], v[46:47], v[136:137], 0\n"
    "v_add_f32 v73, v73, v133\n"
    "v_mfma_f32_16x16x16_bf16 v[188:191], v[20:21], v[136:137], 0\n"
    "v_add_f32 v74, v74, v134\n"
    "s_waitcnt lgkmcnt(3)\n"
    "v_mfma_f32_16x16x16_bf16 v[192:195], v[22:23], v[136:137], 0\n"
    "v_add_f32 v75, v75, v135\n"
    "v_cvt_pk_bf16_f32 v94, v60, v61\n"
    "v_mfma_f32_16x16x16_bf16 v[140:143], v[52:53], v[138:139], v[120:123]\n"
    "v_cvt_pk_bf16_f32 v95, v62, v63\n"
    "v_mfma_f32_16x16x16_bf16 v[200:203], v[44:45], v[138:139], 0\n"
    "v_cvt_pk_bf16_f32 v98, v72, v73\n"
    "v_mfma_f32_16x16x16_bf16 v[100:103], v[46:47], v[138:139], 0\n"
    "v_cvt_pk_bf16_f32 v99, v74, v75\n"
    "v_mfma_f32_16x16x16_bf16 v[104:107], v[20:21], v[138:139], 0\n"
    "v_mfma_f32_16x16x16_bf16 v[116:119], v[22:23], v[138:139], 0\n"
    "v_mfma_f32_16x16x32_bf16 v[196:199], v[40:43], v[92:95], v[196:199]\n"
    "v_mfma_f32_16x16x32_bf16 v[180:183], v[24:27], v[92:95], v[180:183]\n"
    "s_waitcnt lgkmcnt(2)\n"
    "v_mfma_f32_16x16x32_bf16 v[184:187], v[28:31], v[92:95], v[184:187]\n"
    "s_waitcnt lgkmcnt(1)\n"
    "v_mfma_f32_16x16x32_bf16 v[188:191], v[32:35], v[92:95], v[188:191]\n"
    "s_waitcnt lgkmcnt(0)\n"
    "v_mfma_f32_16x16x32_bf16 v[192:195], v[36:39], v[92:95], v[192:195]\n"
    "v_mfma_f32_16x16x32_bf16 v[140:143], v[40:43], v[96:99], v[140:143]\n"
    "v_mfma_f32_16x16x32_bf16 v[200:203], v[24:27], v[96:99], v[200:203]\n"
    "v_mfma_f32_16x16x32_bf16 v[100:103], v[28:31], v[96:99], v[100:103]\n"
    "s_nop 0\n"
    "v_rcp_f32 v196, v196\n"
    "v_rcp_f32 v197, v197\n"
    "v_mfma_f32_16x16x32_bf16 v[104:107], v[32:35], v[96:99], v[104:107]\n"
    "v_mul_f32 v180, v180, v196\n"
    "v_mul_f32 v181, v181, v196\n"
    "v_mfma_f32_16x16x32_bf16 v[116:119], v[36:39], v[96:99], v[116:119]\n"
    "v_mul_f32 v182, v182, v196\n"
    "v_mul_f32 v183, v183, v196\n"
    "v_cvt_pk_bf16_f32 v56, v180, v181\n"
    "v_cvt_pk_bf16_f32 v57, v182, v183\n"
    "v_pk_mul_f32 v[184:185], v[184:185], v[196:197] op_sel:[0,1] op_sel_hi:[1,1]\n"
    "v_pk_mul_f32 v[186:187], v[186:187], v[196:197] op_sel:[0,1] op_sel_hi:[1,1]\n"
    "v_cvt_pk_bf16_f32 v58, v184, v185\n"
    "v_cvt_pk_bf16_f32 v59, v186, v187\n"
    "buffer_store_dwordx4 v[56:59], v0, s[4:7], s75 offen sc1\n"
    "v_pk_mul_f32 v[188:189], v[188:189], v[196:197] op_sel:[0,1] op_sel_hi:[1,1]\n"
    "v_pk_mul_f32 v[190:191], v[190:191], v[196:197] op_sel:[0,1] op_sel_hi:[1,1]\n"
    "v_cvt_pk_bf16_f32 v60, v188, v189\n"
    "v_cvt_pk_bf16_f32 v61, v190, v191\n"
    "v_pk_mul_f32 v[192:193], v[192:193], v[196:197] op_sel:[0,1] op_sel_hi:[1,1]\n"
    "v_pk_mul_f32 v[194:195], v[194:195], v[196:197] op_sel:[0,1] op_sel_hi:[1,1]\n"
    "v_cvt_pk_bf16_f32 v62, v192, v193\n"
    "v_cvt_pk_bf16_f32 v63, v194, v195\n"
    "buffer_store_dwordx4 v[60:63], v90, s[4:7], s76 offen sc1\n"
    "s_add_u32 s75, s75, 20480\n"
    "s_add_u32 s76, s76, 20480\n"
    "v_rcp_f32 v140, v140\n"
    "v_rcp_f32 v141, v141\n"
    "s_nop 0\n"
    "v_pk_mul_f32 v[200:201], v[200:201], v[140:141] op_sel:[0,0] op_sel_hi:[1,0]\n"
    "v_pk_mul_f32 v[202:203], v[202:203], v[140:141] op_sel:[0,0] op_sel_hi:[1,0]\n"
    "v_cvt_pk_bf16_f32 v64, v200, v201\n"
    "v_cvt_pk_bf16_f32 v65, v202, v203\n"
    "v_pk_mul_f32 v[100:101], v[100:101], v[140:141] op_sel:[0,1] op_sel_hi:[1,1]\n"
    "v_pk_mul_f32 v[102:103], v[102:103], v[140:141] op_sel:[0,1] op_sel_hi:[1,1]\n"
    "v_cvt_pk_bf16_f32 v66, v100, v101\n"
    "v_cvt_pk_bf16_f32 v67, v102, v103\n"
    "buffer_store_dwordx4 v[64:67], v0, s[4:7], s75 offen sc1\n"
    "v_pk_mul_f32 v[104:105], v[104:105], v[140:141] op_sel:[0,1] op_sel_hi:[1,1]\n"
    "v_pk_mul_f32 v[106:107], v[106:107], v[140:141] op_sel:[0,1] op_sel_hi:[1,1]\n"
    "v_cvt_pk_bf16_f32 v68, v104, v105\n"
    "v_cvt_pk_bf16_f32 v69, v106, v107\n"
    "v_pk_mul_f32 v[116:117], v[116:117], v[140:141] op_sel:[0,1] op_sel_hi:[1,1]\n"
    "v_pk_mul_f32 v[118:119], v[118:119], v[140:141] op_sel:[0,1] op_sel_hi:[1,1]\n"
    "v_cvt_pk_bf16_f32 v70, v116, v117\n"
    "v_cvt_pk_bf16_f32 v71, v118, v119\n"
    "buffer_store_dwordx4 v[68:71], v90, s[4:7], s76 offen sc1\n"
    "s_waitcnt vmcnt(4)\n"
    "v_xor_b32 v220, s29, v220\n"
    "v_xor_b32 v221, s29, v221\n"
    "v_xor_b32 v222, s29, v222\n"
    "v_xor_b32 v223, s29, v223\n"
    "v_or3_b32 v220, v220, v221, v222\n"
    "v_or_b32 v220, v220, v223\n"
    "v_cmp_ne_u32 vcc, 0, v220\n"
    "s_cbranch_vccnz pf2s_7_%=\n"
    "pf2r_8_%=:\n"
    "ds_read_b128 a[0:3], v0 offset:61440\n"
    "s_add_u32 m0, s21, 102400\n"
    "v_lshlrev_b32 v16, 16, v244\n"
    "ds_read_b128 a[4:7], v0 offset:62464\n"
    "buffer_load_dwordx4 v0, s[4:7], s92 offen offset:0 sc1 lds\n"
    "v_and_b32 v17, 0xffff0000, v244\n"
    "ds_read_b128 a[8:11], v0 offset:63488\n"
    "ds_read_b128 a[12:15], v0 offset:64512\n"
    "buffer_load_dwordx4 v0, s[4:7], s92 offen offset:1024 sc1 lds\n"
    "v_lshlrev_b32 v18, 16, v245\n"
    "ds_read_b128 a[16:19], v1 offset:0\n"
    "ds_read_b128 a[20:23], v1 offset:1024\n"
    "buffer_load_dwordx4 v0, s[4:7], s92 offen offset:2048 sc1 lds\n"
    "v_and_b32 v19, 0xffff0000, v245\n"
    "ds_read_b128 a[24:27], v1 offset:2048\n"
    "v_lshlrev_b32 v20, 16, v246\n"
    "ds_read_b128 a[28:31], v1 offset:3072\n"
    "buffer_load_dwordx4 v0, s[4:7], s92 offen offset:3072 sc1 lds\n"
    "ds_read_b128 a[32:35], v1 offset:4096\n"
    "v_and_b32 v21, 0xffff0000, v246\n"
    "ds_read_b128 a[36:39], v1 offset:5120\n"
    "s_add_u32 m0, s21, 118784\n"
    "ds_read_b128 a[40:43], v1 offset:6144\n"
    "v_lshlrev_b32 v22, 16, v247\n"
    "ds_read_b128 a[44:47], v1 offset:7168\n"
    "buffer_load_dwordx4 v0, s[4:7], s93 offen offset:0 sc1 lds\n"
    "v_and_b32 v23, 0xffff0000, v247\n"
    "ds_read_b128 a[48:51], v1 offset:8192\n"
    "ds_read_b128 a[52:55], v1 offset:9216\n"
    "buffer_load_dwordx4 v0, s[4:7], s93 offen offset:1024 sc1 lds\n"
    "v_lshlrev_b32 v24, 16, v248\n"
    "ds_read_b128 a[56:59], v1 offset:10240\n"
    "ds_read_b128 a[60:63], v1 offset:11264\n"
    "buffer_load_dwordx4 v0, s[4:7], s93 offen offset:2048 sc1 lds\n"
    "v_and_b32 v25, 0xffff0000, v248\n"
    "ds_read_b128 a[64:67], v1 offset:12288\n"
    "v_lshlrev_b32 v26, 16, v249\n"
    "ds_read_b128 a[68:71], v1 offset:13312\n"
    "buffer_load_dwordx4 v0, s[4:7], s93 offen offset:3072 sc1 lds\n"
    "ds_read_b128 a[72:75], v1 offset:14336\n"
    "v_and_b32 v27, 0xffff0000, v249\n"
    "ds_read_b128 a[76:79], v1 offset:15360\n"
    "s_add_u32 m0, s21, 135168\n"
    "ds_read_b128 a[80:83], v1 offset:16384\n"
    "v_lshlrev_b32 v28, 16, v250\n"
    "ds_read_b128 a[84:87], v1 offset:17408\n"
    "buffer_load_dwordx4 v0, s[4:7], s94 offen offset:0 sc1 lds\n"
    "v_and_b32 v29, 0xffff0000, v250\n"
    "ds_read_b128 a[88:91], v1 offset:18432\n"
    "ds_read_b128 a[92:95], v1 offset:19456\n"
    "buffer_load_dwordx4 v0, s[4:7], s94 offen offset:1024 sc1 lds\n"
    "v_lshlrev_b32 v30, 16, v251\n"
    "ds_read_b128 v[176:179], v1 offset:20480\n"
    "ds_read_b128 v[180:183], v1 offset:21504\n"
    "buffer_load_dwordx4 v0, s[4:7], s94 offen offset:2048 sc1 lds\n"
    "v_and_b32 v31, 0xffff0000, v251\n"
    "ds_read_b128 v[184:187], v1 offset:22528\n"
    "buffer_load_dwordx4 v0, s[4:7], s94 offen offset:3072 sc1 lds\n"
    "v_lshlrev_b32 v32, 16, v252\n"
    "ds_read_b128 v[188:191], v1 offset:23552\n"
    "ds_read_b128 v[192:195], v1 offset:24576\n"
    "s_lshl_b32 s31, %2, 11\n"
    "s_add_u32 m0, s31, 151552\n"
    "v_and_b32 v33, 0xffff0000, v252\n"
    "ds_read_b128 v[196:199], v1 offset:25600\n"
    "ds_read_b128 v[200:203], v1 offset:26624\n"
    "buffer_load_dwordx4 v0, s[4:7], s49 offen offset:0 sc1 lds\n"
    "v_lshlrev_b32 v34, 16, v253\n"
    "ds_read_b128 v[204:207], v1 offset:27648\n"
    "v_and_b32 v35, 0xffff0000, v253\n"
    "ds_read_b128 v[208:211], v1 offset:28672\n"
    "buffer_load_dwordx4 v0, s[4:7], s50 offen offset:1024 sc1 lds\n"
    "ds_read_b128 v[212:215], v1 offset:29696\n"
    "s_and_b32 s13, s13, 0xffff\n"
    "ds_read_b128 v[216:219], v1 offset:30720\n"
    "buffer_load_dwordx4 a[96:99], v0, s[4:7], s97 offen offset:0 sc1\n"
    "ds_read_b128 v[36:39], v1 offset:31744\n"
    "v_lshrrev_b32 v12, 5, v3\n"
    "ds_read_b128 v[40:43], v1 offset:32768\n"
    "buffer_load_dwordx4 a[100:103], v0, s[4:7], s97 offen offset:1024 sc1\n"
    "v_lshl_add_u32 v12, v12, 30, v0\n"
    "ds_read_b128 v[44:47], v1 offset:33792\n"
    "ds_read_b128 v[48:51], v1 offset:34816\n"
    "buffer_load_dwordx4 a[104:107], v0, s[4:7], s97 offen offset:2048 sc1\n"
    "v_lshlrev_b32 v13, 6, v4\n"
    "ds_read_b128 v[52:55], v1 offset:35840\n"
    "ds_read_b128 v[56:59], v0 offset:0\n"
    "buffer_load_dwordx4 a[108:111], v0, s[4:7], s97 offen offset:3072 sc1\n"
    "v_lshl_add_u32 v13, v5, 28, v13\n"
    "ds_read_b128 v[60:63], v0 offset:1024\n"
    "v_lshrrev_b32 v14, 1, v5\n"
    "ds_read_b128 v[64:67], v0 offset:2048\n"
    "buffer_load_dwordx4 a[112:115], v240, s[4:7], s97 offen offset:0 sc1\n"
    "s_add_u32 s97, s97, 5120\n"
    "s_cmp_eq_u32 s97, s98\n"
    "s_cselect_b32 s97, s20, s97\n"
    "ds_read_b128 v[68:71], v0 offset:3072\n"
    "v_lshl_add_u32 v14, v14, 4, v4\n"
    "ds_read_b128 v[72:75], v0 offset:4096\n"
    "buffer_load_dwordx4 a[116:119], v0, s[4:7], s97 offen offset:0 sc1\n"
    "ds_read_b128 v[76:79], v0 offset:5120\n"
    "v_lshlrev_b32 v14, 1, v14\n"
    "ds_read_b128 v[80:83], v0 offset:6144\n"
    "buffer_load_dwordx4 a[120:123], v0, s[4:7], s97 offen offset:1024 sc1\n"
    "v_and_or_b32 v14, v5, 1, v14\n"
    "ds_read_b128 v[84:87], v0 offset:7168\n"
    "ds_read_b128 v[88:91], v0 offset:8192\n"
    "buffer_load_dwordx4 a[124:127], v0, s[4:7], s97 offen offset:2048 sc1\n"
    "v_lshlrev_b32 v14, 3, v14\n"
    "ds_read_b128 v[92:95], v0 offset:9216\n"
    "buffer_load_dwordx4 v[96:99], v0, s[4:7], s84 offen offset:0 sc1\n"
    "buffer_load_dwordx4 v[100:103], v0, s[4:7], s84 offen offset:1024 sc1\n"
    "buffer_load_dwordx4 v[104:107], v0, s[4:7], s84 offen offset:2048 sc1\n"
    "buffer_load_dwordx4 v[108:111], v0, s[4:7], s84 offen offset:3072 sc1\n"
    "buffer_load_dwordx4 v[112:115], v0, s[4:7], s85 offen offset:0 sc1\n"
    "buffer_load_dwordx4 v[116:119], v0, s[4:7], s85 offen offset:1024 sc1\n"
    "buffer_load_dwordx4 v[120:123], v0, s[4:7], s85 offen offset:2048 sc1\n"
    "buffer_load_dwordx4 v[124:127], v0, s[4:7], s85 offen offset:3072 sc1\n"
    "buffer_load_dwordx4 v[128:131], v0, s[4:7], s86 offen offset:0 sc1\n"
    "buffer_load_dwordx4 v[132:135], v0, s[4:7], s86 offen offset:1024 sc1\n"
    "buffer_load_dwordx4 v[136:139], v0, s[4:7], s86 offen offset:2048 sc1\n"
    "buffer_load_dwordx4 v[140:143], v0, s[4:7], s86 offen offset:3072 sc1\n"
    "s_waitcnt lgkmcnt(0)\n"
    "s_barrier\n"
    "buffer_store_dword v9, v6, s[4:7], s46 offen offset:0 sc1\n"
    "s_waitcnt vmcnt(9)\n"
    "v_max3_u32 v7, v108, v100, v104\n"
    "v_max_u32 v7, v7, v96\n"
    "v_cmp_eq_u32 vcc, -1, v7\n"
    "s_cbranch_vccnz o0s_11_%=\n"
    "o0r_12_%=:\n"
    "v_mfma_f32_16x16x32_bf16 v[16:19], a[0:3], v[96:99], v[16:19]\n"
    "ds_read_b128 a[0:3], v0 offset:10240\n"
    "buffer_load_dwordx4 v[144:147], v0, s[4:7], s87 offen offset:0 sc1\n"
    "v_mfma_f32_16x16x32_bf16 v[20:23], a[4:7], v[96:99], v[20:23]\n"
    "ds_read_b128 a[4:7], v0 offset:11264\n"
    "v_mfma_f32_16x16x32_bf16 v[24:27], a[8:11], v[96:99], v[24:27]\n"
    "ds_read_b128 a[8:11], v0 offset:12288\n"
    "buffer_load_dwordx4 v[148:151], v0, s[4:7], s87 offen offset:1024 sc1\n"
    "v_mfma_f32_16x16x32_bf16 v[28:31], a[12:15], v[96:99], v[28:31]\n"
    "v_mfma_f32_16x16x32_bf16 v[32:35], a[16:19], v[96:99], v[32:35]\n"
    "buffer_load_dwordx4 v[152:155], v0, s[4:7], s87 offen offset:2048 sc1\n"
    "v_mfma_f32_16x16x32_bf16 v[16:19], a[20:23], v[100:103], v[16:19]\n"
    "ds_read_b128 a[12:15], v0 offset:13312\n"
    "v_mfma_f32_16x16x32_bf16 v[20:23], a[24:27], v[100:103], v[20:23]\n"
    "ds_read_b128 a[16:19], v0 offset:14336\n"
    "buffer_load_dwordx4 v[156:159], v0, s[4:7], s87 offen offset:3072 sc1\n"
    "v_mfma_f32_16x16x32_bf16 v[24:27], a[28:31], v[100:103], v[24:27]\n"
    "ds_read_b128 a[20:23], v0 offset:15360\n"
    "v_mfma_f32_16x16x32_bf16 v[28:31], a[32:35], v[100:103], v[28:31]\n"
    "buffer_load_dwordx4 v[160:163], v0, s[4:7], s88 offen offset:0 sc1\n"
    "v_mfma_f32_16x16x32_bf16 v[32:35], a[36:39], v[100:103], v[32:35]\n"
    "v_mfma_f32_16x16x32_bf16 v[16:19], a[40:43], v[104:107], v[16:19]\n"
    "ds_read_b128 a[24:27], v0 offset:16384\n"
    "buffer_load_dwordx4 v[164:167], v0, s[4:7], s88 offen offset:1024 sc1\n"
    "v_mfma_f32_16x16x32_bf16 v[20:23], a[44:47], v[104:107], v[20:23]\n"
    "ds_read_b128 a[28:31], v0 offset:17408\n"
    "v_mfma_f32_16x16x32_bf16 v[24:27], a[48:51], v[104:107], v[24:27]\n"
    "ds_read_b128 a[32:35], v0 offset:18432\n"
    "buffer_load_dwordx4 v[168:171], v0, s[4:7], s88 offen offset:2048 sc1\n"
    "v_mfma_f32_16x16x32_bf16 v[28:31], a[52:55], v[104:107], v[28:31]\n"
    "v_mfma_f32_16x16x32_bf16 v[32:35], a[56:59], v[104:107], v[32:35]\n"
    "buffer_load_dwordx4 v[172:175], v0, s[4:7], s88 offen offset:3072 sc1\n"
    "v_mfma_f32_16x16x32_bf16 v[16:19], a[60:63], v[108:111], v[16:19]\n"
    "ds_read_b128 a[36:39], v0 offset:19456\n"
    "v_mfma_f32_16x16x32_bf16 v[20:23], a[64:67], v[108:111], v[20:23]\n"
    "ds_read_b128 a[40:43], v0 offset:20480\n"
    "s_add_u32 m0, s21, 61440\n"
    "v_mfma_f32_16x16x32_bf16 v[24:27], a[68:71], v[108:111], v[24:27]\n"
    "ds_read_b128 a[44:47], v0 offset:21504\n"
    "s_waitcnt vmcnt(13)\n"
    "v_max3_u32 v7, v124, v116, v120\n"
    "v_mfma_f32_16x16x32_bf16 v[28:31], a[72:75], v[108:111], v[28:31]\n"
    "v_max_u32 v7, v7, v112\n"
    "buffer_load_dwordx4 v0, s[4:7], s95 offen offset:0 sc1 lds\n"
    "v_mfma_f32_16x16x32_bf16 v[32:35], a[76:79], v[108:111], v[32:35]\n"
    "v_cmp_eq_u32 vcc, -1, v7\n"
    "s_cbranch_vccnz o1s_13_%=\n"
    "o1r_14_%=:\n"
    "v_mfma_f32_16x16x32_bf16 v[16:19], a[80:83], v[112:115], v[16:19]\n"
    "ds_read_b128 a[48:51], v0 offset:22528\n"
    "v_mfma_f32_16x16x32_bf16 v[20:23], a[84:87], v[112:115], v[20:23]\n"
    "ds_read_b128 a[52:55], v0 offset:23552\n"
    "buffer_load_dwordx4 v0, s[4:7], s95 offen offset:1024 sc1 lds\n"
    "v_mfma_f32_16x16x32_bf16 v[24:27], a[88:91], v[112:115], v[24:27]\n"
    "ds_read_b128 a[56:59], v0 offset:24576\n"
    "v_mfma_f32_16x16x32_bf16 v[28:31], a[92:95], v[112:115], v[28:31]\n"
    "v_mfma_f32_16x16x32_bf16 v[32:35], v[176:179], v[112:115], v[32:35]\n"
    "buffer_load_dwordx4 v0, s[4:7], s95 offen offset:2048 sc1 lds\n"
    "v_mfma_f32_16x16x32_bf16 v[16:19], v[180:183], v[116:119], v[16:19]\n"
    "ds_read_b128 a[60:63], v0 offset:25600\n"
    "v_mfma_f32_16x16x32_bf16 v[20:23], v[184:187], v[116:119], v[20:23]\n"
    "ds_read_b128 a[64:67], v0 offset:26624\n"
    "buffer_load_dwordx4 v0, s[4:7], s95 offen offset:3072 sc1 lds\n"
    "v_mfma_f32_16x16x32_bf16 v[24:27], v[188:191], v[116:119], v[24:27]\n"
    "ds_read_b128 a[68:71], v0 offset:27648\n"
    "v_mfma_f32_16x16x32_bf16 v[28:31], v[192:195], v[116:119], v[28:31]\n"
    "v_mfma_f32_16x16x32_bf16 v[32:35], v[196:199], v[116:119], v[32:35]\n"
    "s_add_u32 m0, s21, 77824\n"
    "v_mfma_f32_16x16x32_bf16 v[16:19], v[200:203], v[120:123], v[16:19]\n"
    "ds_read_b128 a[72:75], v0 offset:28672\n"
    "v_mfma_f32_16x16x32_bf16 v[20:23], v[204:207], v[120:123], v[20:23]\n"
    "ds_read_b128 a[76:79], v0 offset:29696\n"
    "v_mfma_f32_16x16x32_bf16 v[24:27], v[208:211], v[120:123], v[24:27]\n"
    "ds_read_b128 a[80:83], v0 offset:30720\n"
    "buffer_load_dwordx4 v0, s[4:7], s96 offen offset:0 sc1 lds\n"
    "v_mfma_f32_16x16x32_bf16 v[28:31], v[212:215], v[120:123], v[28:31]\n"
    "v_mfma_f32_16x16x32_bf16 v[32:35], v[216:219], v[120:123], v[32:35]\n"
    "buffer_load_dwordx4 v0, s[4:7], s96 offen offset:1024 sc1 lds\n"
    "v_mfma_f32_16x16x32_bf16 v[16:19], v[36:39], v[124:127], v[16:19]\n"
    "ds_read_b128 a[84:87], v0 offset:31744\n"
    "v_mfma_f32_16x16x32_bf16 v[20:23], v[40:43], v[124:127], v[20:23]\n"
    "ds_read_b128 a[88:91], v0 offset:32768\n"
    "v_mfma_f32_16x16x32_bf16 v[24:27], v[44:47], v[124:127], v[24:27]\n"
    "ds_read_b128 a[92:95], v0 offset:33792\n"
    "s_waitcnt vmcnt(15)\n"
    "v_max3_u32 v7, v140, v132, v136\n"
    "v_mfma_f32_16x16x32_bf16 v[28:31], v[48:51], v[124:127], v[28:31]\n"
    "v_max_u32 v7, v7, v128\n"
    "buffer_load_dwordx4 v0, s[4:7], s96 offen offset:2048 sc1 lds\n"
    "v_mfma_f32_16x16x32_bf16 v[32:35], v[52:55], v[124:127], v[32:35]\n"
    "v_cmp_eq_u32 vcc, -1, v7\n"
    "s_cbranch_vccnz o2s_15_%=\n"
    "o2r_16_%=:\n"
    "v_mfma_f32_16x16x32_bf16 v[16:19], v[56:59], v[128:131], v[16:19]\n"
    "ds_read_b128 v[176:179], v0 offset:34816\n"
    "buffer_load_dwordx4 v0, s[4:7], s96 offen offset:3072 sc1 lds\n"
    "v_mfma_f32_16x16x32_bf16 v[20:23], v[60:63], v[128:131], v[20:23]\n"
    "ds_read_b128 v[180:183], v0 offset:35840\n"
    "v_mfma_f32_16x16x32_bf16 v[24:27], v[64:67], v[128:131], v[24:27]\n"
    "ds_read_b128 v[184:187], v0 offset:36864\n"
    "v_mfma_f32_16x16x32_bf16 v[28:31], v[68:71], v[128:131], v[28:31]\n"
    "buffer_load_dwordx4 a[128:131], v0, s[4:7], s97 offen offset:3072 sc1\n"
    "v_mfma_f32_16x16x32_bf16 v[32:35], v[72:75], v[128:131], v[32:35]\n"
    "v_mfma_f32_16x16x32_bf16 v[16:19], v[76:79], v[132:135], v[16:19]\n"
    "ds_read_b128 v[188:191], v0 offset:37888\n"
    "v_mfma_f32_16x16x32_bf16 v[20:23], v[80:83], v[132:135], v[20:23]\n"
    "ds_read_b128 v[192:195], v0 offset:38912\n"
    "v_mfma_f32_16x16x32_bf16 v[24:27], v[84:87], v[132:135], v[24:27]\n"
    "ds_read_b128 v[196:199], v0 offset:39936\n"
    "v_mfma_f32_16x16x32_bf16 v[28:31], v[88:91], v[132:135], v[28:31]\n"
    "buffer_load_dwordx4 a[132:135], v240, s[4:7], s97 offen offset:0 sc1\n"
    "s_add_u32 s97, s97, 5120\n"
    "s_cmp_eq_u32 s97, s98\n"
    "s_cselect_b32 s97, s20, s97\n"
    "v_mfma_f32_16x16x32_bf16 v[32:35], v[92:95], v[132:135], v[32:35]\n"
    "buffer_load_dwordx4 a[136:139], v0, s[4:7], s97 offen offset:0 sc1\n"
    "s_waitcnt lgkmcnt(15)\n"
    "v_mfma_f32_16x16x32_bf16 v[16:19], a[0:3], v[136:139], v[16:19]\n"
    "ds_read_b128 v[200:203], v0 offset:40960\n"
    "v_mfma_f32_16x16x32_bf16 v[20:23], a[4:7], v[136:139], v[20:23]\n"
    "ds_read_b128 v[204:207], v0 offset:41984\n"
    "buffer_load_dwordx4 a[140:143], v0, s[4:7], s97 offen offset:1024 sc1\n"
    "v_mfma_f32_16x16x32_bf16 v[24:27], a[8:11], v[136:139], v[24:27]\n"
    "ds_read_b128 v[208:211], v0 offset:43008\n"
    "v_mfma_f32_16x16x32_bf16 v[28:31], a[12:15], v[136:139], v[28:31]\n"
    "buffer_load_dwordx4 a[144:147], v0, s[4:7], s97 offen offset:2048 sc1\n"
    "s_waitcnt lgkmcnt(15)\n"
    "v_mfma_f32_16x16x32_bf16 v[32:35], a[16:19], v[136:139], v[32:35]\n"
    "v_mfma_f32_16x16x32_bf16 v[16:19], a[20:23], v[140:143], v[16:19]\n"
    "ds_read_b128 v[212:215], v0 offset:44032\n"
    "v_mfma_f32_16x16x32_bf16 v[20:23], a[24:27], v[140:143], v[20:23]\n"
    "ds_read_b128 v[216:219], v0 offset:45056\n"
    "buffer_load_dwordx4 a[148:151], v0, s[4:7], s97 offen offset:3072 sc1\n"
    "v_mfma_f32_16x16x32_bf16 v[24:27], a[28:31], v[140:143], v[24:27]\n"
    "ds_read_b128 v[36:39], v0 offset:46080\n"
    "s_waitcnt vmcnt(18)\n"
    "v_max3_u32 v7, v156, v148, v152\n"
    "s_waitcnt lgkmcnt(15)\n"
    "v_mfma_f32_16x16x32_bf16 v[28:31], a[32:35], v[140:143], v[28:31]\n"
    "v_max_u32 v7, v7, v144\n"
    "v_mfma_f32_16x16x32_bf16 v[32:35], a[36:39], v[140:143], v[32:35]\n"
    "v_cmp_eq_u32 vcc, -1, v7\n"
    "s_cbranch_vccnz o3s_17_%=\n"
    "o3r_18_%=:\n"
    "v_mfma_f32_16x16x32_bf16 v[16:19], a[40:43], v[144:147], v[16:19]\n"
    "ds_read_b128 v[40:43], v0 offset:47104\n"
    "v_mfma_f32_16x16x32_bf16 v[20:23], a[44:47], v[144:147], v[20:23]\n"
    "ds_read_b128 v[44:47], v0 offset:48128\n"
    "s_waitcnt lgkmcnt(15)\n"
    "v_mfma_f32_16x16x32_bf16 v[24:27], a[48:51], v[144:147], v[24:27]\n"
    "ds_read_b128 v[48:51], v0 offset:49152\n"
    "buffer_load_dwordx4 a[152:155], v240, s[4:7], s97 offen offset:0 sc1\n"
    "s_add_u32 s97, s97, 5120\n"
    "s_cmp_eq_u32 s97, s98\n"
    "s_cselect_b32 s97, s20, s97\n"
    "v_mfma_f32_16x16x32_bf16 v[28:31], a[52:55], v[144:147], v[28:31]\n"
    "buffer_load_dwordx4 a[156:159], v0, s[4:7], s97 offen offset:0 sc1\n"
    "v_mfma_f32_16x16x32_bf16 v[32:35], a[56:59], v[144:147], v[32:35]\n"
    "buffer_load_dwordx4 a[160:163], v0, s[4:7], s97 offen offset:1024 sc1\n"
    "v_mfma_f32_16x16x32_bf16 v[16:19], a[60:63], v[148:151], v[16:19]\n"
    "ds_read_b128 v[52:55], v0 offset:50176\n"
    "s_waitcnt lgkmcnt(15)\n"
    "v_mfma_f32_16x16x32_bf16 v[20:23], a[64:67], v[148:151], v[20:23]\n"
    "ds_read_b128 v[56:59], v0 offset:51200\n"
    "v_mfma_f32_16x16x32_bf16 v[24:27], a[68:71], v[148:151], v[24:27]\n"
    "ds_read_b128 v[60:63], v0 offset:52224\n"
    "buffer_load_dwordx4 a[164:167], v0, s[4:7], s97 offen offset:2048 sc1\n"
    "v_mfma_f32_16x16x32_bf16 v[28:31], a[72:75], v[148:151], v[28:31]\n"
    "v_mfma_f32_16x16x32_bf16 v[32:35], a[76:79], v[148:151], v[32:35]\n"
    "s_waitcnt lgkmcnt(15)\n"
    "v_mfma_f32_16x16x32_bf16 v[16:19], a[80:83], v[152:155], v[16:19]\n"
    "ds_read_b128 v[64:67], v0 offset:53248\n"
    "v_mfma_f32_16x16x32_bf16 v[20:23], a[84:87], v[152:155], v[20:23]\n"
    "ds_read_b128 v[68:71], v0 offset:54272\n"
    "v_mfma_f32_16x16x32_bf16 v[24:27], a[88:91], v[152:155], v[24:27]\n"
    "ds_read_b128 v[72:75], v0 offset:55296\n"
    "v_mfma_f32_16x16x32_bf16 v[28:31], a[92:95], v[152:155], v[28:31]\n"
    "s_waitcnt lgkmcnt(15)\n"
    "v_mfma_f32_16x16x32_bf16 v[32:35], v[176:179], v[152:155], v[32:35]\n"
    "v_mfma_f32_16x16x32_bf16 v[16:19], v[180:183], v[156:159], v[16:19]\n"
    "ds_read_b128 v[76:79], v0 offset:56320\n"
    "v_mfma_f32_16x16x32_bf16 v[20:23], v[184:187], v[156:159], v[20:23]\n"
    "ds_read_b128 v[80:83], v0 offset:57344\n"
    "v_mfma_f32_16x16x32_bf16 v[24:27], v[188:191], v[156:159], v[24:27]\n"
    "ds_read_b128 v[84:87], v0 offset:58368\n"
    "s_waitcnt vmcnt(18)\n"
    "v_max3_u32 v7, v172, v164, v168\n"
    "s_waitcnt lgkmcnt(15)\n"
    "v_mfma_f32_16x16x32_bf16 v[28:31], v[192:195], v[156:159], v[28:31]\n"
    "v_max_u32 v7, v7, v160\n"
    "v_mfma_f32_16x16x32_bf16 v[32:35], v[196:199], v[156:159], v[32:35]\n"
    "v_cmp_eq_u32 vcc, -1, v7\n"
    "s_cbranch_vccnz o4s_19_%=\n"
    "o4r_20_%=:\n"
    "v_mfma_f32_16x16x32_bf16 v[16:19], v[200:203], v[160:163], v[16:19]\n"
    "ds_read_b128 v[88:91], v0 offset:59392\n"
    "v_mfma_f32_16x16x32_bf16 v[20:23], v[204:207], v[160:163], v[20:23]\n"
    "ds_read_b128 v[92:95], v0 offset:60416\n"
    "s_waitcnt lgkmcnt(14)\n"
    "v_mfma_f32_16x16x32_bf16 v[24:27], v[208:211], v[160:163], v[24:27]\n"
    "v_mfma_f32_16x16x32_bf16 v[28:31], v[212:215], v[160:163], v[28:31]\n"
    "v_mfma_f32_16x16x32_bf16 v[32:35], v[216:219], v[160:163], v[32:35]\n"
    "v_mfma_f32_16x16x32_bf16 v[16:19], v[36:39], v[164:167], v[16:19]\n"
    "s_waitcnt lgkmcnt(6)\n"
    "v_mfma_f32_16x16x32_bf16 v[16:19], v[56:59], v[168:171], v[16:19]\n"
    "s_waitcnt lgkmcnt(3)\n"
    "v_mfma_f32_16x16x32_bf16 v[16:19], v[76:79], v[172:175], v[16:19]\n"
    "v_mfma_f32_16x16x32_bf16 v[20:23], v[40:43], v[164:167], v[20:23]\n"
    "v_mfma_f32_16x16x32_bf16 v[20:23], v[60:63], v[168:171], v[20:23]\n"
    "v_mfma_f32_16x16x32_bf16 v[20:23], v[80:83], v[172:175], v[20:23]\n"
    "v_mfma_f32_16x16x32_bf16 v[24:27], v[44:47], v[164:167], v[24:27]\n"
    "v_mfma_f32_16x16x32_bf16 v[24:27], v[64:67], v[168:171], v[24:27]\n"
    "s_nop 2\n"
    "v_cvt_pk_bf16_f32 v224, v16, v17\n"
    "v_cvt_pk_bf16_f32 v225, v18, v19\n"
    "s_waitcnt lgkmcnt(2)\n"
    "v_mfma_f32_16x16x32_bf16 v[24:27], v[84:87], v[172:175], v[24:27]\n"
    "v_mfma_f32_16x16x32_bf16 v[28:31], v[48:51], v[164:167], v[28:31]\n"
    "v_cvt_pk_bf16_f32 v226, v20, v21\n"
    "v_cvt_pk_bf16_f32 v227, v22, v23\n"
    "v_mfma_f32_16x16x32_bf16 v[28:31], v[68:71], v[168:171], v[28:31]\n"
    "buffer_store_dwordx4 v[224:227], v0, s[4:7], s89 offen offset:0 sc1\n"
    "s_waitcnt lgkmcnt(1)\n"
    "v_mfma_f32_16x16x32_bf16 v[28:31], v[88:91], v[172:175], v[28:31]\n"
    "v_mfma_f32_16x16x32_bf16 v[32:35], v[52:55], v[164:167], v[32:35]\n"
    "s_nop 0\n"
    "v_cvt_pk_bf16_f32 v228, v24, v25\n"
    "v_cvt_pk_bf16_f32 v229, v26, v27\n"
    "v_mfma_f32_16x16x32_bf16 v[32:35], v[72:75], v[168:171], v[32:35]\n"
    "s_waitcnt lgkmcnt(0)\n"
    "v_mfma_f32_16x16x32_bf16 v[32:35], v[92:95], v[172:175], v[32:35]\n"
    "s_nop 1\n"
    "v_cvt_pk_bf16_f32 v230, v28, v29\n"
    "v_cvt_pk_bf16_f32 v231, v30, v31\n"
    "buffer_store_dwordx4 v[228:231], v0, s[4:7], s89 offen offset:1024 sc1\n"
    "s_nop 2\n"
    "v_cvt_pk_bf16_f32 v232, v32, v33\n"
    "v_cvt_pk_bf16_f32 v233, v34, v35\n"
    "v_mov_b32 v254, v232\n"
    "v_mov_b32 v255, v233\n"
    "v_permlane32_swap_b32 v232, v234\n"
    "v_permlane32_swap_b32 v233, v235\n"
    "buffer_store_dwordx4 v[232:235], v12, s[4:7], s90 offen sc1\n"
    "v_pk_add_f32 v[236:237], v[16:17], v[18:19]\n"
    "v_pk_mul_f32 v[238:239], v[16:17], v[16:17]\n"
    "v_pk_fma_f32 v[238:239], v[18:19], v[18:19], v[238:239]\n"
    "v_pk_add_f32 v[236:237], v[236:237], v[20:21]\n"
    "v_pk_fma_f32 v[238:239], v[20:21], v[20:21], v[238:239]\n"
    "v_pk_add_f32 v[236:237], v[236:237], v[22:23]\n"
    "v_pk_fma_f32 v[238:239], v[22:23], v[22:23], v[238:239]\n"
    "v_pk_add_f32 v[236:237], v[236:237], v[24:25]\n"
    "v_pk_fma_f32 v[238:239], v[24:25], v[24:25], v[238:239]\n"
    "v_pk_add_f32 v[236:237], v[236:237], v[26:27]\n"
    "v_pk_fma_f32 v[238:239], v[26:27], v[26:27], v[238:239]\n"
    "v_pk_add_f32 v[236:237], v[236:237], v[28:29]\n"
    "v_pk_fma_f32 v[238:239], v[28:29], v[28:29], v[238:239]\n"
    "v_pk_add_f32 v[236:237], v[236:237], v[30:31]\n"
    "v_pk_fma_f32 v[238:239], v[30:31], v[30:31], v[238:239]\n"
    "v_pk_add_f32 v[236:237], v[236:237], v[32:33]\n"
    "v_pk_fma_f32 v[238:239], v[32:33], v[32:33], v[238:239]\n"
    "v_pk_add_f32 v[236:237], v[236:237], v[34:35]\n"
    "v_pk_fma_f32 v[238:239], v[34:35], v[34:35], v[238:239]\n"
    "v_add_f32 v236, v236, v237\n"
    "v_add_f32 v237, v238, v239\n"
    "s_nop 1\n"
    "v_permlane16_swap_b32 v236, v237\n"
    "v_add_f32 v236, v236, v237\n"
    "v_mov_b32 v237, v236\n"
    "s_nop 1\n"
    "v_permlane32_swap_b32 v236, v237\n"
    "v_add_f32 v236, v236, v237\n"
    "v_mov_b32 v237, v236\n"
    "s_nop 1\n"
    "v_permlane16_swap_b32 v236, v237\n"
    "buffer_store_dwordx2 v[236:237], v13, s[4:7], s91 offen sc1\n"
    "s_barrier\n"
    "s_add_u32 m0, s83, 0\n"
    "s_nop 0\n"
    "buffer_load_dwordx4 v240, s[4:7], s92 offen offset:0 sc1 lds\n"
    "buffer_load_dwordx4 v240, s[4:7], s92 offen offset:1024 sc1 lds\n"
    "buffer_load_dwordx4 v241, s[4:7], s92 offen offset:2048 sc1 lds\n"
    "s_add_u32 m0, s83, 12288\n"
    "s_nop 0\n"
    "buffer_load_dwordx4 v240, s[4:7], s93 offen offset:0 sc1 lds\n"
    "buffer_load_dwordx4 v240, s[4:7], s93 offen offset:1024 sc1 lds\n"
    "buffer_load_dwordx4 v241, s[4:7], s93 offen offset:2048 sc1 lds\n"
    "s_add_u32 m0, s83, 24576\n"
    "s_nop 0\n"
    "buffer_load_dwordx4 v240, s[4:7], s94 offen offset:0 sc1 lds\n"
    "buffer_load_dwordx4 v240, s[4:7], s94 offen offset:1024 sc1 lds\n"
    "buffer_load_dwordx4 v241, s[4:7], s94 offen offset:2048 sc1 lds\n"
    "v_mov_b32 v246, v224\n"
    "v_mov_b32 v247, v225\n"
    "v_mov_b32 v248, v226\n"
    "v_mov_b32 v249, v227\n"
    "v_mov_b32 v250, v228\n"
    "v_mov_b32 v251, v229\n"
    "v_mov_b32 v252, v230\n"
    "v_mov_b32 v253, v231\n"
    "buffer_store_dword v9, v6, s[4:7], s48 offen offset:0 sc1\n"
    "buffer_load_dwordx4 a[168:171], v0, s[4:7], s97 offen offset:3072 sc1\n"
    "buffer_load_dwordx4 a[172:175], v240, s[4:7], s97 offen offset:0 sc1\n"
    "s_add_u32 s97, s97, 5120\n"
    "s_cmp_eq_u32 s97, s98\n"
    "s_cselect_b32 s97, s20, s97\n"
    "buffer_load_dwordx4 a[176:179], v0, s[4:7], s97 offen offset:0 sc1\n"
    "buffer_load_dwordx4 a[180:183], v0, s[4:7], s97 offen offset:1024 sc1\n"
    "buffer_load_dwordx4 a[184:187], v0, s[4:7], s97 offen offset:2048 sc1\n"
    "buffer_load_dwordx4 a[188:191], v0, s[4:7], s97 offen offset:3072 sc1\n"
    "buffer_load_dwordx4 a[192:195], v240, s[4:7], s97 offen offset:0 sc1\n"
    "s_add_u32 s97, s97, 5120\n"
    "s_cmp_eq_u32 s97, s98\n"
    "s_cselect_b32 s97, s20, s97\n"
    "buffer_load_dwordx4 a[196:199], v0, s[4:7], s97 offen offset:0 sc1\n"
    "buffer_load_dwordx4 a[200:203], v0, s[4:7], s97 offen offset:1024 sc1\n"
    "buffer_load_dwordx4 a[204:207], v0, s[4:7], s97 offen offset:2048 sc1\n"
    "s_mov_b32 s22, 0xbfb8aa3b\n"
    "s_mov_b32 s23, 0xbfb8aa3b\n"
    "v_lshlrev_b32 v78, 6, v4\n"
    "v_lshl_add_u32 v78, v5, 4, v78\n"
    "v_add_u32 v89, 21504, v78\n"
    "v_add_u32 v88, 21504, v0\n"
    "ds_read_b128 v[176:179], v1 offset:36864\n"
    "ds_read_b128 v[180:183], v1 offset:37888\n"
    "ds_read_b128 v[184:187], v1 offset:38912\n"
    "ds_read_b128 v[188:191], v1 offset:39936\n"
    "ds_read_b128 v[192:195], v1 offset:40960\n"
    "ds_read_b128 v[196:199], v1 offset:41984\n"
    "ds_read_b128 v[200:203], v1 offset:43008\n"
    "buffer_load_dwordx4 v[96:99], v0, s[4:7], s36 offen offset:0 sc1\n"
    "buffer_load_dwordx4 v[100:103], v0, s[4:7], s36 offen offset:1024 sc1\n"
    "buffer_load_dwordx4 v[104:107], v0, s[4:7], s36 offen offset:2048 sc1\n"
    "buffer_load_dwordx4 v[108:111], v0, s[4:7], s36 offen offset:3072 sc1\n"
    "buffer_load_dwordx4 v[112:115], v0, s[4:7], s37 offen offset:0 sc1\n"
    "buffer_load_dwordx4 v[116:119], v0, s[4:7], s37 offen offset:1024 sc1\n"
    "buffer_load_dwordx4 v[120:123], v0, s[4:7], s37 offen offset:2048 sc1\n"
    "buffer_load_dwordx4 v[124:127], v0, s[4:7], s37 offen offset:3072 sc1\n"
    "buffer_load_dwordx4 v[128:131], v0, s[4:7], s38 offen offset:0 sc1\n"
    "buffer_load_dwordx4 v[132:135], v0, s[4:7], s38 offen offset:1024 sc1\n"
    "buffer_load_dwordx4 v[136:139], v0, s[4:7], s38 offen offset:2048 sc1\n"
    "buffer_load_dwordx4 v[140:143], v0, s[4:7], s38 offen offset:3072 sc1\n"
    "buffer_load_dwordx4 v[80:83], v78, s[4:7], s41 offen sc1\n"
    "buffer_load_dwordx4 v[84:87], v89, s[4:7], s41 offen sc1\n"
    "s_waitcnt vmcnt(10)\n"
    "v_max3_u32 v7, v108, v100, v104\n"
    "v_max_u32 v7, v7, v96\n"
    "v_cmp_eq_u32 vcc, -1, v7\n"
    "s_cbranch_vccnz h0s_21_%=\n"
    "h0r_22_%=:\n"
    "s_waitcnt lgkmcnt(3)\n"
    "v_mfma_f32_16x16x32_bf16 v[16:19], v[176:179], v[96:99], 0\n"
    "ds_read_b128 v[204:207], v1 offset:44032\n"
    "v_mfma_f32_16x16x32_bf16 v[20:23], v[180:183], v[96:99], 0\n"
    "ds_read_b128 v[176:179], v1 offset:45056\n"
    "buffer_load_dwordx4 v[144:147], v0, s[4:7], s39 offen offset:0 sc1\n"
    "v_mfma_f32_16x16x32_bf16 v[24:27], v[184:187], v[96:99], 0\n"
    "ds_read_b128 v[180:183], v1 offset:46080\n"
    "s_mul_i32 s24, s47, 0xffff\n"
    "v_mfma_f32_16x16x32_bf16 v[28:31], v[188:191], v[96:99], 0\n"
    "ds_read_b128 v[184:187], v1 offset:47104\n"
    "buffer_load_dwordx4 v[148:151], v0, s[4:7], s39 offen offset:1024 sc1\n"
    "s_waitcnt lgkmcnt(3)\n"
    "v_mfma_f32_16x16x32_bf16 v[16:19], v[192:195], v[100:103], v[16:19]\n"
    "ds_read_b128 v[188:191], v1 offset:48128\n"
    "s_add_u32 s25, s44, 57344\n"
    "v_mfma_f32_16x16x32_bf16 v[20:23], v[196:199], v[100:103], v[20:23]\n"
    "ds_read_b128 v[192:195], v1 offset:49152\n"
    "buffer_load_dwordx4 v[152:155], v0, s[4:7], s39 offen offset:2048 sc1\n"
    "v_mfma_f32_16x16x32_bf16 v[24:27], v[200:203], v[100:103], v[24:27]\n"
    "ds_read_b128 v[196:199], v1 offset:50176\n"
    "s_add_u32 s26, s45, 57344\n"
    "v_mfma_f32_16x16x32_bf16 v[28:31], v[204:207], v[100:103], v[28:31]\n"
    "ds_read_b128 v[200:203], v1 offset:51200\n"
    "buffer_load_dwordx4 v[156:159], v0, s[4:7], s39 offen offset:3072 sc1\n"
    "s_waitcnt lgkmcnt(3)\n"
    "v_mfma_f32_16x16x32_bf16 v[16:19], v[176:179], v[104:107], v[16:19]\n"
    "ds_read_b128 v[204:207], v1 offset:52224\n"
    "v_add_u32 v90, s44, v0\n"
    "v_mfma_f32_16x16x32_bf16 v[20:23], v[180:183], v[104:107], v[20:23]\n"
    "ds_read_b128 v[176:179], v1 offset:53248\n"
    "buffer_load_dwordx4 v[160:163], v0, s[4:7], s40 offen offset:0 sc1\n"
    "v_mfma_f32_16x16x32_bf16 v[24:27], v[184:187], v[104:107], v[24:27]\n"
    "ds_read_b128 v[180:183], v1 offset:54272\n"
    "v_add_u32 v91, s45, v0\n"
    "v_mfma_f32_16x16x32_bf16 v[28:31], v[188:191], v[104:107], v[28:31]\n"
    "ds_read_b128 v[184:187], v1 offset:55296\n"
    "buffer_load_dwordx4 v[164:167], v0, s[4:7], s40 offen offset:1024 sc1\n"
    "s_waitcnt lgkmcnt(3)\n"
    "v_mfma_f32_16x16x32_bf16 v[16:19], v[192:195], v[108:111], v[16:19]\n"
    "ds_read_b128 v[188:191], v1 offset:56320\n"
    "v_mfma_f32_16x16x32_bf16 v[20:23], v[196:199], v[108:111], v[20:23]\n"
    "ds_read_b128 v[192:195], v1 offset:57344\n"
    "s_waitcnt vmcnt(12)\n"
    "v_max3_u32 v7, v124, v116, v120\n"
    "v_mfma_f32_16x16x32_bf16 v[24:27], v[200:203], v[108:111], v[24:27]\n"
    "ds_read_b128 v[196:199], v1 offset:58368\n"
    "v_max_u32 v7, v7, v112\n"
    "v_mfma_f32_16x16x32_bf16 v[28:31], v[204:207], v[108:111], v[28:31]\n"
    "ds_read_b128 v[200:203], v1 offset:59392\n"
    "v_cmp_eq_u32 vcc, -1, v7\n"
    "s_cbranch_vccnz h1s_23_%=\n"
    "h1r_24_%=:\n"
    "s_waitcnt lgkmcnt(3)\n"
    "v_mfma_f32_16x16x32_bf16 v[16:19], v[176:179], v[112:115], v[16:19]\n"
    "ds_read_b128 v[204:207], v1 offset:60416\n"
    "buffer_load_dwordx4 v[168:171], v0, s[4:7], s40 offen offset:2048 sc1\n"
    "buffer_load_dwordx4 v[172:175], v0, s[4:7], s40 offen offset:3072 sc1\n"
    "v_mfma_f32_16x16x32_bf16 v[20:23], v[180:183], v[112:115], v[20:23]\n"
    "ds_read_b128 v[176:179], v1 offset:61440\n"
    "s_add_u32 m0, s83, 36864\n"
    "v_mfma_f32_16x16x32_bf16 v[24:27], v[184:187], v[112:115], v[24:27]\n"
    "ds_read_b128 v[180:183], v1 offset:62464\n"
    "v_mfma_f32_16x16x32_bf16 v[28:31], v[188:191], v[112:115], v[28:31]\n"
    "ds_read_b128 v[184:187], v1 offset:63488\n"
    "buffer_load_dwordx4 v240, s[4:7], s95 offen offset:0 sc1 lds\n"
    "s_waitcnt lgkmcnt(3)\n"
    "v_mfma_f32_16x16x32_bf16 v[16:19], v[192:195], v[116:119], v[16:19]\n"
    "ds_read_b128 v[188:191], v1 offset:64512\n"
    "s_mov_b32 s32, 32768\n"
    "buffer_load_dwordx4 v[92:95], v0, s[4:7], s32 offen offset:1024 sc1\n"
    "v_mfma_f32_16x16x32_bf16 v[20:23], v[196:199], v[116:119], v[20:23]\n"
    "ds_read_b128 v[192:195], v2 offset:0\n"
    "buffer_load_dwordx4 v240, s[4:7], s95 offen offset:1024 sc1 lds\n"
    "v_mfma_f32_16x16x32_bf16 v[24:27], v[200:203], v[116:119], v[24:27]\n"
    "ds_read_b128 v[196:199], v2 offset:1024\n"
    "v_mfma_f32_16x16x32_bf16 v[28:31], v[204:207], v[116:119], v[28:31]\n"
    "ds_read_b128 v[200:203], v2 offset:2048\n"
    "s_waitcnt lgkmcnt(3)\n"
    "v_mfma_f32_16x16x32_bf16 v[16:19], v[176:179], v[120:123], v[16:19]\n"
    "ds_read_b128 v[204:207], v2 offset:3072\n"
    "buffer_load_dwordx4 v241, s[4:7], s95 offen offset:2048 sc1 lds\n"
    "v_mfma_f32_16x16x32_bf16 v[20:23], v[180:183], v[120:123], v[20:23]\n"
    "ds_read_b128 v[176:179], v2 offset:4096\n"
    "v_mfma_f32_16x16x32_bf16 v[24:27], v[184:187], v[120:123], v[24:27]\n"
    "ds_read_b128 v[180:183], v2 offset:5120\n"
    "s_add_u32 m0, s83, 49152\n"
    "v_mfma_f32_16x16x32_bf16 v[28:31], v[188:191], v[120:123], v[28:31]\n"
    "ds_read_b128 v[184:187], v2 offset:6144\n"
    "s_waitcnt lgkmcnt(3)\n"
    "v_mfma_f32_16x16x32_bf16 v[16:19], v[192:195], v[124:127], v[16:19]\n"
    "ds_read_b128 v[188:191], v2 offset:7168\n"
    "buffer_load_dwordx4 v240, s[4:7], s96 offen offset:0 sc1 lds\n"
    "v_mfma_f32_16x16x32_bf16 v[20:23], v[196:199], v[124:127], v[20:23]\n"
    "ds_read_b128 v[192:195], v2 offset:8192\n"
    "s_waitcnt vmcnt(15)\n"
    "v_max3_u32 v7, v140, v132, v136\n"
    "v_mfma_f32_16x16x32_bf16 v[24:27], v[200:203], v[124:127], v[24:27]\n"
    "ds_read_b128 v[196:199], v2 offset:9216\n"
    "v_max_u32 v7, v7, v128\n"
    "v_mfma_f32_16x16x32_bf16 v[28:31], v[204:207], v[124:127], v[28:31]\n"
    "ds_read_b128 v[200:203], v2 offset:10240\n"
    "v_cmp_eq_u32 vcc, -1, v7\n"
    "s_cbranch_vccnz h2s_25_%=\n"
    "h2r_26_%=:\n"
    "s_waitcnt lgkmcnt(3)\n"
    "v_mfma_f32_16x16x32_bf16 v[16:19], v[176:179], v[128:131], v[16:19]\n"
    "ds_read_b128 v[204:207], v2 offset:11264\n"
    "buffer_load_dwordx4 v240, s[4:7], s96 offen offset:1024 sc1 lds\n"
    "v_mfma_f32_16x16x32_bf16 v[20:23], v[180:183], v[128:131], v[20:23]\n"
    "ds_read_b128 v[176:179], v2 offset:12288\n"
    "buffer_load_dwordx4 v241, s[4:7], s96 offen offset:2048 sc1 lds\n"
    "v_mfma_f32_16x16x32_bf16 v[24:27], v[184:187], v[128:131], v[24:27]\n"
    "ds_read_b128 v[180:183], v2 offset:13312\n"
    "v_mfma_f32_16x16x32_bf16 v[28:31], v[188:191], v[128:131], v[28:31]\n"
    "ds_read_b128 v[184:187], v2 offset:14336\n"
    "s_waitcnt lgkmcnt(3)\n"
    "v_mfma_f32_16x16x32_bf16 v[16:19], v[192:195], v[132:135], v[16:19]\n"
    "ds_read_b128 v[188:191], v2 offset:15360\n"
    "buffer_load_dwordx4 a[208:211], v0, s[4:7], s97 offen offset:3072 sc1\n"
    "v_mfma_f32_16x16x32_bf16 v[20:23], v[196:199], v[132:135], v[20:23]\n"
    "ds_read_b128 v[192:195], v2 offset:16384\n"
    "s_waitcnt vmcnt(17)\n"
    "v_add_f32 v80, v80, v82\n"
    "v_add_f32 v81, v81, v83\n"
    "s_waitcnt vmcnt(16)\n"
    "v_add_f32 v84, v84, v86\n"
    "v_add_f32 v85, v85, v87\n"
    "v_cmp_u_f32 vcc, v80, v84\n"
    "s_cbranch_vccnz sts_41_%=\n"
    "str_42_%=:\n"
    "v_mfma_f32_16x16x32_bf16 v[24:27], v[200:203], v[132:135], v[24:27]\n"
    "ds_read_b128 v[196:199], v2 offset:17408\n"
    "v_permlane16_swap_b32 v80, v81\n"
    "v_mfma_f32_16x16x32_bf16 v[28:31], v[204:207], v[132:135], v[28:31]\n"
    "ds_read_b128 v[200:203], v2 offset:18432\n"
    "v_add_f32 v80, v80, v81\n"
    "s_waitcnt lgkmcnt(3)\n"
    "v_mfma_f32_16x16x32_bf16 v[16:19], v[176:179], v[136:139], v[16:19]\n"
    "ds_read_b128 v[204:207], v2 offset:19456\n"
    "v_mov_b32 v81, v80\n"
    "v_mfma_f32_16x16x32_bf16 v[20:23], v[180:183], v[136:139], v[20:23]\n"
    "s_barrier\n"
    "ds_read_b128 v[176:179], v0 offset:61440\n"
    "v_permlane32_swap_b32 v80, v81\n"
    "v_mfma_f32_16x16x32_bf16 v[24:27], v[184:187], v[136:139], v[24:27]\n"
    "ds_read_b128 v[180:183], v0 offset:62464\n"
    "v_add_f32 v80, v80, v81\n"
    "v_mfma_f32_16x16x32_bf16 v[28:31], v[188:191], v[136:139], v[28:31]\n"
    "ds_read_b128 v[184:187], v0 offset:63488\n"
    "buffer_load_dwordx4 a[212:215], v240, s[4:7], s97 offen offset:0 sc1\n"
    "s_add_u32 s97, s97, 5120\n"
    "s_cmp_eq_u32 s97, s98\n"
    "s_cselect_b32 s97, s20, s97\n"
    "s_waitcnt lgkmcnt(3)\n"
    "v_mfma_f32_16x16x32_bf16 v[16:19], v[192:195], v[140:143], v[16:19]\n"
    "ds_read_b128 v[188:191], v0 offset:64512\n"
    "v_mov_b32 v81, v80\n"
    "v_mfma_f32_16x16x32_bf16 v[20:23], v[196:199], v[140:143], v[20:23]\n"
    "ds_read_b128 v[192:195], v1 offset:0\n"
    "s_waitcnt vmcnt(13)\n"
    "v_max3_u32 v7, v156, v148, v152\n"
    "v_permlane16_swap_b32 v80, v81\n"
    "v_mfma_f32_16x16x32_bf16 v[24:27], v[200:203], v[140:143], v[24:27]\n"
    "ds_read_b128 v[196:199], v1 offset:1024\n"
    "v_max_u32 v7, v7, v144\n"
    "v_mul_f32 v80, 0xbacccccd, v80\n"
    "v_mfma_f32_16x16x32_bf16 v[28:31], v[204:207], v[140:143], v[28:31]\n"
    "ds_read_b128 v[200:203], v1 offset:2048\n"
    "v_cmp_eq_u32 vcc, -1, v7\n"
    "s_cbranch_vccnz h3s_27_%=\n"
    "h3r_28_%=:\n"
    "v_mul_f32 v81, 0x3acccccd, v81\n"
    "s_waitcnt lgkmcnt(3)\n"
    "v_mfma_f32_16x16x32_bf16 v[16:19], v[176:179], v[144:147], v[16:19]\n"
    "ds_read_b128 v[204:207], v1 offset:3072\n"
    "v_fma_f32 v81, -v80, v80, v81\n"
    "buffer_load_dwordx4 a[216:219], v0, s[4:7], s97 offen offset:0 sc1\n"
    "v_mfma_f32_16x16x32_bf16 v[20:23], v[180:183], v[144:147], v[20:23]\n"
    "ds_read_b128 v[176:179], v1 offset:4096\n"
    "v_max_f32 v81, 0, v81\n"
    "v_mfma_f32_16x16x32_bf16 v[24:27], v[184:187], v[144:147], v[24:27]\n"
    "ds_read_b128 v[180:183], v1 offset:5120\n"
    "v_add_f32 v81, 0x3727c5ac, v81\n"
    "v_mfma_f32_16x16x32_bf16 v[28:31], v[188:191], v[144:147], v[28:31]\n"
    "ds_read_b128 v[184:187], v1 offset:6144\n"
    "buffer_load_dwordx4 a[220:223], v0, s[4:7], s97 offen offset:1024 sc1\n"
    "s_waitcnt lgkmcnt(3)\n"
    "v_mfma_f32_16x16x32_bf16 v[16:19], v[192:195], v[148:151], v[16:19]\n"
    "ds_read_b128 v[188:191], v1 offset:7168\n"
    "v_rsq_f32 v81, v81\n"
    "v_mfma_f32_16x16x32_bf16 v[20:23], v[196:199], v[148:151], v[20:23]\n"
    "ds_read_b128 v[192:195], v1 offset:8192\n"
    "v_cvt_pk_bf16_f32 v72, v80, v80\n"
    "v_mfma_f32_16x16x32_bf16 v[24:27], v[200:203], v[148:151], v[24:27]\n"
    "ds_read_b128 v[196:199], v1 offset:9216\n"
    "v_mul_f32 v76, s22, v81\n"
    "v_mfma_f32_16x16x32_bf16 v[28:31], v[204:207], v[148:151], v[28:31]\n"
    "ds_read_b128 v[200:203], v1 offset:10240\n"
    "v_lshlrev_b32 v83, 16, v72\n"
    "s_waitcnt lgkmcnt(3)\n"
    "v_mfma_f32_16x16x32_bf16 v[16:19], v[176:179], v[152:155], v[16:19]\n"
    "ds_read_b128 v[204:207], v1 offset:11264\n"
    "v_sub_f32 v83, v80, v83\n"
    "v_mfma_f32_16x16x32_bf16 v[20:23], v[180:183], v[152:155], v[20:23]\n"
    "ds_read_b128 v[176:179], v1 offset:12288\n"
    "v_cvt_pk_bf16_f32 v73, v83, v83\n"
    "v_mfma_f32_16x16x32_bf16 v[24:27], v[184:187], v[152:155], v[24:27]\n"
    "ds_read_b128 v[180:183], v1 offset:13312\n"
    "buffer_load_dwordx4 a[224:227], v0, s[4:7], s97 offen offset:2048 sc1\n"
    "v_mfma_f32_16x16x32_bf16 v[28:31], v[188:191], v[152:155], v[28:31]\n"
    "ds_read_b128 v[184:187], v1 offset:14336\n"
    "v_permlane16_swap_b32 v84, v85\n"
    "s_waitcnt lgkmcnt(3)\n"
    "v_mfma_f32_16x16x32_bf16 v[16:19], v[192:195], v[156:159], v[16:19]\n"
    "ds_read_b128 v[188:191], v1 offset:15360\n"
    "v_add_f32 v84, v84, v85\n"
    "v_mfma_f32_16x16x32_bf16 v[20:23], v[196:199], v[156:159], v[20:23]\n"
    "ds_read_b128 v[192:195], v1 offset:16384\n"
    "s_waitcnt vmcnt(12)\n"
    "v_max3_u32 v7, v172, v164, v168\n"
    "v_mov_b32 v85, v84\n"
    "v_mfma_f32_16x16x32_bf16 v[24:27], v[200:203], v[156:159], v[24:27]\n"
    "ds_read_b128 v[196:199], v1 offset:17408\n"
    "v_max_u32 v7, v7, v160\n"
    "v_mfma_f32_16x16x32_bf16 v[28:31], v[204:207], v[156:159], v[28:31]\n"
    "ds_read_b128 v[200:203], v1 offset:18432\n"
    "v_cmp_eq_u32 vcc, -1, v7\n"
    "s_cbranch_vccnz h4s_29_%=\n"
    "h4r_30_%=:\n"
    "v_permlane32_swap_b32 v84, v85\n"
    "buffer_load_dwordx4 a[228:231], v0, s[4:7], s97 offen offset:3072 sc1\n"
    "s_waitcnt lgkmcnt(3)\n"
    "v_mfma_f32_16x16x32_bf16 v[16:19], v[176:179], v[160:163], v[16:19]\n"
    "ds_read_b128 v[204:207], v1 offset:19456\n"
    "v_add_f32 v84, v84, v85\n"
    "v_mfma_f32_16x16x32_bf16 v[20:23], v[180:183], v[160:163], v[20:23]\n"
    "ds_read_b128 v[176:179], v1 offset:20480\n"
    "v_mov_b32 v85, v84\n"
    "v_mfma_f32_16x16x32_bf16 v[24:27], v[184:187], v[160:163], v[24:27]\n"
    "ds_read_b128 v[180:183], v1 offset:21504\n"
    "v_permlane16_swap_b32 v84, v85\n"
    "v_mfma_f32_16x16x32_bf16 v[28:31], v[188:191], v[160:163], v[28:31]\n"
    "ds_read_b128 v[184:187], v1 offset:22528\n"
    "v_mul_f32 v84, 0xbacccccd, v84\n"
    "buffer_load_dwordx4 a[232:235], v240, s[4:7], s97 offen offset:0 sc1\n"
    "s_add_u32 s97, s97, 5120\n"
    "s_cmp_eq_u32 s97, s98\n"
    "s_cselect_b32 s97, s20, s97\n"
    "s_waitcnt lgkmcnt(3)\n"
    "v_mfma_f32_16x16x32_bf16 v[16:19], v[192:195], v[164:167], v[16:19]\n"
    "ds_read_b128 v[188:191], v1 offset:23552\n"
    "v_mul_f32 v85, 0x3acccccd, v85\n"
    "v_mfma_f32_16x16x32_bf16 v[20:23], v[196:199], v[164:167], v[20:23]\n"
    "ds_read_b128 v[192:195], v1 offset:24576\n"
    "v_fma_f32 v85, -v84, v84, v85\n"
    "v_mfma_f32_16x16x32_bf16 v[24:27], v[200:203], v[164:167], v[24:27]\n"
    "ds_read_b128 v[196:199], v1 offset:25600\n"
    "v_max_f32 v85, 0, v85\n"
    "v_mfma_f32_16x16x32_bf16 v[28:31], v[204:207], v[164:167], v[28:31]\n"
    "ds_read_b128 v[200:203], v1 offset:26624\n"
    "v_add_f32 v85, 0x3727c5ac, v85\n"
    "s_waitcnt lgkmcnt(3)\n"
    "v_mfma_f32_16x16x32_bf16 v[16:19], v[176:179], v[168:171], v[16:19]\n"
    "ds_read_b128 v[204:207], v1 offset:27648\n"
    "buffer_load_dwordx4 a[236:239], v0, s[4:7], s97 offen offset:0 sc1\n"
    "v_mfma_f32_16x16x32_bf16 v[20:23], v[180:183], v[168:171], v[20:23]\n"
    "ds_read_b64 v[176:177], v2 offset:20480\n"
    "v_rsq_f32 v85, v85\n"
    "v_mfma_f32_16x16x32_bf16 v[24:27], v[184:187], v[168:171], v[24:27]\n"
    "ds_read_b64 v[180:181], v2 offset:21504\n"
    "v_cvt_pk_bf16_f32 v74, v84, v84\n"
    "v_mfma_f32_16x16x32_bf16 v[28:31], v[188:191], v[168:171], v[28:31]\n"
    "ds_read_b64 v[184:185], v2 offset:22528\n"
    "v_mul_f32 v77, s22, v85\n"
    "s_waitcnt lgkmcnt(3)\n"
    "v_mfma_f32_16x16x32_bf16 v[16:19], v[192:195], v[172:175], v[16:19]\n"
    "ds_read_b64 v[188:189], v2 offset:23552\n"
    "v_lshlrev_b32 v87, 16, v74\n"
    "v_mfma_f32_16x16x32_bf16 v[20:23], v[196:199], v[172:175], v[20:23]\n"
    "ds_read_b128 v[192:195], v0 offset:0\n"
    "v_sub_f32 v87, v84, v87\n"
    "v_cvt_pk_bf16_f32 v75, v87, v87\n"
    "buffer_load_dwordx4 a[240:243], v0, s[4:7], s97 offen offset:1024 sc1\n"
    "v_mfma_f32_16x16x32_bf16 v[24:27], v[200:203], v[172:175], v[24:27]\n"
    "ds_read_b128 v[196:199], v0 offset:1024\n"
    "v_mfma_f32_16x16x32_bf16 v[28:31], v[204:207], v[172:175], v[28:31]\n"
    "ds_read_b128 v[200:203], v0 offset:2048\n"
    "buffer_load_dwordx4 a[244:247], v0, s[4:7], s97 offen offset:2048 sc1\n"
    "s_waitcnt lgkmcnt(3)\n"
    "v_mfma_f32_16x16x16_bf16 v[16:19], v[176:177], v[72:73], v[16:19]\n"
    "ds_read_b128 v[204:207], v0 offset:3072\n"
    "v_mfma_f32_16x16x16_bf16 v[20:23], v[180:181], v[72:73], v[20:23]\n"
    "ds_read_b128 v[176:179], v0 offset:4096\n"
    "v_mfma_f32_16x16x16_bf16 v[24:27], v[184:185], v[72:73], v[24:27]\n"
    "ds_read_b128 v[180:183], v0 offset:5120\n"
    "s_waitcnt vmcnt(15)\n"
    "v_xor_b32 v92, s29, v92\n"
    "v_xor_b32 v93, s29, v93\n"
    "v_xor_b32 v94, s29, v94\n"
    "v_xor_b32 v95, s29, v95\n"
    "v_or3_b32 v92, v92, v93, v94\n"
    "v_or_b32 v92, v92, v95\n"
    "v_cmp_ne_u32 vcc, 0, v92\n"
    "s_cbranch_vccnz gfs_43_%=\n"
    "gfr_44_%=:\n"
    "v_mfma_f32_16x16x16_bf16 v[28:31], v[188:189], v[72:73], v[28:31]\n"
    "ds_read_b128 v[184:187], v0 offset:6144\n"
    "s_waitcnt lgkmcnt(3)\n"
    "v_mfma_f32_16x16x32_bf16 v[32:35], v[192:195], v[96:99], 0\n"
    "ds_read_b128 v[188:191], v0 offset:7168\n"
    "v_mfma_f32_16x16x32_bf16 v[36:39], v[196:199], v[96:99], 0\n"
    "ds_read_b128 v[192:195], v0 offset:8192\n"
    "v_mfma_f32_16x16x32_bf16 v[40:43], v[200:203], v[96:99], 0\n"
    "ds_read_b128 v[196:199], v0 offset:9216\n"
    "buffer_load_dwordx4 v[96:99], v88, s[4:7], s36 offen offset:0 sc1\n"
    "v_mfma_f32_16x16x32_bf16 v[32:35], v[204:207], v[100:103], v[32:35]\n"
    "ds_read_b128 v[200:203], v0 offset:10240\n"
    "buffer_load_dwordx4 a[248:251], v0, s[4:7], s97 offen offset:3072 sc1\n"
    "s_waitcnt lgkmcnt(3)\n"
    "v_mfma_f32_16x16x32_bf16 v[36:39], v[176:179], v[100:103], v[36:39]\n"
    "ds_read_b128 v[204:207], v0 offset:11264\n"
    "v_mul_f32 v16, v16, v76\n"
    "v_mfma_f32_16x16x32_bf16 v[40:43], v[180:183], v[100:103], v[40:43]\n"
    "ds_read_b128 v[176:179], v0 offset:12288\n"
    "buffer_load_dwordx4 v[100:103], v88, s[4:7], s36 offen offset:1024 sc1\n"
    "v_mul_f32 v17, v17, v76\n"
    "v_mfma_f32_16x16x32_bf16 v[32:35], v[184:187], v[104:107], v[32:35]\n"
    "ds_read_b128 v[180:183], v0 offset:13312\n"
    "v_mul_f32 v18, v18, v76\n"
    "v_mfma_f32_16x16x32_bf16 v[36:39], v[188:191], v[104:107], v[36:39]\n"
    "ds_read_b128 v[184:187], v0 offset:14336\n"
    "v_mul_f32 v19, v19, v76\n"
    "v_mul_f32 v20, v20, v76\n"
    "s_waitcnt lgkmcnt(3)\n"
    "v_mfma_f32_16x16x32_bf16 v[40:43], v[192:195], v[104:107], v[40:43]\n"
    "ds_read_b128 v[188:191], v0 offset:15360\n"
    "buffer_load_dwordx4 v[104:107], v88, s[4:7], s36 offen offset:2048 sc1\n"
    "v_mul_f32 v21, v21, v76\n"
    "v_mfma_f32_16x16x32_bf16 v[32:35], v[196:199], v[108:111], v[32:35]\n"
    "ds_read_b128 v[192:195], v0 offset:16384\n"
    "v_mul_f32 v22, v22, v76\n"
    "v_mul_f32 v23, v23, v76\n"
    "v_mfma_f32_16x16x32_bf16 v[36:39], v[200:203], v[108:111], v[36:39]\n"
    "ds_read_b128 v[196:199], v0 offset:17408\n"
    "v_exp_f32 v208, v16\n"
    "v_mfma_f32_16x16x32_bf16 v[40:43], v[204:207], v[108:111], v[40:43]\n"
    "ds_read_b128 v[200:203], v0 offset:18432\n"
    "buffer_load_dwordx4 v[108:111], v88, s[4:7], s36 offen offset:3072 sc1\n"
    "v_exp_f32 v209, v17\n"
    "v_exp_f32 v210, v18\n"
    "s_waitcnt lgkmcnt(3)\n"
    "v_mfma_f32_16x16x32_bf16 v[32:35], v[176:179], v[112:115], v[32:35]\n"
    "ds_read_b128 v[204:207], v0 offset:19456\n"
    "v_exp_f32 v211, v19\n"
    "v_mfma_f32_16x16x32_bf16 v[36:39], v[180:183], v[112:115], v[36:39]\n"
    "ds_read_b128 v[176:179], v0 offset:20480\n"
    "v_exp_f32 v212, v20\n"
    "v_exp_f32 v213, v21\n"
    "v_mfma_f32_16x16x32_bf16 v[40:43], v[184:187], v[112:115], v[40:43]\n"
    "ds_read_b128 v[180:183], v0 offset:21504\n"
    "buffer_load_dwordx4 v[112:115], v88, s[4:7], s37 offen offset:0 sc1\n"
    "v_exp_f32 v214, v22\n"
    "v_mfma_f32_16x16x32_bf16 v[32:35], v[188:191], v[116:119], v[32:35]\n"
    "ds_read_b128 v[184:187], v0 offset:22528\n"
    "v_exp_f32 v215, v23\n"
    "v_fma_f32 v208, v208, s22, s22\n"
    "s_waitcnt lgkmcnt(3)\n"
    "v_mfma_f32_16x16x32_bf16 v[36:39], v[192:195], v[116:119], v[36:39]\n"
    "ds_read_b128 v[188:191], v0 offset:23552\n"
    "v_fma_f32 v209, v209, s22, s22\n"
    "v_mfma_f32_16x16x32_bf16 v[40:43], v[196:199], v[116:119], v[40:43]\n"
    "ds_read_b128 v[192:195], v0 offset:24576\n"
    "buffer_load_dwordx4 v[116:119], v88, s[4:7], s37 offen offset:1024 sc1\n"
    "v_fma_f32 v210, v210, s22, s22\n"
    "v_fma_f32 v211, v211, s22, s22\n"
    "v_mfma_f32_16x16x32_bf16 v[32:35], v[200:203], v[120:123], v[32:35]\n"
    "ds_read_b128 v[196:199], v0 offset:25600\n"
    "v_fma_f32 v212, v212, s22, s22\n"
    "v_mfma_f32_16x16x32_bf16 v[36:39], v[204:207], v[120:123], v[36:39]\n"
    "ds_read_b128 v[200:203], v0 offset:26624\n"
    "v_fma_f32 v213, v213, s22, s22\n"
    "v_fma_f32 v214, v214, s22, s22\n"
    "s_waitcnt lgkmcnt(3)\n"
    "v_mfma_f32_16x16x32_bf16 v[40:43], v[176:179], v[120:123], v[40:43]\n"
    "ds_read_b128 v[204:207], v0 offset:27648\n"
    "buffer_load_dwordx4 v[120:123], v88, s[4:7], s37 offen offset:2048 sc1\n"
    "v_fma_f32 v215, v215, s22, s22\n"
    "v_mfma_f32_16x16x32_bf16 v[32:35], v[180:183], v[124:127], v[32:35]\n"
    "ds_read_b128 v[176:179], v0 offset:28672\n"
    "v_rcp_f32 v208, v208\n"
    "v_rcp_f32 v209, v209\n"
    "v_mfma_f32_16x16x32_bf16 v[36:39], v[184:187], v[124:127], v[36:39]\n"
    "ds_read_b128 v[180:183], v0 offset:29696\n"
    "v_rcp_f32 v210, v210\n"
    "v_mfma_f32_16x16x32_bf16 v[40:43], v[188:191], v[124:127], v[40:43]\n"
    "ds_read_b128 v[184:187], v0 offset:30720\n"
    "buffer_load_dwordx4 v[124:127], v88, s[4:7], s37 offen offset:3072 sc1\n"
    "v_rcp_f32 v211, v211\n"
    "v_rcp_f32 v212, v212\n"
    "s_waitcnt lgkmcnt(3)\n"
    "v_mfma_f32_16x16x32_bf16 v[32:35], v[192:195], v[128:131], v[32:35]\n"
    "ds_read_b128 v[188:191], v0 offset:31744\n"
    "v_rcp_f32 v213, v213\n"
    "v_mfma_f32_16x16x32_bf16 v[36:39], v[196:199], v[128:131], v[36:39]\n"
    "ds_read_b128 v[192:195], v0 offset:32768\n"
    "buffer_load_dwordx4 a[252:255], v240, s[4:7], s97 offen offset:0 sc1\n"
    "s_add_u32 s97, s97, 5120\n"
    "s_cmp_eq_u32 s97, s98\n"
    "s_cselect_b32 s97, s20, s97\n"
    "v_rcp_f32 v214, v214\n"
    "v_mfma_f32_16x16x32_bf16 v[40:43], v[200:203], v[128:131], v[40:43]\n"
    "ds_read_b128 v[196:199], v0 offset:33792\n"
    "buffer_load_dwordx4 v[128:131], v88, s[4:7], s38 offen offset:0 sc1\n"
    "v_rcp_f32 v215, v215\n"
    "v_mul_f32 v16, v16, v208\n"
    "v_mfma_f32_16x16x32_bf16 v[32:35], v[204:207], v[132:135], v[32:35]\n"
    "ds_read_b128 v[200:203], v0 offset:34816\n"
    "v_mul_f32 v17, v17, v209\n"
    "s_waitcnt lgkmcnt(3)\n"
    "v_mfma_f32_16x16x32_bf16 v[36:39], v[176:179], v[132:135], v[36:39]\n"
    "ds_read_b128 v[204:207], v0 offset:35840\n"
    "v_mul_f32 v18, v18, v210\n"
    "v_mul_f32 v19, v19, v211\n"
    "v_mfma_f32_16x16x32_bf16 v[40:43], v[180:183], v[132:135], v[40:43]\n"
    "s_waitcnt vmcnt(21)\n"
    "s_barrier\n"
    "ds_read_b128 v[176:179], v0 offset:36864\n"
    "buffer_load_dwordx4 v[132:135], v88, s[4:7], s38 offen offset:1024 sc1\n"
    "v_mul_f32 v20, v20, v212\n"
    "v_mfma_f32_16x16x32_bf16 v[32:35], v[184:187], v[136:139], v[32:35]\n"
    "ds_read_b128 v[180:183], v0 offset:37888\n"
    "v_mul_f32 v21, v21, v213\n"
    "v_mul_f32 v22, v22, v214\n"
    "v_mfma_f32_16x16x32_bf16 v[36:39], v[188:191], v[136:139], v[36:39]\n"
    "ds_read_b128 v[184:187], v0 offset:38912\n"
    "v_mul_f32 v23, v23, v215\n"
    "s_waitcnt lgkmcnt(3)\n"
    "v_mfma_f32_16x16x32_bf16 v[40:43], v[192:195], v[136:139], v[40:43]\n"
    "ds_read_b128 v[188:191], v0 offset:39936\n"
    "buffer_load_dwordx4 v[136:139], v88, s[4:7], s38 offen offset:2048 sc1\n"
    "v_cvt_pk_bf16_f32 v216, v16, v17\n"
    "v_cvt_pk_bf16_f32 v217, v18, v19\n"
    "v_mfma_f32_16x16x32_bf16 v[32:35], v[196:199], v[140:143], v[32:35]\n"
    "ds_read_b128 v[192:195], v0 offset:40960\n"
    "v_cvt_pk_bf16_f32 v218, v20, v21\n"
    "v_mfma_f32_16x16x32_bf16 v[36:39], v[200:203], v[140:143], v[36:39]\n"
    "ds_read_b128 v[196:199], v0 offset:41984\n"
    "v_cvt_pk_bf16_f32 v219, v22, v23\n"
    "buffer_store_dwordx4 v[216:219], v0, s[4:7], s42 offen offset:0 sc1\n"
    "v_mfma_f32_16x16x32_bf16 v[40:43], v[204:207], v[140:143], v[40:43]\n"
    "ds_read_b128 v[200:203], v0 offset:43008\n"
    "buffer_load_dwordx4 v[140:143], v88, s[4:7], s38 offen offset:3072 sc1\n"
    "v_mul_f32 v24, v24, v76\n"
    "s_waitcnt lgkmcnt(3)\n"
    "v_mfma_f32_16x16x32_bf16 v[32:35], v[176:179], v[144:147], v[32:35]\n"
    "ds_read_b128 v[204:207], v0 offset:44032\n"
    "v_mul_f32 v25, v25, v76\n"
    "v_mul_f32 v26, v26, v76\n"
    "v_mfma_f32_16x16x32_bf16 v[36:39], v[180:183], v[144:147], v[36:39]\n"
    "ds_read_b128 v[176:179], v0 offset:45056\n"
    "v_mul_f32 v27, v27, v76\n"
    "v_mfma_f32_16x16x32_bf16 v[40:43], v[184:187], v[144:147], v[40:43]\n"
    "ds_read_b128 v[180:183], v0 offset:46080\n"
    "buffer_load_dwordx4 v[144:147], v88, s[4:7], s39 offen offset:0 sc1\n"
    "v_mul_f32 v28, v28, v76\n"
    "v_mul_f32 v29, v29, v76\n"
    "v_mfma_f32_16x16x32_bf16 v[32:35], v[188:191], v[148:151], v[32:35]\n"
    "ds_read_b128 v[184:187], v0 offset:47104\n"
    "v_mul_f32 v30, v30, v76\n"
    "s_waitcnt lgkmcnt(3)\n"
    "v_mfma_f32_16x16x32_bf16 v[36:39], v[192:195], v[148:151], v[36:39]\n"
    "ds_read_b128 v[188:191], v0 offset:48128\n"
    "v_mul_f32 v31, v31, v76\n"
    "v_exp_f32 v208, v24\n"
    "v_mfma_f32_16x16x32_bf16 v[40:43], v[196:199], v[148:151], v[40:43]\n"
    "ds_read_b128 v[192:195], v0 offset:49152\n"
    "buffer_load_dwordx4 v[148:151], v88, s[4:7], s39 offen offset:1024 sc1\n"
    "v_exp_f32 v209, v25\n"
    "v_mfma_f32_16x16x32_bf16 v[32:35], v[200:203], v[152:155], v[32:35]\n"
    "ds_read_b128 v[196:199], v0 offset:50176\n"
    "v_exp_f32 v210, v26\n"
    "v_exp_f32 v211, v27\n"
    "v_mfma_f32_16x16x32_bf16 v[36:39], v[204:207], v[152:155], v[36:39]\n"
    "ds_read_b128 v[200:203], v0 offset:51200\n"
    "v_exp_f32 v212, v28\n"
    "s_waitcnt lgkmcnt(3)\n"
    "v_mfma_f32_16x16x32_bf16 v[40:43], v[176:179], v[152:155], v[40:43]\n"
    "ds_read_b128 v[204:207], v0 offset:52224\n"
    "buffer_load_dwordx4 v[152:155], v88, s[4:7], s39 offen offset:2048 sc1\n"
    "v_exp_f32 v213, v29\n"
    "v_exp_f32 v214, v30\n"
    "v_mfma_f32_16x16x32_bf16 v[32:35], v[180:183], v[156:159], v[32:35]\n"
    "ds_read_b128 v[176:179], v0 offset:53248\n"
    "buffer_load_dwordx4 a[0:3], v0, s[4:7], s97 offen offset:0 sc1\n"
    "v_exp_f32 v215, v31\n"
    "v_mfma_f32_16x16x32_bf16 v[36:39], v[184:187], v[156:159], v[36:39]\n"
    "ds_read_b128 v[180:183], v0 offset:54272\n"
    "v_fma_f32 v208, v208, s22, s22\n"
    "v_mfma_f32_16x16x32_bf16 v[40:43], v[188:191], v[156:159], v[40:43]\n"
    "ds_read_b128 v[184:187], v0 offset:55296\n"
    "buffer_load_dwordx4 v[156:159], v88, s[4:7], s39 offen offset:3072 sc1\n"
    "v_fma_f32 v209, v209, s22, s22\n"
    "v_fma_f32 v210, v210, s22, s22\n"
    "s_waitcnt lgkmcnt(3)\n"
    "v_mfma_f32_16x16x32_bf16 v[32:35], v[192:195], v[160:163], v[32:35]\n"
    "ds_read_b128 v[188:191], v0 offset:56320\n"
    "v_fma_f32 v211, v211, s22, s22\n"
    "v_mfma_f32_16x16x32_bf16 v[36:39], v[196:199], v[160:163], v[36:39]\n"
    "ds_read_b128 v[192:195], v0 offset:57344\n"
    "v_fma_f32 v212, v212, s22, s22\n"
    "v_fma_f32 v213, v213, s22, s22\n"
    "v_mfma_f32_16x16x32_bf16 v[40:43], v[200:203], v[160:163], v[40:43]\n"
    "ds_read_b128 v[196:199], v0 offset:58368\n"
    "buffer_load_dwordx4 v[160:163], v88, s[4:7], s40 offen offset:0 sc1\n"
    "v_fma_f32 v214, v214, s22, s22\n"
    "v_mfma_f32_16x16x32_bf16 v[32:35], v[204:207], v[164:167], v[32:35]\n"
    "ds_read_b128 v[200:203], v0 offset:59392\n"
    "v_fma_f32 v215, v215, s22, s22\n"
    "v_rcp_f32 v208, v208\n"
    "s_waitcnt lgkmcnt(3)\n"
    "v_mfma_f32_16x16x32_bf16 v[36:39], v[176:179], v[164:167], v[36:39]\n"
    "ds_read_b128 v[204:207], v0 offset:60416\n"
    "v_rcp_f32 v209, v209\n"
    "v_mfma_f32_16x16x32_bf16 v[40:43], v[180:183], v[164:167], v[40:43]\n"
    "ds_read_b64 v[176:177], v2 offset:24576\n"
    "buffer_load_dwordx4 v[164:167], v88, s[4:7], s40 offen offset:1024 sc1\n"
    "v_rcp_f32 v210, v210\n"
    "v_rcp_f32 v211, v211\n"
    "v_mfma_f32_16x16x32_bf16 v[32:35], v[184:187], v[168:171], v[32:35]\n"
    "ds_read_b64 v[180:181], v2 offset:25600\n"
    "v_rcp_f32 v212, v212\n"
    "v_mfma_f32_16x16x32_bf16 v[36:39], v[188:191], v[168:171], v[36:39]\n"
    "ds_read_b64 v[184:185], v2 offset:26624\n"
    "v_rcp_f32 v213, v213\n"
    "v_rcp_f32 v214, v214\n"
    "s_waitcnt lgkmcnt(3)\n"
    "v_mfma_f32_16x16x32_bf16 v[40:43], v[192:195], v[168:171], v[40:43]\n"
    "ds_read_b128 v[188:191], v1 offset:36864\n"
    "buffer_load_dwordx4 v[168:171], v88, s[4:7], s40 offen offset:2048 sc1\n"
    "v_rcp_f32 v215, v215\n"
    "v_mfma_f32_16x16x32_bf16 v[32:35], v[196:199], v[172:175], v[32:35]\n"
    "ds_read_b128 v[192:195], v1 offset:37888\n"
    "v_mul_f32 v24, v24, v208\n"
    "v_mul_f32 v25, v25, v209\n"
    "v_mfma_f32_16x16x32_bf16 v[36:39], v[200:203], v[172:175], v[36:39]\n"
    "ds_read_b128 v[196:199], v1 offset:38912\n"
    "v_mul_f32 v26, v26, v210\n"
    "v_mfma_f32_16x16x32_bf16 v[40:43], v[204:207], v[172:175], v[40:43]\n"
    "ds_read_b128 v[200:203], v1 offset:39936\n"
    "buffer_load_dwordx4 v[172:175], v88, s[4:7], s40 offen offset:3072 sc1\n"
    "v_mul_f32 v27, v27, v211\n"
    "v_mul_f32 v28, v28, v212\n"
    "s_waitcnt lgkmcnt(3)\n"
    "v_mfma_f32_16x16x16_bf16 v[32:35], v[176:177], v[72:73], v[32:35]\n"
    "ds_read_b128 v[204:207], v1 offset:40960\n"
    "s_waitcnt vmcnt(19)\n"
    "v_max3_u32 v7, v108, v100, v104\n"
    "v_mul_f32 v29, v29, v213\n"
    "v_mfma_f32_16x16x16_bf16 v[36:39], v[180:181], v[72:73], v[36:39]\n"
    "ds_read_b128 v[176:179], v1 offset:41984\n"
    "v_max_u32 v7, v7, v96\n"
    "v_mul_f32 v30, v30, v214\n"
    "v_mul_f32 v31, v31, v215\n"
    "v_mfma_f32_16x16x16_bf16 v[40:43], v[184:185], v[72:73], v[40:43]\n"
    "ds_read_b128 v[180:183], v1 offset:43008\n"
    "v_cmp_eq_u32 vcc, -1, v7\n"
    "s_cbranch_vccnz h5s_31_%=\n"
    "h5r_32_%=:\n"
    "v_cvt_pk_bf16_f32 v220, v24, v25\n"
    "v_cvt_pk_bf16_f32 v221, v26, v27\n"
    "v_cvt_pk_bf16_f32 v222, v28, v29\n"
    "v_cvt_pk_bf16_f32 v223, v30, v31\n"
    "buffer_store_dwordx4 v[220:223], v0, s[4:7], s42 offen offset:1024 sc1\n"
    "v_mfma_f32_16x16x32_bf16 v[44:47], v[188:191], v[96:99], 0\n"
    "ds_read_b128 v[184:187], v1 offset:44032\n"
    "s_waitcnt lgkmcnt(3)\n"
    "v_mfma_f32_16x16x32_bf16 v[48:51], v[192:195], v[96:99], 0\n"
    "ds_read_b128 v[188:191], v1 offset:45056\n"
    "buffer_load_dwordx4 a[4:7], v0, s[4:7], s97 offen offset:1024 sc1\n"
    "v_mfma_f32_16x16x32_bf16 v[52:55], v[196:199], v[96:99], 0\n"
    "ds_read_b128 v[192:195], v1 offset:46080\n"
    "v_mul_f32 v32, v32, v76\n"
    "v_mfma_f32_16x16x32_bf16 v[56:59], v[200:203], v[96:99], 0\n"
    "ds_read_b128 v[196:199], v1 offset:47104\n"
    "v_mul_f32 v33, v33, v76\n"
    "v_mfma_f32_16x16x32_bf16 v[44:47], v[204:207], v[100:103], v[44:47]\n"
    "ds_read_b128 v[200:203], v1 offset:48128\n"
    "v_mul_f32 v34, v34, v76\n"
    "s_waitcnt lgkmcnt(3)\n"
    "v_mfma_f32_16x16x32_bf16 v[48:51], v[176:179], v[100:103], v[48:51]\n"
    "ds_read_b128 v[204:207], v1 offset:49152\n"
    "buffer_load_dwordx4 a[8:11], v0, s[4:7], s97 offen offset:2048 sc1\n"
    "v_mfma_f32_16x16x32_bf16 v[52:55], v[180:183], v[100:103], v[52:55]\n"
    "ds_read_b128 v[176:179], v1 offset:50176\n"
    "v_mul_f32 v35, v35, v76\n"
    "v_mfma_f32_16x16x32_bf16 v[56:59], v[184:187], v[100:103], v[56:59]\n"
    "ds_read_b128 v[180:183], v1 offset:51200\n"
    "v_mul_f32 v36, v36, v76\n"
    "v_mfma_f32_16x16x32_bf16 v[44:47], v[188:191], v[104:107], v[44:47]\n"
    "ds_read_b128 v[184:187], v1 offset:52224\n"
    "v_mul_f32 v37, v37, v76\n"
    "s_waitcnt lgkmcnt(3)\n"
    "v_mfma_f32_16x16x32_bf16 v[48:51], v[192:195], v[104:107], v[48:51]\n"
    "ds_read_b128 v[188:191], v1 offset:53248\n"
    "v_mul_f32 v38, v38, v76\n"
    "v_mfma_f32_16x16x32_bf16 v[52:55], v[196:199], v[104:107], v[52:55]\n"
    "ds_read_b128 v[192:195], v1 offset:54272\n"
    "buffer_load_dwordx4 a[12:15], v0, s[4:7], s97 offen offset:3072 sc1\n"
    "v_mul_f32 v39, v39, v76\n"
    "v_mfma_f32_16x16x32_bf16 v[56:59], v[200:203], v[104:107], v[56:59]\n"
    "ds_read_b128 v[196:199], v1 offset:55296\n"
    "v_exp_f32 v208, v32\n"
    "v_mfma_f32_16x16x32_bf16 v[44:47], v[204:207], v[108:111], v[44:47]\n"
    "ds_read_b128 v[200:203], v1 offset:56320\n"
    "v_exp_f32 v209, v33\n"
    "s_waitcnt lgkmcnt(3)\n"
    "v_mfma_f32_16x16x32_bf16 v[48:51], v[176:179], v[108:111], v[48:51]\n"
    "ds_read_b128 v[204:207], v1 offset:57344\n"
    "s_waitcnt vmcnt(19)\n"
    "v_max3_u32 v7, v124, v116, v120\n"
    "v_exp_f32 v210, v34\n"
    "v_exp_f32 v211, v35\n"
    "v_mfma_f32_16x16x32_bf16 v[52:55], v[180:183], v[108:111], v[52:55]\n"
    "ds_read_b128 v[176:179], v1 offset:58368\n"
    "v_max_u32 v7, v7, v112\n"
    "buffer_load_dwordx4 a[16:19], v240, s[4:7], s97 offen offset:0 sc1\n"
    "s_add_u32 s97, s97, 5120\n"
    "s_cmp_eq_u32 s97, s98\n"
    "s_cselect_b32 s97, s20, s97\n"
    "v_exp_f32 v212, v36\n"
    "v_mfma_f32_16x16x32_bf16 v[56:59], v[184:187], v[108:111], v[56:59]\n"
    "ds_read_b128 v[180:183], v1 offset:59392\n"
    "v_cmp_eq_u32 vcc, -1, v7\n"
    "s_cbranch_vccnz h6s_33_%=\n"
    "h6r_34_%=:\n"
    "v_exp_f32 v213, v37\n"
    "v_mfma_f32_16x16x32_bf16 v[44:47], v[188:191], v[112:115], v[44:47]\n"
    "ds_read_b128 v[184:187], v1 offset:60416\n"
    "v_exp_f32 v214, v38\n"
    "s_waitcnt lgkmcnt(3)\n"
    "v_mfma_f32_16x16x32_bf16 v[48:51], v[192:195], v[112:115], v[48:51]\n"
    "ds_read_b128 v[188:191], v1 offset:61440\n"
    "buffer_load_dwordx4 a[20:23], v0, s[4:7], s97 offen offset:0 sc1\n"
    "v_exp_f32 v215, v39\n"
    "v_fma_f32 v208, v208, s22, s22\n"
    "v_mfma_f32_16x16x32_bf16 v[52:55], v[196:199], v[112:115], v[52:55]\n"
    "ds_read_b128 v[192:195], v1 offset:62464\n"
    "v_fma_f32 v209, v209, s22, s22\n"
    "v_mfma_f32_16x16x32_bf16 v[56:59], v[200:203], v[112:115], v[56:59]\n"
    "ds_read_b128 v[196:199], v1 offset:63488\n"
    "v_fma_f32 v210, v210, s22, s22\n"
    "v_mfma_f32_16x16x32_bf16 v[44:47], v[204:207], v[116:119], v[44:47]\n"
    "ds_read_b128 v[200:203], v1 offset:64512\n"
    "v_fma_f32 v211, v211, s22, s22\n"
    "s_waitcnt lgkmcnt(3)\n"
    "v_mfma_f32_16x16x32_bf16 v[48:51], v[176:179], v[116:119], v[48:51]\n"
    "ds_read_b128 v[204:207], v2 offset:0\n"
    "buffer_load_dwordx4 a[24:27], v0, s[4:7], s97 offen offset:1024 sc1\n"
    "v_fma_f32 v212, v212, s22, s22\n"
    "v_fma_f32 v213, v213, s22, s22\n"
    "v_mfma_f32_16x16x32_bf16 v[52:55], v[180:183], v[116:119], v[52:55]\n"
    "ds_read_b128 v[176:179], v2 offset:1024\n"
    "v_fma_f32 v214, v214, s22, s22\n"
    "v_mfma_f32_16x16x32_bf16 v[56:59], v[184:187], v[116:119], v[56:59]\n"
    "ds_read_b128 v[180:183], v2 offset:2048\n"
    "v_fma_f32 v215, v215, s22, s22\n"
    "v_mfma_f32_16x16x32_bf16 v[44:47], v[188:191], v[120:123], v[44:47]\n"
    "ds_read_b128 v[184:187], v2 offset:3072\n"
    "buffer_load_dwordx4 a[28:31], v0, s[4:7], s97 offen offset:2048 sc1\n"
    "v_rcp_f32 v208, v208\n"
    "s_waitcnt lgkmcnt(3)\n"
    "v_mfma_f32_16x16x32_bf16 v[48:51], v[192:195], v[120:123], v[48:51]\n"
    "ds_read_b128 v[188:191], v2 offset:4096\n"
    "v_rcp_f32 v209, v209\n"
    "v_rcp_f32 v210, v210\n"
    "v_mfma_f32_16x16x32_bf16 v[52:55], v[196:199], v[120:123], v[52:55]\n"
    "ds_read_b128 v[192:195], v2 offset:5120\n"
    "v_rcp_f32 v211, v211\n"
    "v_mfma_f32_16x16x32_bf16 v[56:59], v[200:203], v[120:123], v[56:59]\n"
    "ds_read_b128 v[196:199], v2 offset:6144\n"
    "v_rcp_f32 v212, v212\n"
    "v_mfma_f32_16x16x32_bf16 v[44:47], v[204:207], v[124:127], v[44:47]\n"
    "ds_read_b128 v[200:203], v2 offset:7168\n"
    "buffer_load_dwordx4 a[32:35], v0, s[4:7], s97 offen offset:3072 sc1\n"
    "v_rcp_f32 v213, v213\n"
    "s_waitcnt lgkmcnt(3)\n"
    "v_mfma_f32_16x16x32_bf16 v[48:51], v[176:179], v[124:127], v[48:51]\n"
    "ds_read_b128 v[204:207], v2 offset:8192\n"
    "s_waitcnt vmcnt(18)\n"
    "v_max3_u32 v7, v140, v132, v136\n"
    "v_rcp_f32 v214, v214\n"
    "v_rcp_f32 v215, v215\n"
    "v_mfma_f32_16x16x32_bf16 v[52:55], v[180:183], v[124:127], v[52:55]\n"
    "ds_read_b128 v[176:179], v2 offset:9216\n"
    "v_max_u32 v7, v7, v128\n"
    "v_mul_f32 v32, v32, v208\n"
    "v_mfma_f32_16x16x32_bf16 v[56:59], v[184:187], v[124:127], v[56:59]\n"
    "ds_read_b128 v[180:183], v2 offset:10240\n"
    "v_cmp_eq_u32 vcc, -1, v7\n"
    "s_cbranch_vccnz h7s_35_%=\n"
    "h7r_36_%=:\n"
    "buffer_load_dwordx4 a[36:39], v240, s[4:7], s97 offen offset:0 sc1\n"
    "s_add_u32 s97, s97, 5120\n"
    "s_cmp_eq_u32 s97, s98\n"
    "s_cselect_b32 s97, s20, s97\n"
    "v_mul_f32 v33, v33, v209\n"
    "v_mfma_f32_16x16x32_bf16 v[44:47], v[188:191], v[128:131], v[44:47]\n"
    "ds_read_b128 v[184:187], v2 offset:11264\n"
    "v_mul_f32 v34, v34, v210\n"
    "s_waitcnt lgkmcnt(3)\n"
    "v_mfma_f32_16x16x32_bf16 v[48:51], v[192:195], v[128:131], v[48:51]\n"
    "ds_read_b128 v[188:191], v2 offset:12288\n"
    "v_mul_f32 v35, v35, v211\n"
    "v_mul_f32 v36, v36, v212\n"
    "v_mfma_f32_16x16x32_bf16 v[52:55], v[196:199], v[128:131], v[52:55]\n"
    "ds_read_b128 v[192:195], v2 offset:13312\n"
    "v_mul_f32 v37, v37, v213\n"
    "v_mfma_f32_16x16x32_bf16 v[56:59], v[200:203], v[128:131], v[56:59]\n"
    "ds_read_b128 v[196:199], v2 offset:14336\n"
    "buffer_load_dwordx4 a[40:43], v0, s[4:7], s97 offen offset:0 sc1\n"
    "v_mul_f32 v38, v38, v214\n"
    "v_mfma_f32_16x16x32_bf16 v[44:47], v[204:207], v[132:135], v[44:47]\n"
    "ds_read_b128 v[200:203], v2 offset:15360\n"
    "v_mul_f32 v39, v39, v215\n"
    "s_waitcnt lgkmcnt(3)\n"
    "v_mfma_f32_16x16x32_bf16 v[48:51], v[176:179], v[132:135], v[48:51]\n"
    "ds_read_b128 v[204:207], v2 offset:16384\n"
    "v_cvt_pk_bf16_f32 v224, v32, v33\n"
    "v_cvt_pk_bf16_f32 v225, v34, v35\n"
    "v_mfma_f32_16x16x32_bf16 v[52:55], v[180:183], v[132:135], v[52:55]\n"
    "ds_read_b128 v[176:179], v2 offset:17408\n"
    "buffer_load_dwordx4 a[44:47], v0, s[4:7], s97 offen offset:1024 sc1\n"
    "v_cvt_pk_bf16_f32 v226, v36, v37\n"
    "v_mfma_f32_16x16x32_bf16 v[56:59], v[184:187], v[132:135], v[56:59]\n"
    "ds_read_b128 v[180:183], v2 offset:18432\n"
    "v_cvt_pk_bf16_f32 v227, v38, v39\n"
    "v_mfma_f32_16x16x32_bf16 v[44:47], v[188:191], v[136:139], v[44:47]\n"
    "ds_read_b128 v[184:187], v2 offset:19456\n"
    "buffer_store_dwordx4 v[224:227], v0, s[4:7], s42 offen offset:2048 sc1\n"
    "s_waitcnt lgkmcnt(3)\n"
    "v_mfma_f32_16x16x32_bf16 v[48:51], v[192:195], v[136:139], v[48:51]\n"
    "ds_read_b128 v[188:191], v0 offset:61440\n"
    "v_mul_f32 v40, v40, v76\n"
    "v_mul_f32 v41, v41, v76\n"
    "v_mfma_f32_16x16x32_bf16 v[52:55], v[196:199], v[136:139], v[52:55]\n"
    "ds_read_b128 v[192:195], v0 offset:62464\n"
    "buffer_load_dwordx4 a[48:51], v0, s[4:7], s97 offen offset:2048 sc1\n"
    "v_mul_f32 v42, v42, v76\n"
    "v_mfma_f32_16x16x32_bf16 v[56:59], v[200:203], v[136:139], v[56:59]\n"
    "ds_read_b128 v[196:199], v0 offset:63488\n"
    "v_mul_f32 v43, v43, v76\n"
    "v_mfma_f32_16x16x32_bf16 v[44:47], v[204:207], v[140:143], v[44:47]\n"
    "ds_read_b128 v[200:203], v0 offset:64512\n"
    "v_exp_f32 v208, v40\n"
    "s_waitcnt lgkmcnt(3)\n"
    "v_mfma_f32_16x16x32_bf16 v[48:51], v[176:179], v[140:143], v[48:51]\n"
    "ds_read_b128 v[204:207], v1 offset:0\n"
    "s_waitcnt vmcnt(18)\n"
    "v_max3_u32 v7, v156, v148, v152\n"
    "buffer_load_dwordx4 a[52:55], v0, s[4:7], s97 offen offset:3072 sc1\n"
    "v_exp_f32 v209, v41\n"
    "v_exp_f32 v210, v42\n"
    "v_mfma_f32_16x16x32_bf16 v[52:55], v[180:183], v[140:143], v[52:55]\n"
    "ds_read_b128 v[176:179], v1 offset:1024\n"
    "v_max_u32 v7, v7, v144\n"
    "v_exp_f32 v211, v43\n"
    "v_mfma_f32_16x16x32_bf16 v[56:59], v[184:187], v[140:143], v[56:59]\n"
    "ds_read_b128 v[180:183], v1 offset:2048\n"
    "v_cmp_eq_u32 vcc, -1, v7\n"
    "s_cbranch_vccnz h8s_37_%=\n"
    "h8r_38_%=:\n"
    "v_fma_f32 v208, v208, s22, s22\n"
    "v_mfma_f32_16x16x32_bf16 v[44:47], v[188:191], v[144:147], v[44:47]\n"
    "ds_read_b128 v[184:187], v1 offset:3072\n"
    "buffer_load_dwordx4 a[56:59], v240, s[4:7], s97 offen offset:0 sc1\n"
    "s_add_u32 s97, s97, 5120\n"
    "s_cmp_eq_u32 s97, s98\n"
    "s_cselect_b32 s97, s20, s97\n"
    "v_fma_f32 v209, v209, s22, s22\n"
    "s_waitcnt lgkmcnt(3)\n"
    "v_mfma_f32_16x16x32_bf16 v[48:51], v[192:195], v[144:147], v[48:51]\n"
    "ds_read_b128 v[188:191], v1 offset:4096\n"
    "v_fma_f32 v210, v210, s22, s22\n"
    "v_fma_f32 v211, v211, s22, s22\n"
    "v_mfma_f32_16x16x32_bf16 v[52:55], v[196:199], v[144:147], v[52:55]\n"
    "ds_read_b128 v[192:195], v1 offset:5120\n"
    "v_rcp_f32 v208, v208\n"
    "v_mfma_f32_16x16x32_bf16 v[56:59], v[200:203], v[144:147], v[56:59]\n"
    "ds_read_b128 v[196:199], v1 offset:6144\n"
    "v_rcp_f32 v209, v209\n"
    "v_mfma_f32_16x16x32_bf16 v[44:47], v[204:207], v[148:151], v[44:47]\n"
    "ds_read_b128 v[200:203], v1 offset:7168\n"
    "buffer_load_dwordx4 a[60:63], v0, s[4:7], s97 offen offset:0 sc1\n"
    "v_rcp_f32 v210, v210\n"
    "s_waitcnt lgkmcnt(3)\n"
    "v_mfma_f32_16x16x32_bf16 v[48:51], v[176:179], v[148:151], v[48:51]\n"
    "ds_read_b128 v[204:207], v1 offset:8192\n"
    "v_rcp_f32 v211, v211\n"
    "v_mul_f32 v40, v40, v208\n"
    "v_mfma_f32_16x16x32_bf16 v[52:55], v[180:183], v[148:151], v[52:55]\n"
    "ds_read_b128 v[176:179], v1 offset:9216\n"
    "v_mul_f32 v41, v41, v209\n"
    "v_mfma_f32_16x16x32_bf16 v[56:59], v[184:187], v[148:151], v[56:59]\n"
    "ds_read_b128 v[180:183], v1 offset:10240\n"
    "buffer_load_dwordx4 a[64:67], v0, s[4:7], s97 offen offset:1024 sc1\n"
    "v_mul_f32 v42, v42, v210\n"
    "v_mfma_f32_16x16x32_bf16 v[44:47], v[188:191], v[152:155], v[44:47]\n"
    "ds_read_b128 v[184:187], v1 offset:11264\n"
    "v_mul_f32 v43, v43, v211\n"
    "s_waitcnt lgkmcnt(3)\n"
    "v_mfma_f32_16x16x32_bf16 v[48:51], v[192:195], v[152:155], v[48:51]\n"
    "ds_read_b128 v[188:191], v1 offset:12288\n"
    "v_cvt_pk_bf16_f32 v228, v40, v41\n"
    "v_cvt_pk_bf16_f32 v229, v42, v43\n"
    "s_nop 0\n"
    "v_permlane32_swap_b32 v228, v230\n"
    "v_permlane32_swap_b32 v229, v231\n"
    "buffer_store_dwordx4 v[228:231], v12, s[4:7], s43 offen sc1\n"
    "s_add_u32 s42, s42, 57344\n"
    "s_add_u32 s43, s43, 57344\n"
    "v_mfma_f32_16x16x32_bf16 v[52:55], v[196:199], v[152:155], v[52:55]\n"
    "ds_read_b128 v[192:195], v1 offset:13312\n"
    "buffer_load_dwordx4 a[68:71], v0, s[4:7], s97 offen offset:2048 sc1\n"
    "v_mfma_f32_16x16x32_bf16 v[56:59], v[200:203], v[152:155], v[56:59]\n"
    "ds_read_b128 v[196:199], v1 offset:14336\n"
    "v_mul_u32_u24 v16, s47, v246\n"
    "v_mfma_f32_16x16x32_bf16 v[44:47], v[204:207], v[156:159], v[44:47]\n"
    "ds_read_b128 v[200:203], v1 offset:15360\n"
    "buffer_load_dwordx4 a[72:75], v0, s[4:7], s97 offen offset:3072 sc1\n"
    "s_waitcnt lgkmcnt(3)\n"
    "v_mfma_f32_16x16x32_bf16 v[48:51], v[176:179], v[156:159], v[48:51]\n"
    "ds_read_b128 v[204:207], v1 offset:16384\n"
    "s_waitcnt vmcnt(21)\n"
    "v_max3_u32 v7, v172, v164, v168\n"
    "v_mfma_f32_16x16x32_bf16 v[52:55], v[180:183], v[156:159], v[52:55]\n"
    "ds_read_b128 v[176:179], v1 offset:17408\n"
    "v_max_u32 v7, v7, v160\n"
    "v_and_b32 v17, s24, v246\n"
    "v_mfma_f32_16x16x32_bf16 v[56:59], v[184:187], v[156:159], v[56:59]\n"
    "ds_read_b128 v[180:183], v1 offset:18432\n"
    "v_cmp_eq_u32 vcc, -1, v7\n"
    "s_cbranch_vccnz h9s_39_%=\n"
    "h9r_40_%=:\n"
    "v_mul_u32_u24 v18, s47, v247\n"
    "v_mfma_f32_16x16x32_bf16 v[44:47], v[188:191], v[160:163], v[44:47]\n"
    "ds_read_b128 v[184:187], v1 offset:19456\n"
    "v_and_b32 v19, s24, v247\n"
    "s_waitcnt lgkmcnt(3)\n"
    "v_mfma_f32_16x16x32_bf16 v[48:51], v[192:195], v[160:163], v[48:51]\n"
    "ds_read_b128 v[188:191], v1 offset:20480\n"
    "v_mul_u32_u24 v20, s47, v248\n"
    "v_mfma_f32_16x16x32_bf16 v[52:55], v[196:199], v[160:163], v[52:55]\n"
    "ds_read_b128 v[192:195], v1 offset:21504\n"
    "buffer_load_dwordx4 a[76:79], v240, s[4:7], s97 offen offset:0 sc1\n"
    "s_mov_b32 s97, s98\n"
    "v_mfma_f32_16x16x32_bf16 v[56:59], v[200:203], v[160:163], v[56:59]\n"
    "ds_read_b128 v[196:199], v1 offset:22528\n"
    "v_and_b32 v21, s24, v248\n"
    "v_mfma_f32_16x16x32_bf16 v[44:47], v[204:207], v[164:167], v[44:47]\n"
    "ds_read_b128 v[200:203], v1 offset:23552\n"
    "buffer_load_dwordx4 a[80:83], v0, s[4:7], s97 offen offset:0 sc1\n"
    "s_waitcnt lgkmcnt(3)\n"
    "v_mfma_f32_16x16x32_bf16 v[48:51], v[176:179], v[164:167], v[48:51]\n"
    "ds_read_b128 v[204:207], v1 offset:24576\n"
    "v_mul_u32_u24 v22, s47, v249\n"
    "v_mfma_f32_16x16x32_bf16 v[52:55], v[180:183], v[164:167], v[52:55]\n"
    "ds_read_b128 v[176:179], v1 offset:25600\n"
    "v_and_b32 v23, s24, v249\n"
    "v_mfma_f32_16x16x32_bf16 v[56:59], v[184:187], v[164:167], v[56:59]\n"
    "ds_read_b128 v[180:183], v1 offset:26624\n"
    "v_mul_u32_u24 v24, s47, v250\n"
    "v_mfma_f32_16x16x32_bf16 v[44:47], v[188:191], v[168:171], v[44:47]\n"
    "ds_read_b128 v[184:187], v1 offset:27648\n"
    "buffer_load_dwordx4 a[84:87], v0, s[4:7], s97 offen offset:1024 sc1\n"
    "s_waitcnt lgkmcnt(3)\n"
    "v_mfma_f32_16x16x32_bf16 v[48:51], v[192:195], v[168:171], v[48:51]\n"
    "ds_read_b64 v[188:189], v2 offset:20480\n"
    "v_and_b32 v25, s24, v250\n"
    "v_mfma_f32_16x16x32_bf16 v[52:55], v[196:199], v[168:171], v[52:55]\n"
    "ds_read_b64 v[192:193], v2 offset:21504\n"
    "v_mul_u32_u24 v26, s47, v251\n"
    "v_mfma_f32_16x16x32_bf16 v[56:59], v[200:203], v[168:171], v[56:59]\n"
    "ds_read_b64 v[196:197], v2 offset:22528\n"
    "v_and_b32 v27, s24, v251\n"
    "v_mfma_f32_16x16x32_bf16 v[44:47], v[204:207], v[172:175], v[44:47]\n"
    "ds_read_b64 v[200:201], v2 offset:23552\n"
    "buffer_load_dwordx4 a[88:91], v0, s[4:7], s97 offen offset:2048 sc1\n"
    "s_waitcnt lgkmcnt(3)\n"
    "v_mfma_f32_16x16x32_bf16 v[48:51], v[176:179], v[172:175], v[48:51]\n"
    "ds_read_b128 v[204:207], v0 offset:0\n"
    "v_mul_u32_u24 v28, s47, v252\n"
    "v_mfma_f32_16x16x32_bf16 v[52:55], v[180:183], v[172:175], v[52:55]\n"
    "ds_read_b128 v[176:179], v0 offset:1024\n"
    "v_and_b32 v29, s24, v252\n"
    "v_mfma_f32_16x16x32_bf16 v[56:59], v[184:187], v[172:175], v[56:59]\n"
    "ds_read_b128 v[180:183], v0 offset:2048\n"
    "v_mul_u32_u24 v30, s47, v253\n"
    "v_mfma_f32_16x16x16_bf16 v[44:47], v[188:189], v[74:75], v[44:47]\n"
    "ds_read_b128 v[184:187], v0 offset:3072\n"
    "buffer_load_dwordx4 a[92:95], v0, s[4:7], s97 offen offset:3072 sc1\n"
    "s_waitcnt lgkmcnt(3)\n"
    "v_mfma_f32_16x16x16_bf16 v[48:51], v[192:193], v[74:75], v[48:51]\n"
    "ds_read_b128 v[188:191], v0 offset:4096\n"
    "v_and_b32 v31, s24, v253\n"
    "v_mfma_f32_16x16x16_bf16 v[52:55], v[196:197], v[74:75], v[52:55]\n"
    "ds_read_b128 v[192:195], v0 offset:5120\n"
    "v_mul_u32_u24 v32, s47, v254\n"
    "v_mfma_f32_16x16x16_bf16 v[56:59], v[200:201], v[74:75], v[56:59]\n"
    "ds_read_b128 v[196:199], v0 offset:6144\n"
    "buffer_load_dwordx4 v[232:235], v240, s[4:7], s97 offen offset:0 sc1\n"
    "s_add_u32 s98, s98, 5120\n"
    "s_add_u32 s97, s98, 4096\n"
    "v_and_b32 v33, s24, v254\n"
    "v_mfma_f32_16x16x32_bf16 v[60:63], v[204:207], v[96:99], 0\n"
    "ds_read_b128 v[200:203], v0 offset:7168\n"
    "v_mul_u32_u24 v34, s47, v255\n"
    "v_and_b32 v35, s24, v255\n"
    "s_waitcnt lgkmcnt(3)\n"
    "v_mfma_f32_16x16x32_bf16 v[64:67], v[176:179], v[96:99], 0\n"
    "ds_read_b128 v[204:207], v0 offset:8192\n"
    "buffer_load_dwordx2 v[236:237], v14, s[4:7], s98 offen offset:0 sc1\n"
    "v_mfma_f32_16x16x32_bf16 v[68:71], v[180:183], v[96:99], 0\n"
    "ds_read_b128 v[176:179], v0 offset:9216\n"
    "v_mul_f32 v44, v44, v77\n"
    "v_mfma_f32_16x16x32_bf16 v[60:63], v[184:187], v[100:103], v[60:63]\n"
    "ds_read_b128 v[180:183], v0 offset:10240\n"
    "v_mul_f32 v45, v45, v77\n"
    "v_mfma_f32_16x16x32_bf16 v[64:67], v[188:191], v[100:103], v[64:67]\n"
    "ds_read_b128 v[184:187], v0 offset:11264\n"
    "v_mul_f32 v46, v46, v77\n"
    "s_waitcnt lgkmcnt(3)\n"
    "v_mfma_f32_16x16x32_bf16 v[68:71], v[192:195], v[100:103], v[68:71]\n"
    "ds_read_b128 v[188:191], v0 offset:12288\n"
    "buffer_load_dwordx2 v[238:239], v14, s[4:7], s98 offen offset:1024 sc1\n"
    "v_mfma_f32_16x16x32_bf16 v[60:63], v[196:199], v[104:107], v[60:63]\n"
    "ds_read_b128 v[192:195], v0 offset:13312\n"
    "v_mul_f32 v47, v47, v77\n"
    "v_mfma_f32_16x16x32_bf16 v[64:67], v[200:203], v[104:107], v[64:67]\n"
    "ds_read_b128 v[196:199], v0 offset:14336\n"
    "v_mul_f32 v48, v48, v77\n"
    "v_mfma_f32_16x16x32_bf16 v[68:71], v[204:207], v[104:107], v[68:71]\n"
    "ds_read_b128 v[200:203], v0 offset:15360\n"
    "v_mul_f32 v49, v49, v77\n"
    "s_waitcnt lgkmcnt(3)\n"
    "v_mfma_f32_16x16x32_bf16 v[60:63], v[176:179], v[108:111], v[60:63]\n"
    "ds_read_b128 v[204:207], v0 offset:16384\n"
    "v_mul_f32 v50, v50, v77\n"
    "v_mul_f32 v51, v51, v77\n"
    "v_mfma_f32_16x16x32_bf16 v[64:67], v[180:183], v[108:111], v[64:67]\n"
    "ds_read_b128 v[176:179], v0 offset:17408\n"
    "v_exp_f32 v208, v44\n"
    "v_mfma_f32_16x16x32_bf16 v[68:71], v[184:187], v[108:111], v[68:71]\n"
    "ds_read_b128 v[180:183], v0 offset:18432\n"
    "buffer_load_dwordx2 v[240:241], v14, s[4:7], s98 offen offset:2048 sc1\n"
    "v_exp_f32 v209, v45\n"
    "v_exp_f32 v210, v46\n"
    "v_mfma_f32_16x16x32_bf16 v[60:63], v[188:191], v[112:115], v[60:63]\n"
    "ds_read_b128 v[184:187], v0 offset:19456\n"
    "v_exp_f32 v211, v47\n"
    "s_waitcnt lgkmcnt(3)\n"
    "v_mfma_f32_16x16x32_bf16 v[64:67], v[192:195], v[112:115], v[64:67]\n"
    "ds_read_b128 v[188:191], v0 offset:20480\n"
    "v_exp_f32 v212, v48\n"
    "v_exp_f32 v213, v49\n"
    "v_mfma_f32_16x16x32_bf16 v[68:71], v[196:199], v[112:115], v[68:71]\n"
    "ds_read_b128 v[192:195], v0 offset:21504\n"
    "buffer_load_dwordx2 v[242:243], v14, s[4:7], s98 offen offset:3072 sc1\n"
    "v_exp_f32 v214, v50\n"
    "v_mfma_f32_16x16x32_bf16 v[60:63], v[200:203], v[116:119], v[60:63]\n"
    "ds_read_b128 v[196:199], v0 offset:22528\n"
    "v_exp_f32 v215, v51\n"
    "v_fma_f32 v208, v208, s22, s22\n"
    "v_mfma_f32_16x16x32_bf16 v[64:67], v[204:207], v[116:119], v[64:67]\n"
    "ds_read_b128 v[200:203], v0 offset:23552\n"
    "v_fma_f32 v209, v209, s22, s22\n"
    "s_waitcnt lgkmcnt(3)\n"
    "v_mfma_f32_16x16x32_bf16 v[68:71], v[176:179], v[116:119], v[68:71]\n"
    "ds_read_b128 v[204:207], v0 offset:24576\n"
    "v_fma_f32 v210, v210, s22, s22\n"
    "v_fma_f32 v211, v211, s22, s22\n"
    "v_mfma_f32_16x16x32_bf16 v[60:63], v[180:183], v[120:123], v[60:63]\n"
    "ds_read_b128 v[176:179], v0 offset:25600\n"
    "buffer_load_dwordx2 v[244:245], v14, s[4:7], s97 offen offset:0 sc1\n"
    "v_fma_f32 v212, v212, s22, s22\n"
    "v_mfma_f32_16x16x32_bf16 v[64:67], v[184:187], v[120:123], v[64:67]\n"
    "ds_read_b128 v[180:183], v0 offset:26624\n"
    "v_fma_f32 v213, v213, s22, s22\n"
    "v_fma_f32 v214, v214, s22, s22\n"
    "buffer_load_dwordx4 v[96:99], v90, s[4:7], s52 offen sc1\n"
    "v_mfma_f32_16x16x32_bf16 v[68:71], v[188:191], v[120:123], v[68:71]\n"
    "ds_read_b128 v[184:187], v0 offset:27648\n"
    "v_fma_f32 v215, v215, s22, s22\n"
    "s_waitcnt lgkmcnt(3)\n"
    "v_mfma_f32_16x16x32_bf16 v[60:63], v[192:195], v[124:127], v[60:63]\n"
    "ds_read_b128 v[188:191], v0 offset:28672\n"
    "v_rcp_f32 v208, v208\n"
    "v_rcp_f32 v209, v209\n"
    "v_mfma_f32_16x16x32_bf16 v[64:67], v[196:199], v[124:127], v[64:67]\n"
    "ds_read_b128 v[192:195], v0 offset:29696\n"
    "v_rcp_f32 v210, v210\n"
    "buffer_load_dwordx4 v[100:103], v90, s[4:7], s53 offen sc1\n"
    "v_mfma_f32_16x16x32_bf16 v[68:71], v[200:203], v[124:127], v[68:71]\n"
    "ds_read_b128 v[196:199], v0 offset:30720\n"
    "v_rcp_f32 v211, v211\n"
    "v_rcp_f32 v212, v212\n"
    "v_mfma_f32_16x16x32_bf16 v[60:63], v[204:207], v[128:131], v[60:63]\n"
    "ds_read_b128 v[200:203], v0 offset:31744\n"
    "v_rcp_f32 v213, v213\n"
    "s_waitcnt lgkmcnt(3)\n"
    "v_mfma_f32_16x16x32_bf16 v[64:67], v[176:179], v[128:131], v[64:67]\n"
    "ds_read_b128 v[204:207], v0 offset:32768\n"
    "v_rcp_f32 v214, v214\n"
    "buffer_load_dwordx4 v[104:107], v90, s[4:7], s55 offen sc1\n"
    "v_mfma_f32_16x16x32_bf16 v[68:71], v[180:183], v[128:131], v[68:71]\n"
    "ds_read_b128 v[176:179], v0 offset:33792\n"
    "v_rcp_f32 v215, v215\n"
    "v_mul_f32 v44, v44, v208\n"
    "v_mfma_f32_16x16x32_bf16 v[60:63], v[184:187], v[132:135], v[60:63]\n"
    "ds_read_b128 v[180:183], v0 offset:34816\n"
    "v_mul_f32 v45, v45, v209\n"
    "v_mfma_f32_16x16x32_bf16 v[64:67], v[188:191], v[132:135], v[64:67]\n"
    "ds_read_b128 v[184:187], v0 offset:35840\n"
    "v_mul_f32 v46, v46, v210\n"
    "v_mul_f32 v47, v47, v211\n"
    "buffer_load_dwordx4 v[108:111], v90, s[4:7], s56 offen sc1\n"
    "s_waitcnt lgkmcnt(3)\n"
    "v_mfma_f32_16x16x32_bf16 v[68:71], v[192:195], v[132:135], v[68:71]\n"
    "ds_read_b128 v[188:191], v0 offset:36864\n"
    "v_mul_f32 v48, v48, v212\n"
    "v_mfma_f32_16x16x32_bf16 v[60:63], v[196:199], v[136:139], v[60:63]\n"
    "ds_read_b128 v[192:195], v0 offset:37888\n"
    "v_mul_f32 v49, v49, v213\n"
    "v_mul_f32 v50, v50, v214\n"
    "v_mfma_f32_16x16x32_bf16 v[64:67], v[200:203], v[136:139], v[64:67]\n"
    "ds_read_b128 v[196:199], v0 offset:38912\n"
    "v_mul_f32 v51, v51, v215\n"
    "buffer_load_dwordx4 v[112:115], v90, s[4:7], s58 offen sc1\n"
    "v_mfma_f32_16x16x32_bf16 v[68:71], v[204:207], v[136:139], v[68:71]\n"
    "ds_read_b128 v[200:203], v0 offset:39936\n"
    "v_cvt_pk_bf16_f32 v216, v44, v45\n"
    "v_cvt_pk_bf16_f32 v217, v46, v47\n"
    "s_waitcnt lgkmcnt(3)\n"
    "v_mfma_f32_16x16x32_bf16 v[60:63], v[176:179], v[140:143], v[60:63]\n"
    "ds_read_b128 v[204:207], v0 offset:40960\n"
    "v_cvt_pk_bf16_f32 v218, v48, v49\n"
    "v_mfma_f32_16x16x32_bf16 v[64:67], v[180:183], v[140:143], v[64:67]\n"
    "ds_read_b128 v[176:179], v0 offset:41984\n"
    "v_cvt_pk_bf16_f32 v219, v50, v51\n"
    "buffer_store_dwordx4 v[216:219], v0, s[4:7], s42 offen offset:0 sc1\n"
    "buffer_load_dwordx4 v[116:119], v90, s[4:7], s59 offen sc1\n"
    "v_mfma_f32_16x16x32_bf16 v[68:71], v[184:187], v[140:143], v[68:71]\n"
    "ds_read_b128 v[180:183], v0 offset:43008\n"
    "v_mul_f32 v52, v52, v77\n"
    "v_mfma_f32_16x16x32_bf16 v[60:63], v[188:191], v[144:147], v[60:63]\n"
    "ds_read_b128 v[184:187], v0 offset:44032\n"
    "v_mul_f32 v53, v53, v77\n"
    "v_mul_f32 v54, v54, v77\n"
    "s_waitcnt lgkmcnt(3)\n"
    "v_mfma_f32_16x16x32_bf16 v[64:67], v[192:195], v[144:147], v[64:67]\n"
    "ds_read_b128 v[188:191], v0 offset:45056\n"
    "v_mul_f32 v55, v55, v77\n"
    "buffer_load_dwordx4 v[120:123], v90, s[4:7], s61 offen sc1\n"
    "v_mfma_f32_16x16x32_bf16 v[68:71], v[196:199], v[144:147], v[68:71]\n"
    "ds_read_b128 v[192:195], v0 offset:46080\n"
    "v_mul_f32 v56, v56, v77\n"
    "v_mul_f32 v57, v57, v77\n"
    "v_mfma_f32_16x16x32_bf16 v[60:63], v[200:203], v[148:151], v[60:63]\n"
    "ds_read_b128 v[196:199], v0 offset:47104\n"
    "v_mul_f32 v58, v58, v77\n"
    "v_mfma_f32_16x16x32_bf16 v[64:67], v[204:207], v[148:151], v[64:67]\n"
    "ds_read_b128 v[200:203], v0 offset:48128\n"
    "v_mul_f32 v59, v59, v77\n"
    "v_exp_f32 v208, v52\n"
    "buffer_load_dwordx4 v[124:127], v90, s[4:7], s62 offen sc1\n"
    "s_waitcnt lgkmcnt(3)\n"
    "v_mfma_f32_16x16x32_bf16 v[68:71], v[176:179], v[148:151], v[68:71]\n"
    "ds_read_b128 v[204:207], v0 offset:49152\n"
    "v_exp_f32 v209, v53\n"
    "v_mfma_f32_16x16x32_bf16 v[60:63], v[180:183], v[152:155], v[60:63]\n"
    "ds_read_b128 v[176:179], v0 offset:50176\n"
    "v_exp_f32 v210, v54\n"
    "v_exp_f32 v211, v55\n"
    "v_mfma_f32_16x16x32_bf16 v[64:67], v[184:187], v[152:155], v[64:67]\n"
    "ds_read_b128 v[180:183], v0 offset:51200\n"
    "v_exp_f32 v212, v56\n"
    "buffer_load_dwordx4 v[128:131], v91, s[4:7], s52 offen sc1\n"
    "v_mfma_f32_16x16x32_bf16 v[68:71], v[188:191], v[152:155], v[68:71]\n"
    "ds_read_b128 v[184:187], v0 offset:52224\n"
    "v_exp_f32 v213, v57\n"
    "v_exp_f32 v214, v58\n"
    "s_waitcnt lgkmcnt(3)\n"
    "v_mfma_f32_16x16x32_bf16 v[60:63], v[192:195], v[156:159], v[60:63]\n"
    "ds_read_b128 v[188:191], v0 offset:53248\n"
    "v_exp_f32 v215, v59\n"
    "v_mfma_f32_16x16x32_bf16 v[64:67], v[196:199], v[156:159], v[64:67]\n"
    "ds_read_b128 v[192:195], v0 offset:54272\n"
    "v_fma_f32 v208, v208, s22, s22\n"
    "buffer_load_dwordx4 v[132:135], v91, s[4:7], s53 offen sc1\n"
    "v_mfma_f32_16x16x32_bf16 v[68:71], v[200:203], v[156:159], v[68:71]\n"
    "ds_read_b128 v[196:199], v0 offset:55296\n"
    "v_fma_f32 v209, v209, s22, s22\n"
    "v_fma_f32 v210, v210, s22, s22\n"
    "v_mfma_f32_16x16x32_bf16 v[60:63], v[204:207], v[160:163], v[60:63]\n"
    "ds_read_b128 v[200:203], v0 offset:56320\n"
    "v_fma_f32 v211, v211, s22, s22\n"
    "s_waitcnt lgkmcnt(3)\n"
    "v_mfma_f32_16x16x32_bf16 v[64:67], v[176:179], v[160:163], v[64:67]\n"
    "ds_read_b128 v[204:207], v0 offset:57344\n"
    "v_fma_f32 v212, v212, s22, s22\n"
    "v_fma_f32 v213, v213, s22, s22\n"
    "buffer_load_dwordx4 v[136:139], v91, s[4:7], s55 offen sc1\n"
    "v_mfma_f32_16x16x32_bf16 v[68:71], v[180:183], v[160:163], v[68:71]\n"
    "ds_read_b128 v[176:179], v0 offset:58368\n"
    "v_fma_f32 v214, v214, s22, s22\n"
    "v_mfma_f32_16x16x32_bf16 v[60:63], v[184:187], v[164:167], v[60:63]\n"
    "ds_read_b128 v[180:183], v0 offset:59392\n"
    "v_fma_f32 v215, v215, s22, s22\n"
    "v_rcp_f32 v208, v208\n"
    "v_mfma_f32_16x16x32_bf16 v[64:67], v[188:191], v[164:167], v[64:67]\n"
    "ds_read_b128 v[184:187], v0 offset:60416\n"
    "v_rcp_f32 v209, v209\n"
    "buffer_load_dwordx4 v[140:143], v91, s[4:7], s56 offen sc1\n"
    "s_waitcnt lgkmcnt(3)\n"
    "v_mfma_f32_16x16x32_bf16 v[68:71], v[192:195], v[164:167], v[68:71]\n"
    "ds_read_b64 v[188:189], v2 offset:24576\n"
    "v_rcp_f32 v210, v210\n"
    "v_rcp_f32 v211, v211\n"
    "v_mfma_f32_16x16x32_bf16 v[60:63], v[196:199], v[168:171], v[60:63]\n"
    "ds_read_b64 v[192:193], v2 offset:25600\n"
    "v_rcp_f32 v212, v212\n"
    "v_mfma_f32_16x16x32_bf16 v[64:67], v[200:203], v[168:171], v[64:67]\n"
    "ds_read_b64 v[196:197], v2 offset:26624\n"
    "v_rcp_f32 v213, v213\n"
    "v_rcp_f32 v214, v214\n"
    "v_mfma_f32_16x16x32_bf16 v[68:71], v[204:207], v[168:171], v[68:71]\n"
    "v_rcp_f32 v215, v215\n"
    "s_waitcnt lgkmcnt(3)\n"
    "v_mfma_f32_16x16x32_bf16 v[60:63], v[176:179], v[172:175], v[60:63]\n"
    "v_mul_f32 v52, v52, v208\n"
    "v_mul_f32 v53, v53, v209\n"
    "v_mfma_f32_16x16x32_bf16 v[64:67], v[180:183], v[172:175], v[64:67]\n"
    "v_mul_f32 v54, v54, v210\n"
    "v_mul_f32 v55, v55, v211\n"
    "v_mfma_f32_16x16x32_bf16 v[68:71], v[184:187], v[172:175], v[68:71]\n"
    "v_mul_f32 v56, v56, v212\n"
    "v_mul_f32 v57, v57, v213\n"
    "s_waitcnt lgkmcnt(2)\n"
    "v_mfma_f32_16x16x16_bf16 v[60:63], v[188:189], v[74:75], v[60:63]\n"
    "v_mul_f32 v58, v58, v214\n"
    "v_mul_f32 v59, v59, v215\n"
    "s_waitcnt lgkmcnt(1)\n"
    "v_mfma_f32_16x16x16_bf16 v[64:67], v[192:193], v[74:75], v[64:67]\n"
    "v_cvt_pk_bf16_f32 v220, v52, v53\n"
    "v_mov_b32 v176, v90\n"
    "s_waitcnt lgkmcnt(0)\n"
    "v_mfma_f32_16x16x16_bf16 v[68:71], v[196:197], v[74:75], v[68:71]\n"
    "v_cvt_pk_bf16_f32 v221, v54, v55\n"
    "v_cvt_pk_bf16_f32 v222, v56, v57\n"
    "v_cvt_pk_bf16_f32 v223, v58, v59\n"
    "buffer_store_dwordx4 v[220:223], v0, s[4:7], s42 offen offset:1024 sc1\n"
    "v_mov_b32 v177, v91\n"
    "v_add_u32 v178, 57344, v90\n"
    "v_add_u32 v179, 57344, v91\n"
    "v_add_u32 v14, 12288, v14\n"
    "s_waitcnt vmcnt(10)\n"
    "v_max3_u32 v7, v108, v100, v104\n"
    "v_max_u32 v7, v7, v96\n"
    "v_cmp_eq_u32 vcc, -1, v7\n"
    "s_cbranch_vccnz f0s_45_%=\n"
    "f0r_46_%=:\n"
    "v_mfma_f32_16x16x32_bf16 v[16:19], a[96:99], v[96:99], v[16:19]\n"
    "buffer_load_dwordx4 v[144:147], v177, s[4:7], s58 offen sc1\n"
    "v_mfma_f32_16x16x32_bf16 v[20:23], a[100:103], v[96:99], v[20:23]\n"
    "v_mul_f32 v60, v60, v77\n"
    "v_mul_f32 v61, v61, v77\n"
    "v_mfma_f32_16x16x32_bf16 v[24:27], a[104:107], v[96:99], v[24:27]\n"
    "v_mul_f32 v62, v62, v77\n"
    "v_mul_f32 v63, v63, v77\n"
    "v_mfma_f32_16x16x32_bf16 v[28:31], a[108:111], v[96:99], v[28:31]\n"
    "v_mul_f32 v64, v64, v77\n"
    "v_mul_f32 v65, v65, v77\n"
    "v_mfma_f32_16x16x32_bf16 v[32:35], a[112:115], v[96:99], v[32:35]\n"
    "v_mul_f32 v66, v66, v77\n"
    "v_mul_f32 v67, v67, v77\n"
    "v_mfma_f32_16x16x32_bf16 v[16:19], a[116:119], v[100:103], v[16:19]\n"
    "buffer_load_dwordx4 v[148:151], v177, s[4:7], s59 offen sc1\n"
    "v_mfma_f32_16x16x32_bf16 v[20:23], a[120:123], v[100:103], v[20:23]\n"
    "v_exp_f32 v208, v60\n"
    "v_mfma_f32_16x16x32_bf16 v[24:27], a[124:127], v[100:103], v[24:27]\n"
    "v_exp_f32 v209, v61\n"
    "v_mfma_f32_16x16x32_bf16 v[28:31], a[128:131], v[100:103], v[28:31]\n"
    "v_exp_f32 v210, v62\n"
    "v_mfma_f32_16x16x32_bf16 v[32:35], a[132:135], v[100:103], v[32:35]\n"
    "v_exp_f32 v211, v63\n"
    "v_mfma_f32_16x16x32_bf16 v[16:19], a[156:159], v[104:107], v[16:19]\n"
    "buffer_load_dwordx4 v[152:155], v177, s[4:7], s61 offen sc1\n"
    "v_exp_f32 v212, v64\n"
    "v_exp_f32 v213, v65\n"
    "v_mfma_f32_16x16x32_bf16 v[20:23], a[160:163], v[104:107], v[20:23]\n"
    "v_exp_f32 v214, v66\n"
    "v_exp_f32 v215, v67\n"
    "v_mfma_f32_16x16x32_bf16 v[24:27], a[164:167], v[104:107], v[24:27]\n"
    "v_fma_f32 v208, v208, s22, s22\n"
    "v_fma_f32 v209, v209, s22, s22\n"
    "v_mfma_f32_16x16x32_bf16 v[28:31], a[168:171], v[104:107], v[28:31]\n"
    "v_fma_f32 v210, v210, s22, s22\n"
    "v_fma_f32 v211, v211, s22, s22\n"
    "v_mfma_f32_16x16x32_bf16 v[32:35], a[172:175], v[104:107], v[32:35]\n"
    "v_fma_f32 v212, v212, s22, s22\n"
    "v_fma_f32 v213, v213, s22, s22\n"
    "v_mfma_f32_16x16x32_bf16 v[16:19], a[176:179], v[108:111], v[16:19]\n"
    "buffer_load_dwordx4 v[156:159], v177, s[4:7], s62 offen sc1\n"
    "v_fma_f32 v214, v214, s22, s22\n"
    "v_fma_f32 v215, v215, s22, s22\n"
    "v_mfma_f32_16x16x32_bf16 v[20:23], a[180:183], v[108:111], v[20:23]\n"
    "v_rcp_f32 v208, v208\n"
    "v_rcp_f32 v209, v209\n"
    "v_mfma_f32_16x16x32_bf16 v[24:27], a[184:187], v[108:111], v[24:27]\n"
    "s_waitcnt vmcnt(9)\n"
    "v_max3_u32 v7, v124, v116, v120\n"
    "v_rcp_f32 v210, v210\n"
    "v_rcp_f32 v211, v211\n"
    "v_mfma_f32_16x16x32_bf16 v[28:31], a[188:191], v[108:111], v[28:31]\n"
    "v_max_u32 v7, v7, v112\n"
    "v_rcp_f32 v212, v212\n"
    "v_rcp_f32 v213, v213\n"
    "v_mfma_f32_16x16x32_bf16 v[32:35], a[192:195], v[108:111], v[32:35]\n"
    "v_cmp_eq_u32 vcc, -1, v7\n"
    "s_cbranch_vccnz f1s_47_%=\n"
    "f1r_48_%=:\n"
    "v_rcp_f32 v214, v214\n"
    "v_rcp_f32 v215, v215\n"
    "v_mfma_f32_16x16x32_bf16 v[16:19], a[216:219], v[112:115], v[16:19]\n"
    "buffer_load_dwordx2 v[160:161], v14, s[4:7], s44 offen offset:1024 sc1\n"
    "v_mul_f32 v60, v60, v208\n"
    "v_mul_f32 v61, v61, v209\n"
    "v_mfma_f32_16x16x32_bf16 v[20:23], a[220:223], v[112:115], v[20:23]\n"
    "v_mul_f32 v62, v62, v210\n"
    "v_mul_f32 v63, v63, v211\n"
    "v_mfma_f32_16x16x32_bf16 v[24:27], a[224:227], v[112:115], v[24:27]\n"
    "v_mul_f32 v64, v64, v212\n"
    "v_mul_f32 v65, v65, v213\n"
    "v_mfma_f32_16x16x32_bf16 v[28:31], a[228:231], v[112:115], v[28:31]\n"
    "v_mul_f32 v66, v66, v214\n"
    "v_mul_f32 v67, v67, v215\n"
    "v_mfma_f32_16x16x32_bf16 v[32:35], a[232:235], v[112:115], v[32:35]\n"
    "v_cvt_pk_bf16_f32 v224, v60, v61\n"
    "v_cvt_pk_bf16_f32 v225, v62, v63\n"
    "v_mfma_f32_16x16x32_bf16 v[16:19], a[236:239], v[116:119], v[16:19]\n"
    "buffer_load_dwordx4 v[164:167], v176, s[4:7], s54 offen sc1\n"
    "v_cvt_pk_bf16_f32 v226, v64, v65\n"
    "v_cvt_pk_bf16_f32 v227, v66, v67\n"
    "v_mfma_f32_16x16x32_bf16 v[20:23], a[240:243], v[116:119], v[20:23]\n"
    "buffer_store_dwordx4 v[224:227], v0, s[4:7], s42 offen offset:2048 sc1\n"
    "v_mul_f32 v68, v68, v77\n"
    "v_mfma_f32_16x16x32_bf16 v[24:27], a[244:247], v[116:119], v[24:27]\n"
    "v_mul_f32 v69, v69, v77\n"
    "v_mul_f32 v70, v70, v77\n"
    "v_mfma_f32_16x16x32_bf16 v[28:31], a[248:251], v[116:119], v[28:31]\n"
    "v_mul_f32 v71, v71, v77\n"
    "v_exp_f32 v208, v68\n"
    "v_mfma_f32_16x16x32_bf16 v[32:35], a[252:255], v[116:119], v[32:35]\n"
    "v_exp_f32 v209, v69\n"
    "v_exp_f32 v210, v70\n"
    "v_mfma_f32_16x16x32_bf16 v[16:19], a[20:23], v[120:123], v[16:19]\n"
    "buffer_load_dwordx4 v[168:171], v176, s[4:7], s57 offen sc1\n"
    "v_exp_f32 v211, v71\n"
    "v_fma_f32 v208, v208, s22, s22\n"
    "v_mfma_f32_16x16x32_bf16 v[20:23], a[24:27], v[120:123], v[20:23]\n"
    "v_fma_f32 v209, v209, s22, s22\n"
    "v_fma_f32 v210, v210, s22, s22\n"
    "v_mfma_f32_16x16x32_bf16 v[24:27], a[28:31], v[120:123], v[24:27]\n"
    "v_fma_f32 v211, v211, s22, s22\n"
    "v_rcp_f32 v208, v208\n"
    "v_mfma_f32_16x16x32_bf16 v[28:31], a[32:35], v[120:123], v[28:31]\n"
    "v_rcp_f32 v209, v209\n"
    "v_rcp_f32 v210, v210\n"
    "v_mfma_f32_16x16x32_bf16 v[32:35], a[36:39], v[120:123], v[32:35]\n"
    "v_rcp_f32 v211, v211\n"
    "v_mul_f32 v68, v68, v208\n"
    "v_mfma_f32_16x16x32_bf16 v[16:19], a[40:43], v[124:127], v[16:19]\n"
    "buffer_load_dwordx4 v[172:175], v176, s[4:7], s60 offen sc1\n"
    "v_mul_f32 v69, v69, v209\n"
    "v_mul_f32 v70, v70, v210\n"
    "v_mul_f32 v71, v71, v211\n"
    "v_cvt_pk_bf16_f32 v228, v68, v69\n"
    "v_cvt_pk_bf16_f32 v229, v70, v71\n"
    "s_nop 0\n"
    "v_permlane32_swap_b32 v228, v230\n"
    "v_permlane32_swap_b32 v229, v231\n"
    "buffer_store_dwordx4 v[228:231], v12, s[4:7], s43 offen sc1\n"
    "v_mfma_f32_16x16x32_bf16 v[20:23], a[44:47], v[124:127], v[20:23]\n"
    "s_xor_b32 s24, s24, 0xffff0000\n"
    "s_xor_b32 s47, s47, 0x10000\n"
    "v_mfma_f32_16x16x32_bf16 v[24:27], a[48:51], v[124:127], v[24:27]\n"
    "s_waitcnt vmcnt(11)\n"
    "v_max3_u32 v7, v140, v132, v136\n"
    "v_add_u32 v180, s65, v0\n"
    "v_mfma_f32_16x16x32_bf16 v[28:31], a[52:55], v[124:127], v[28:31]\n"
    "v_max_u32 v7, v7, v128\n"
    "v_add_u32 v181, s66, v0\n"
    "v_mfma_f32_16x16x32_bf16 v[32:35], a[56:59], v[124:127], v[32:35]\n"
    "v_cmp_eq_u32 vcc, -1, v7\n"
    "s_cbranch_vccnz f2s_49_%=\n"
    "f2r_50_%=:\n"
    "v_mfma_f32_16x16x32_bf16 v[36:39], a[96:99], v[128:131], 0\n"
    "buffer_load_dwordx4 v[96:99], v176, s[4:7], s63 offen sc1\n"
    "v_add_u32 v182, s67, v0\n"
    "v_mfma_f32_16x16x32_bf16 v[40:43], a[100:103], v[128:131], 0\n"
    "s_and_b32 s32, %2, 1\n"
    "s_mul_i32 s31, s32, 2040\n"
    "v_mfma_f32_16x16x32_bf16 v[44:47], a[104:107], v[128:131], 0\n"
    "v_subrev_u32 v183, s31, v182\n"
    "s_mul_i32 s31, s32, 20416\n"
    "v_mfma_f32_16x16x32_bf16 v[48:51], a[108:111], v[128:131], 0\n"
    "s_sub_u32 s27, s74, s31\n"
    "s_mul_i32 s31, s32, 20476\n"
    "v_mfma_f32_16x16x32_bf16 v[52:55], a[112:115], v[128:131], 0\n"
    "s_sub_u32 s28, s74, s31\n"
    "v_mfma_f32_16x16x32_bf16 v[36:39], a[116:119], v[132:135], v[36:39]\n"
    "buffer_load_dwordx4 v[100:103], v176, s[4:7], s64 offen sc1\n"
    "v_mfma_f32_16x16x32_bf16 v[40:43], a[120:123], v[132:135], v[40:43]\n"
    "v_mfma_f32_16x16x32_bf16 v[44:47], a[124:127], v[132:135], v[44:47]\n"
    "v_mfma_f32_16x16x32_bf16 v[48:51], a[128:131], v[132:135], v[48:51]\n"
    "v_mfma_f32_16x16x32_bf16 v[52:55], a[132:135], v[132:135], v[52:55]\n"
    "v_mfma_f32_16x16x32_bf16 v[36:39], a[156:159], v[136:139], v[36:39]\n"
    "buffer_load_dwordx2 v[104:105], v14, s[4:7], s45 offen offset:1024 sc1\n"
    "v_mfma_f32_16x16x32_bf16 v[40:43], a[160:163], v[136:139], v[40:43]\n"
    "v_mfma_f32_16x16x32_bf16 v[44:47], a[164:167], v[136:139], v[44:47]\n"
    "v_mfma_f32_16x16x32_bf16 v[48:51], a[168:171], v[136:139], v[48:51]\n"
    "v_mfma_f32_16x16x32_bf16 v[52:55], a[172:175], v[136:139], v[52:55]\n"
    "v_mfma_f32_16x16x32_bf16 v[36:39], a[176:179], v[140:143], v[36:39]\n"
    "buffer_load_dwordx4 v[108:111], v177, s[4:7], s54 offen sc1\n"
    "v_mfma_f32_16x16x32_bf16 v[40:43], a[180:183], v[140:143], v[40:43]\n"
    "v_mfma_f32_16x16x32_bf16 v[44:47], a[184:187], v[140:143], v[44:47]\n"
    "s_waitcnt vmcnt(10)\n"
    "v_max3_u32 v7, v156, v148, v152\n"
    "v_mfma_f32_16x16x32_bf16 v[48:51], a[188:191], v[140:143], v[48:51]\n"
    "v_max_u32 v7, v7, v144\n"
    "v_mfma_f32_16x16x32_bf16 v[52:55], a[192:195], v[140:143], v[52:55]\n"
    "v_cmp_eq_u32 vcc, -1, v7\n"
    "s_cbranch_vccnz f3s_51_%=\n"
    "f3r_52_%=:\n"
    "v_mfma_f32_16x16x32_bf16 v[36:39], a[216:219], v[144:147], v[36:39]\n"
    "buffer_load_dwordx4 v[112:115], v177, s[4:7], s57 offen sc1\n"
    "v_mfma_f32_16x16x32_bf16 v[40:43], a[220:223], v[144:147], v[40:43]\n"
    "v_mfma_f32_16x16x32_bf16 v[44:47], a[224:227], v[144:147], v[44:47]\n"
    "v_mfma_f32_16x16x32_bf16 v[48:51], a[228:231], v[144:147], v[48:51]\n"
    "v_mfma_f32_16x16x32_bf16 v[52:55], a[232:235], v[144:147], v[52:55]\n"
    "v_mfma_f32_16x16x32_bf16 v[36:39], a[236:239], v[148:151], v[36:39]\n"
    "buffer_load_dwordx4 v[116:119], v177, s[4:7], s60 offen sc1\n"
    "v_mfma_f32_16x16x32_bf16 v[40:43], a[240:243], v[148:151], v[40:43]\n"
    "v_mfma_f32_16x16x32_bf16 v[44:47], a[244:247], v[148:151], v[44:47]\n"
    "v_mfma_f32_16x16x32_bf16 v[48:51], a[248:251], v[148:151], v[48:51]\n"
    "v_mfma_f32_16x16x32_bf16 v[52:55], a[252:255], v[148:151], v[52:55]\n"
    "v_mfma_f32_16x16x32_bf16 v[36:39], a[20:23], v[152:155], v[36:39]\n"
    "buffer_load_dwordx4 v[120:123], v177, s[4:7], s63 offen sc1\n"
    "v_mul_u32_u24 v56, s47, v246\n"
    "v_mfma_f32_16x16x32_bf16 v[40:43], a[24:27], v[152:155], v[40:43]\n"
    "v_and_b32 v57, s24, v246\n"
    "v_mul_u32_u24 v58, s47, v247\n"
    "v_mfma_f32_16x16x32_bf16 v[44:47], a[28:31], v[152:155], v[44:47]\n"
    "v_and_b32 v59, s24, v247\n"
    "v_mul_u32_u24 v60, s47, v248\n"
    "v_mfma_f32_16x16x32_bf16 v[48:51], a[32:35], v[152:155], v[48:51]\n"
    "v_and_b32 v61, s24, v248\n"
    "v_mul_u32_u24 v62, s47, v249\n"
    "v_mfma_f32_16x16x32_bf16 v[52:55], a[36:39], v[152:155], v[52:55]\n"
    "v_and_b32 v63, s24, v249\n"
    "v_mul_u32_u24 v64, s47, v250\n"
    "v_mfma_f32_16x16x32_bf16 v[36:39], a[40:43], v[156:159], v[36:39]\n"
    "buffer_load_dwordx4 v[124:127], v177, s[4:7], s64 offen sc1\n"
    "v_and_b32 v65, s24, v250\n"
    "v_mfma_f32_16x16x32_bf16 v[40:43], a[44:47], v[156:159], v[40:43]\n"
    "v_mul_u32_u24 v66, s47, v251\n"
    "v_and_b32 v67, s24, v251\n"
    "v_mfma_f32_16x16x32_bf16 v[44:47], a[48:51], v[156:159], v[44:47]\n"
    "s_waitcnt vmcnt(9)\n"
    "v_max3_u32 v7, v172, v164, v168\n"
    "v_mul_u32_u24 v68, s47, v252\n"
    "v_mfma_f32_16x16x32_bf16 v[48:51], a[52:55], v[156:159], v[48:51]\n"
    "v_max_u32 v7, v7, v160\n"
    "v_and_b32 v69, s24, v252\n"
    "v_mfma_f32_16x16x32_bf16 v[52:55], a[56:59], v[156:159], v[52:55]\n"
    "v_cmp_eq_u32 vcc, -1, v7\n"
    "s_cbranch_vccnz f4s_53_%=\n"
    "f4r_54_%=:\n"
    "v_mfma_f32_16x16x16_bf16 v[16:19], v[236:237], v[160:161], v[16:19]\n"
    "v_mul_u32_u24 v70, s47, v253\n"
    "v_and_b32 v71, s24, v253\n"
    "v_mfma_f32_16x16x16_bf16 v[20:23], v[238:239], v[160:161], v[20:23]\n"
    "v_mul_u32_u24 v72, s47, v254\n"
    "v_and_b32 v73, s24, v254\n"
    "v_mfma_f32_16x16x16_bf16 v[24:27], v[240:241], v[160:161], v[24:27]\n"
    "v_mul_u32_u24 v74, s47, v255\n"
    "v_and_b32 v75, s24, v255\n"
    "v_mfma_f32_16x16x16_bf16 v[28:31], v[242:243], v[160:161], v[28:31]\n"
    "v_mfma_f32_16x16x16_bf16 v[32:35], v[244:245], v[160:161], v[32:35]\n"
    "v_mfma_f32_16x16x32_bf16 v[16:19], a[136:139], v[164:167], v[16:19]\n"
    "buffer_load_dwordx4 v[128:131], v178, s[4:7], s52 offen sc1\n"
    "v_mfma_f32_16x16x32_bf16 v[20:23], a[140:143], v[164:167], v[20:23]\n"
    "v_mfma_f32_16x16x32_bf16 v[24:27], a[144:147], v[164:167], v[24:27]\n"
    "buffer_load_dwordx4 v[132:135], v178, s[4:7], s53 offen sc1\n"
    "v_mfma_f32_16x16x32_bf16 v[28:31], a[148:151], v[164:167], v[28:31]\n"
    "v_mfma_f32_16x16x32_bf16 v[32:35], a[152:155], v[164:167], v[32:35]\n"
    "v_mfma_f32_16x16x32_bf16 v[16:19], a[196:199], v[168:171], v[16:19]\n"
    "buffer_load_dwordx4 v[136:139], v178, s[4:7], s55 offen sc1\n"
    "v_mfma_f32_16x16x32_bf16 v[20:23], a[200:203], v[168:171], v[20:23]\n"
    "v_mfma_f32_16x16x32_bf16 v[24:27], a[204:207], v[168:171], v[24:27]\n"
    "v_mfma_f32_16x16x32_bf16 v[28:31], a[208:211], v[168:171], v[28:31]\n"
    "v_mfma_f32_16x16x32_bf16 v[32:35], a[212:215], v[168:171], v[32:35]\n"
    "v_mfma_f32_16x16x32_bf16 v[16:19], a[0:3], v[172:175], v[16:19]\n"
    "buffer_load_dwordx4 v[140:143], v178, s[4:7], s56 offen sc1\n"
    "v_mfma_f32_16x16x32_bf16 v[20:23], a[4:7], v[172:175], v[20:23]\n"
    "v_mfma_f32_16x16x32_bf16 v[24:27], a[8:11], v[172:175], v[24:27]\n"
    "s_waitcnt vmcnt(8)\n"
    "v_max3_u32 v7, v108, v100, v104\n"
    "v_mfma_f32_16x16x32_bf16 v[28:31], a[12:15], v[172:175], v[28:31]\n"
    "v_max_u32 v7, v7, v96\n"
    "v_mfma_f32_16x16x32_bf16 v[32:35], a[16:19], v[172:175], v[32:35]\n"
    "v_cmp_eq_u32 vcc, -1, v7\n"
    "s_cbranch_vccnz f5s_55_%=\n"
    "f5r_56_%=:\n"
    "v_mfma_f32_16x16x32_bf16 v[16:19], a[60:63], v[96:99], v[16:19]\n"
    "buffer_load_dwordx4 v[144:147], v178, s[4:7], s58 offen sc1\n"
    "v_mfma_f32_16x16x32_bf16 v[20:23], a[64:67], v[96:99], v[20:23]\n"
    "v_mfma_f32_16x16x32_bf16 v[24:27], a[68:71], v[96:99], v[24:27]\n"
    "v_mfma_f32_16x16x32_bf16 v[28:31], a[72:75], v[96:99], v[28:31]\n"
    "v_mfma_f32_16x16x32_bf16 v[32:35], a[76:79], v[96:99], v[32:35]\n"
    "v_mfma_f32_16x16x32_bf16 v[16:19], a[80:83], v[100:103], v[16:19]\n"
    "buffer_load_dwordx4 v[148:151], v178, s[4:7], s59 offen sc1\n"
    "v_mfma_f32_16x16x32_bf16 v[20:23], a[84:87], v[100:103], v[20:23]\n"
    "v_mfma_f32_16x16x32_bf16 v[24:27], a[88:91], v[100:103], v[24:27]\n"
    "v_mfma_f32_16x16x32_bf16 v[28:31], a[92:95], v[100:103], v[28:31]\n"
    "v_mfma_f32_16x16x32_bf16 v[32:35], v[232:235], v[100:103], v[32:35]\n"
    "v_mfma_f32_16x16x16_bf16 v[36:39], v[236:237], v[104:105], v[36:39]\n"
    "buffer_load_dwordx4 v[152:155], v178, s[4:7], s61 offen sc1\n"
    "v_mfma_f32_16x16x16_bf16 v[40:43], v[238:239], v[104:105], v[40:43]\n"
    "v_mfma_f32_16x16x16_bf16 v[44:47], v[240:241], v[104:105], v[44:47]\n"
    "s_barrier\n"
    "v_mfma_f32_16x16x16_bf16 v[48:51], v[242:243], v[104:105], v[48:51]\n"
    "v_mfma_f32_16x16x16_bf16 v[52:55], v[244:245], v[104:105], v[52:55]\n"
    "ds_write_b128 v180, v[16:19] offset:0\n"
    "v_mfma_f32_16x16x32_bf16 v[36:39], a[136:139], v[108:111], v[36:39]\n"
    "buffer_load_dwordx4 v[156:159], v178, s[4:7], s62 offen sc1\n"
    "v_mfma_f32_16x16x32_bf16 v[40:43], a[140:143], v[108:111], v[40:43]\n"
    "v_mfma_f32_16x16x32_bf16 v[44:47], a[144:147], v[108:111], v[44:47]\n"
    "s_waitcnt vmcnt(8)\n"
    "v_max3_u32 v7, v124, v116, v120\n"
    "v_mfma_f32_16x16x32_bf16 v[48:51], a[148:151], v[108:111], v[48:51]\n"
    "v_max_u32 v7, v7, v112\n"
    "v_mfma_f32_16x16x32_bf16 v[52:55], a[152:155], v[108:111], v[52:55]\n"
    "v_cmp_eq_u32 vcc, -1, v7\n"
    "s_cbranch_vccnz f6s_57_%=\n"
    "f6r_58_%=:\n"
    "ds_write_b128 v180, v[20:23] offset:1024\n"
    "v_mfma_f32_16x16x32_bf16 v[36:39], a[196:199], v[112:115], v[36:39]\n"
    "buffer_load_dwordx4 v[160:163], v179, s[4:7], s52 offen sc1\n"
    "v_mfma_f32_16x16x32_bf16 v[40:43], a[200:203], v[112:115], v[40:43]\n"
    "v_mfma_f32_16x16x32_bf16 v[44:47], a[204:207], v[112:115], v[44:47]\n"
    "v_mfma_f32_16x16x32_bf16 v[48:51], a[208:211], v[112:115], v[48:51]\n"
    "v_mfma_f32_16x16x32_bf16 v[52:55], a[212:215], v[112:115], v[52:55]\n"
    "ds_write_b128 v180, v[24:27] offset:2048\n"
    "v_mfma_f32_16x16x32_bf16 v[36:39], a[0:3], v[116:119], v[36:39]\n"
    "buffer_load_dwordx4 v[164:167], v179, s[4:7], s53 offen sc1\n"
    "v_mfma_f32_16x16x32_bf16 v[40:43], a[4:7], v[116:119], v[40:43]\n"
    "v_mfma_f32_16x16x32_bf16 v[44:47], a[8:11], v[116:119], v[44:47]\n"
    "v_mfma_f32_16x16x32_bf16 v[48:51], a[12:15], v[116:119], v[48:51]\n"
    "v_mfma_f32_16x16x32_bf16 v[52:55], a[16:19], v[116:119], v[52:55]\n"
    "ds_write_b128 v180, v[28:31] offset:3072\n"
    "v_mfma_f32_16x16x32_bf16 v[36:39], a[60:63], v[120:123], v[36:39]\n"
    "buffer_load_dwordx4 v[168:171], v179, s[4:7], s55 offen sc1\n"
    "v_mfma_f32_16x16x32_bf16 v[40:43], a[64:67], v[120:123], v[40:43]\n"
    "v_mfma_f32_16x16x32_bf16 v[44:47], a[68:71], v[120:123], v[44:47]\n"
    "v_mfma_f32_16x16x32_bf16 v[48:51], a[72:75], v[120:123], v[48:51]\n"
    "v_mfma_f32_16x16x32_bf16 v[52:55], a[76:79], v[120:123], v[52:55]\n"
    "ds_write_b128 v180, v[32:35] offset:4096\n"
    "v_mfma_f32_16x16x32_bf16 v[36:39], a[80:83], v[124:127], v[36:39]\n"
    "buffer_load_dwordx4 v[172:175], v179, s[4:7], s56 offen sc1\n"
    "v_mfma_f32_16x16x32_bf16 v[40:43], a[84:87], v[124:127], v[40:43]\n"
    "v_mfma_f32_16x16x32_bf16 v[44:47], a[88:91], v[124:127], v[44:47]\n"
    "s_waitcnt vmcnt(8)\n"
    "v_max3_u32 v7, v140, v132, v136\n"
    "v_mfma_f32_16x16x32_bf16 v[48:51], a[92:95], v[124:127], v[48:51]\n"
    "v_max_u32 v7, v7, v128\n"
    "v_mfma_f32_16x16x32_bf16 v[52:55], v[232:235], v[124:127], v[52:55]\n"
    "v_cmp_eq_u32 vcc, -1, v7\n"
    "s_cbranch_vccnz f7s_59_%=\n"
    "f7r_60_%=:\n"
    "v_mfma_f32_16x16x32_bf16 v[56:59], a[96:99], v[128:131], v[56:59]\n"
    "buffer_load_dwordx4 v[96:99], v179, s[4:7], s58 offen sc1\n"
    "v_mfma_f32_16x16x32_bf16 v[60:63], a[100:103], v[128:131], v[60:63]\n"
    "v_mfma_f32_16x16x32_bf16 v[64:67], a[104:107], v[128:131], v[64:67]\n"
    "v_mfma_f32_16x16x32_bf16 v[68:71], a[108:111], v[128:131], v[68:71]\n"
    "ds_write_b128 v181, v[36:39] offset:0\n"
    "v_mfma_f32_16x16x32_bf16 v[72:75], a[112:115], v[128:131], v[72:75]\n"
    "v_mfma_f32_16x16x32_bf16 v[56:59], a[116:119], v[132:135], v[56:59]\n"
    "buffer_load_dwordx4 v[100:103], v179, s[4:7], s59 offen sc1\n"
    "v_mfma_f32_16x16x32_bf16 v[60:63], a[120:123], v[132:135], v[60:63]\n"
    "v_mfma_f32_16x16x32_bf16 v[64:67], a[124:127], v[132:135], v[64:67]\n"
    "ds_write_b128 v181, v[40:43] offset:1024\n"
    "v_mfma_f32_16x16x32_bf16 v[68:71], a[128:131], v[132:135], v[68:71]\n"
    "v_mfma_f32_16x16x32_bf16 v[72:75], a[132:135], v[132:135], v[72:75]\n"
    "v_mfma_f32_16x16x32_bf16 v[56:59], a[156:159], v[136:139], v[56:59]\n"
    "buffer_load_dwordx4 v[104:107], v179, s[4:7], s61 offen sc1\n"
    "v_mfma_f32_16x16x32_bf16 v[60:63], a[160:163], v[136:139], v[60:63]\n"
    "ds_write_b128 v181, v[44:47] offset:2048\n"
    "v_mfma_f32_16x16x32_bf16 v[64:67], a[164:167], v[136:139], v[64:67]\n"
    "v_mfma_f32_16x16x32_bf16 v[68:71], a[168:171], v[136:139], v[68:71]\n"
    "v_mfma_f32_16x16x32_bf16 v[72:75], a[172:175], v[136:139], v[72:75]\n"
    "v_mfma_f32_16x16x32_bf16 v[56:59], a[176:179], v[140:143], v[56:59]\n"
    "buffer_load_dwordx4 v[108:111], v179, s[4:7], s62 offen sc1\n"
    "ds_write_b128 v181, v[48:51] offset:3072\n"
    "v_mfma_f32_16x16x32_bf16 v[60:63], a[180:183], v[140:143], v[60:63]\n"
    "v_mfma_f32_16x16x32_bf16 v[64:67], a[184:187], v[140:143], v[64:67]\n"
    "s_waitcnt vmcnt(8)\n"
    "v_max3_u32 v7, v156, v148, v152\n"
    "v_mfma_f32_16x16x32_bf16 v[68:71], a[188:191], v[140:143], v[68:71]\n"
    "v_max_u32 v7, v7, v144\n"
    "v_mfma_f32_16x16x32_bf16 v[72:75], a[192:195], v[140:143], v[72:75]\n"
    "v_cmp_eq_u32 vcc, -1, v7\n"
    "s_cbranch_vccnz f8s_61_%=\n"
    "f8r_62_%=:\n"
    "ds_write_b128 v181, v[52:55] offset:4096\n"
    "v_mfma_f32_16x16x32_bf16 v[56:59], a[216:219], v[144:147], v[56:59]\n"
    "buffer_load_dwordx2 v[112:113], v14, s[4:7], s25 offen offset:1024 sc1\n"
    "v_mfma_f32_16x16x32_bf16 v[60:63], a[220:223], v[144:147], v[60:63]\n"
    "v_mfma_f32_16x16x32_bf16 v[64:67], a[224:227], v[144:147], v[64:67]\n"
    "v_mfma_f32_16x16x32_bf16 v[68:71], a[228:231], v[144:147], v[68:71]\n"
    "v_mfma_f32_16x16x32_bf16 v[72:75], a[232:235], v[144:147], v[72:75]\n"
    "v_mfma_f32_16x16x32_bf16 v[56:59], a[236:239], v[148:151], v[56:59]\n"
    "buffer_load_dwordx4 v[116:119], v178, s[4:7], s54 offen sc1\n"
    "v_mfma_f32_16x16x32_bf16 v[60:63], a[240:243], v[148:151], v[60:63]\n"
    "v_mfma_f32_16x16x32_bf16 v[64:67], a[244:247], v[148:151], v[64:67]\n"
    "v_mfma_f32_16x16x32_bf16 v[68:71], a[248:251], v[148:151], v[68:71]\n"
    "s_waitcnt lgkmcnt(0)\n"
    "s_barrier\n"
    "v_mfma_f32_16x16x32_bf16 v[72:75], a[252:255], v[148:151], v[72:75]\n"
    "v_mfma_f32_16x16x32_bf16 v[56:59], a[20:23], v[152:155], v[56:59]\n"
    "buffer_load_dwordx4 v[120:123], v178, s[4:7], s57 offen sc1\n"
    "ds_read_b128 v[184:187], v182 offset:0\n"
    "v_mfma_f32_16x16x32_bf16 v[60:63], a[24:27], v[152:155], v[60:63]\n"
    "v_mfma_f32_16x16x32_bf16 v[64:67], a[28:31], v[152:155], v[64:67]\n"
    "ds_read_b128 v[188:191], v182 offset:1024\n"
    "v_mfma_f32_16x16x32_bf16 v[68:71], a[32:35], v[152:155], v[68:71]\n"
    "v_mfma_f32_16x16x32_bf16 v[72:75], a[36:39], v[152:155], v[72:75]\n"
    "ds_read_b128 v[192:195], v182 offset:5120\n"
    "v_mfma_f32_16x16x32_bf16 v[56:59], a[40:43], v[156:159], v[56:59]\n"
    "buffer_load_dwordx4 v[124:127], v178, s[4:7], s60 offen sc1\n"
    "v_mfma_f32_16x16x32_bf16 v[60:63], a[44:47], v[156:159], v[60:63]\n"
    "ds_read_b128 v[196:199], v182 offset:6144\n"
    "v_mfma_f32_16x16x32_bf16 v[64:67], a[48:51], v[156:159], v[64:67]\n"
    "s_waitcnt vmcnt(8)\n"
    "v_max3_u32 v7, v172, v164, v168\n"
    "v_mfma_f32_16x16x32_bf16 v[68:71], a[52:55], v[156:159], v[68:71]\n"
    "v_max_u32 v7, v7, v160\n"
    "ds_read_b128 v[200:203], v182 offset:10240\n"
    "v_mfma_f32_16x16x32_bf16 v[72:75], a[56:59], v[156:159], v[72:75]\n"
    "v_cmp_eq_u32 vcc, -1, v7\n"
    "s_cbranch_vccnz f9s_63_%=\n"
    "f9r_64_%=:\n"
    "v_mfma_f32_16x16x32_bf16 v[76:79], a[96:99], v[160:163], 0\n"
    "buffer_load_dwordx4 v[128:131], v178, s[4:7], s63 offen sc1\n"
    "ds_read_b128 v[204:207], v182 offset:11264\n"
    "v_mfma_f32_16x16x32_bf16 v[80:83], a[100:103], v[160:163], 0\n"
    "v_mfma_f32_16x16x32_bf16 v[84:87], a[104:107], v[160:163], 0\n"
    "ds_read_b128 v[208:211], v182 offset:15360\n"
    "v_mfma_f32_16x16x32_bf16 v[88:91], a[108:111], v[160:163], 0\n"
    "v_mfma_f32_16x16x32_bf16 v[92:95], a[112:115], v[160:163], 0\n"
    "ds_read_b128 v[212:215], v182 offset:16384\n"
    "v_mfma_f32_16x16x32_bf16 v[76:79], a[116:119], v[164:167], v[76:79]\n"
    "buffer_load_dwordx4 v[132:135], v178, s[4:7], s64 offen sc1\n"
    "v_mfma_f32_16x16x32_bf16 v[80:83], a[120:123], v[164:167], v[80:83]\n"
    "ds_read_b64 v[216:217], v183 offset:4096\n"
    "v_mfma_f32_16x16x32_bf16 v[84:87], a[124:127], v[164:167], v[84:87]\n"
    "v_mfma_f32_16x16x32_bf16 v[88:91], a[128:131], v[164:167], v[88:91]\n"
    "ds_read_b64 v[218:219], v183 offset:9216\n"
    "v_mfma_f32_16x16x32_bf16 v[92:95], a[132:135], v[164:167], v[92:95]\n"
    "v_mfma_f32_16x16x32_bf16 v[76:79], a[156:159], v[168:171], v[76:79]\n"
    "buffer_load_dwordx2 v[136:137], v14, s[4:7], s26 offen offset:1024 sc1\n"
    "ds_read_b64 v[220:221], v183 offset:14336\n"
    "v_mfma_f32_16x16x32_bf16 v[80:83], a[160:163], v[168:171], v[80:83]\n"
    "v_mfma_f32_16x16x32_bf16 v[84:87], a[164:167], v[168:171], v[84:87]\n"
    "ds_read_b64 v[222:223], v183 offset:19456\n"
    "v_mfma_f32_16x16x32_bf16 v[88:91], a[168:171], v[168:171], v[88:91]\n"
    "v_mfma_f32_16x16x32_bf16 v[92:95], a[172:175], v[168:171], v[92:95]\n"
    "v_mfma_f32_16x16x32_bf16 v[76:79], a[176:179], v[172:175], v[76:79]\n"
    "buffer_load_dwordx4 v[140:143], v179, s[4:7], s54 offen sc1\n"
    "v_mfma_f32_16x16x32_bf16 v[80:83], a[180:183], v[172:175], v[80:83]\n"
    "v_mfma_f32_16x16x32_bf16 v[84:87], a[184:187], v[172:175], v[84:87]\n"
    "s_waitcnt vmcnt(8)\n"
    "v_max3_u32 v7, v108, v100, v104\n"
    "v_mfma_f32_16x16x32_bf16 v[88:91], a[188:191], v[172:175], v[88:91]\n"
    "v_max_u32 v7, v7, v96\n"
    "s_waitcnt lgkmcnt(6)\n"
    "v_add_f32 v184, v184, v192\n"
    "v_mfma_f32_16x16x32_bf16 v[92:95], a[192:195], v[172:175], v[92:95]\n"
    "v_cmp_eq_u32 vcc, -1, v7\n"
    "s_cbranch_vccnz f10s_65_%=\n"
    "f10r_66_%=:\n"
    "v_mfma_f32_16x16x32_bf16 v[76:79], a[216:219], v[96:99], v[76:79]\n"
    "buffer_load_dwordx4 v[144:147], v179, s[4:7], s57 offen sc1\n"
    "v_add_f32 v185, v185, v193\n"
    "v_mfma_f32_16x16x32_bf16 v[80:83], a[220:223], v[96:99], v[80:83]\n"
    "v_add_f32 v186, v186, v194\n"
    "v_mfma_f32_16x16x32_bf16 v[84:87], a[224:227], v[96:99], v[84:87]\n"
    "v_add_f32 v187, v187, v195\n"
    "v_mfma_f32_16x16x32_bf16 v[88:91], a[228:231], v[96:99], v[88:91]\n"
    "v_add_f32 v188, v188, v196\n"
    "v_mfma_f32_16x16x32_bf16 v[92:95], a[232:235], v[96:99], v[92:95]\n"
    "v_add_f32 v189, v189, v197\n"
    "v_mfma_f32_16x16x32_bf16 v[76:79], a[236:239], v[100:103], v[76:79]\n"
    "buffer_load_dwordx4 v[148:151], v179, s[4:7], s60 offen sc1\n"
    "v_mfma_f32_16x16x32_bf16 v[80:83], a[240:243], v[100:103], v[80:83]\n"
    "v_add_f32 v190, v190, v198\n"
    "v_mfma_f32_16x16x32_bf16 v[84:87], a[244:247], v[100:103], v[84:87]\n"
    "v_add_f32 v191, v191, v199\n"
    "v_mfma_f32_16x16x32_bf16 v[88:91], a[248:251], v[100:103], v[88:91]\n"
    "v_add_f32 v184, v184, v200\n"
    "v_mfma_f32_16x16x32_bf16 v[92:95], a[252:255], v[100:103], v[92:95]\n"
    "v_mfma_f32_16x16x32_bf16 v[76:79], a[20:23], v[104:107], v[76:79]\n"
    "buffer_load_dwordx4 v[152:155], v179, s[4:7], s63 offen sc1\n"
    "v_add_f32 v185, v185, v201\n"
    "v_mfma_f32_16x16x32_bf16 v[80:83], a[24:27], v[104:107], v[80:83]\n"
    "v_add_f32 v186, v186, v202\n"
    "v_mfma_f32_16x16x32_bf16 v[84:87], a[28:31], v[104:107], v[84:87]\n"
    "v_mfma_f32_16x16x32_bf16 v[88:91], a[32:35], v[104:107], v[88:91]\n"
    "v_add_f32 v187, v187, v203\n"
    "v_mfma_f32_16x16x32_bf16 v[92:95], a[36:39], v[104:107], v[92:95]\n"
    "v_add_f32 v188, v188, v204\n"
    "v_mfma_f32_16x16x32_bf16 v[76:79], a[40:43], v[108:111], v[76:79]\n"
    "buffer_load_dwordx4 v[156:159], v179, s[4:7], s64 offen sc1\n"
    "v_add_f32 v189, v189, v205\n"
    "v_mfma_f32_16x16x32_bf16 v[80:83], a[44:47], v[108:111], v[80:83]\n"
    "v_mfma_f32_16x16x32_bf16 v[84:87], a[48:51], v[108:111], v[84:87]\n"
    "s_waitcnt vmcnt(8)\n"
    "v_max3_u32 v7, v124, v116, v120\n"
    "v_add_f32 v190, v190, v206\n"
    "v_mfma_f32_16x16x32_bf16 v[88:91], a[52:55], v[108:111], v[88:91]\n"
    "v_max_u32 v7, v7, v112\n"
    "v_add_f32 v191, v191, v207\n"
    "v_mfma_f32_16x16x32_bf16 v[92:95], a[56:59], v[108:111], v[92:95]\n"
    "v_cmp_eq_u32 vcc, -1, v7\n"
    "s_cbranch_vccnz f11s_67_%=\n"
    "f11r_68_%=:\n"
    "v_mfma_f32_16x16x16_bf16 v[56:59], v[236:237], v[112:113], v[56:59]\n"
    "s_waitcnt lgkmcnt(3)\n"
    "v_add_f32 v184, v184, v208\n"
    "v_mfma_f32_16x16x16_bf16 v[60:63], v[238:239], v[112:113], v[60:63]\n"
    "v_add_f32 v185, v185, v209\n"
    "v_mfma_f32_16x16x16_bf16 v[64:67], v[240:241], v[112:113], v[64:67]\n"
    "v_add_f32 v186, v186, v210\n"
    "v_mfma_f32_16x16x16_bf16 v[68:71], v[242:243], v[112:113], v[68:71]\n"
    "v_mfma_f32_16x16x16_bf16 v[72:75], v[244:245], v[112:113], v[72:75]\n"
    "v_add_f32 v187, v187, v211\n"
    "v_mfma_f32_16x16x32_bf16 v[56:59], a[136:139], v[116:119], v[56:59]\n"
    "v_add_f32 v188, v188, v212\n"
    "v_mfma_f32_16x16x32_bf16 v[60:63], a[140:143], v[116:119], v[60:63]\n"
    "v_add_f32 v189, v189, v213\n"
    "v_mfma_f32_16x16x32_bf16 v[64:67], a[144:147], v[116:119], v[64:67]\n"
    "v_mfma_f32_16x16x32_bf16 v[68:71], a[148:151], v[116:119], v[68:71]\n"
    "v_add_f32 v190, v190, v214\n"
    "v_mfma_f32_16x16x32_bf16 v[72:75], a[152:155], v[116:119], v[72:75]\n"
    "v_add_f32 v191, v191, v215\n"
    "v_mfma_f32_16x16x32_bf16 v[56:59], a[196:199], v[120:123], v[56:59]\n"
    "v_cvt_pk_bf16_f32 v224, v184, v185\n"
    "v_mfma_f32_16x16x32_bf16 v[60:63], a[200:203], v[120:123], v[60:63]\n"
    "v_mfma_f32_16x16x32_bf16 v[64:67], a[204:207], v[120:123], v[64:67]\n"
    "v_cvt_pk_bf16_f32 v225, v186, v187\n"
    "v_mfma_f32_16x16x32_bf16 v[68:71], a[208:211], v[120:123], v[68:71]\n"
    "v_cvt_pk_bf16_f32 v226, v188, v189\n"
    "v_mfma_f32_16x16x32_bf16 v[72:75], a[212:215], v[120:123], v[72:75]\n"
    "v_cvt_pk_bf16_f32 v227, v190, v191\n"
    "v_mfma_f32_16x16x32_bf16 v[56:59], a[0:3], v[124:127], v[56:59]\n"
    "v_mfma_f32_16x16x32_bf16 v[60:63], a[4:7], v[124:127], v[60:63]\n"
    "buffer_store_dwordx4 v[224:227], v10, s[12:15], s27 offen\n"
    "v_mfma_f32_16x16x32_bf16 v[64:67], a[8:11], v[124:127], v[64:67]\n"
    "s_waitcnt vmcnt(5)\n"
    "v_max3_u32 v7, v140, v132, v136\n"
    "s_waitcnt lgkmcnt(2)\n"
    "v_add_f32 v216, v216, v218\n"
    "v_mfma_f32_16x16x32_bf16 v[68:71], a[12:15], v[124:127], v[68:71]\n"
    "v_max_u32 v7, v7, v128\n"
    "v_mfma_f32_16x16x32_bf16 v[72:75], a[16:19], v[124:127], v[72:75]\n"
    "v_cmp_eq_u32 vcc, -1, v7\n"
    "s_cbranch_vccnz f12s_69_%=\n"
    "f12r_70_%=:\n"
    "v_mfma_f32_16x16x32_bf16 v[56:59], a[60:63], v[128:131], v[56:59]\n"
    "v_add_f32 v217, v217, v219\n"
    "s_waitcnt lgkmcnt(1)\n"
    "v_add_f32 v216, v216, v220\n"
    "v_mfma_f32_16x16x32_bf16 v[60:63], a[64:67], v[128:131], v[60:63]\n"
    "v_add_f32 v217, v217, v221\n"
    "v_mfma_f32_16x16x32_bf16 v[64:67], a[68:71], v[128:131], v[64:67]\n"
    "v_mfma_f32_16x16x32_bf16 v[68:71], a[72:75], v[128:131], v[68:71]\n"
    "s_waitcnt lgkmcnt(0)\n"
    "v_add_f32 v216, v216, v222\n"
    "v_mfma_f32_16x16x32_bf16 v[72:75], a[76:79], v[128:131], v[72:75]\n"
    "v_add_f32 v217, v217, v223\n"
    "v_mfma_f32_16x16x32_bf16 v[56:59], a[80:83], v[132:135], v[56:59]\n"
    "v_cvt_pk_bf16_f32 v228, v216, v217\n"
    "v_mfma_f32_16x16x32_bf16 v[60:63], a[84:87], v[132:135], v[60:63]\n"
    "v_mfma_f32_16x16x32_bf16 v[64:67], a[88:91], v[132:135], v[64:67]\n"
    "buffer_store_dword v228, v11, s[12:15], s28 offen offset:128\n"
    "v_mfma_f32_16x16x32_bf16 v[68:71], a[92:95], v[132:135], v[68:71]\n"
    "s_add_u32 s27, s27, 20480\n"
    "s_add_u32 s28, s28, 20480\n"
    "v_mfma_f32_16x16x32_bf16 v[72:75], v[232:235], v[132:135], v[72:75]\n"
    "v_mfma_f32_16x16x16_bf16 v[76:79], v[236:237], v[136:137], v[76:79]\n"
    "v_mfma_f32_16x16x16_bf16 v[80:83], v[238:239], v[136:137], v[80:83]\n"
    "v_mfma_f32_16x16x16_bf16 v[84:87], v[240:241], v[136:137], v[84:87]\n"
    "ds_write_b128 v180, v[56:59] offset:40960\n"
    "v_mfma_f32_16x16x16_bf16 v[88:91], v[242:243], v[136:137], v[88:91]\n"
    "v_mfma_f32_16x16x16_bf16 v[92:95], v[244:245], v[136:137], v[92:95]\n"
    "s_nop 0\n"
    "v_mfma_f32_16x16x32_bf16 v[76:79], a[136:139], v[140:143], v[76:79]\n"
    "ds_write_b128 v180, v[60:63] offset:41984\n"
    "v_mfma_f32_16x16x32_bf16 v[80:83], a[140:143], v[140:143], v[80:83]\n"
    "v_mfma_f32_16x16x32_bf16 v[84:87], a[144:147], v[140:143], v[84:87]\n"
    "s_waitcnt vmcnt(2)\n"
    "v_max3_u32 v7, v156, v148, v152\n"
    "v_mfma_f32_16x16x32_bf16 v[88:91], a[148:151], v[140:143], v[88:91]\n"
    "v_max_u32 v7, v7, v144\n"
    "ds_write_b128 v180, v[64:67] offset:43008\n"
    "v_mfma_f32_16x16x32_bf16 v[92:95], a[152:155], v[140:143], v[92:95]\n"
    "v_cmp_eq_u32 vcc, -1, v7\n"
    "s_cbranch_vccnz f13s_71_%=\n"
    "f13r_72_%=:\n"
    "v_mfma_f32_16x16x32_bf16 v[76:79], a[196:199], v[144:147], v[76:79]\n"
    "v_mfma_f32_16x16x32_bf16 v[80:83], a[200:203], v[144:147], v[80:83]\n"
    "v_mfma_f32_16x16x32_bf16 v[84:87], a[204:207], v[144:147], v[84:87]\n"
    "ds_write_b128 v180, v[68:71] offset:44032\n"
    "v_mfma_f32_16x16x32_bf16 v[88:91], a[208:211], v[144:147], v[88:91]\n"
    "v_mfma_f32_16x16x32_bf16 v[92:95], a[212:215], v[144:147], v[92:95]\n"
    "v_mfma_f32_16x16x32_bf16 v[76:79], a[0:3], v[148:151], v[76:79]\n"
    "ds_write_b128 v180, v[72:75] offset:45056\n"
    "v_mfma_f32_16x16x32_bf16 v[76:79], a[60:63], v[152:155], v[76:79]\n"
    "v_mfma_f32_16x16x32_bf16 v[76:79], a[80:83], v[156:159], v[76:79]\n"
    "v_mfma_f32_16x16x32_bf16 v[80:83], a[4:7], v[148:151], v[80:83]\n"
    "v_mfma_f32_16x16x32_bf16 v[80:83], a[64:67], v[152:155], v[80:83]\n"
    "v_mfma_f32_16x16x32_bf16 v[80:83], a[84:87], v[156:159], v[80:83]\n"
    "s_nop 4\n"
    "ds_write_b128 v181, v[76:79] offset:40960\n"
    "v_mfma_f32_16x16x32_bf16 v[84:87], a[8:11], v[148:151], v[84:87]\n"
    "v_mfma_f32_16x16x32_bf16 v[84:87], a[68:71], v[152:155], v[84:87]\n"
    "v_mfma_f32_16x16x32_bf16 v[84:87], a[88:91], v[156:159], v[84:87]\n"
    "ds_write_b128 v181, v[80:83] offset:41984\n"
    "v_mfma_f32_16x16x32_bf16 v[88:91], a[12:15], v[148:151], v[88:91]\n"
    "v_mfma_f32_16x16x32_bf16 v[88:91], a[72:75], v[152:155], v[88:91]\n"
    "v_mfma_f32_16x16x32_bf16 v[88:91], a[92:95], v[156:159], v[88:91]\n"
    "s_nop 3\n"
    "ds_write_b128 v181, v[84:87] offset:43008\n"
    "v_mfma_f32_16x16x32_bf16 v[92:95], a[16:19], v[148:151], v[92:95]\n"
    "v_mfma_f32_16x16x32_bf16 v[92:95], a[76:79], v[152:155], v[92:95]\n"
    "v_mfma_f32_16x16x32_bf16 v[92:95], v[232:235], v[156:159], v[92:95]\n"
    "ds_write_b128 v181, v[88:91] offset:44032\n"
    "s_nop 6\n"
    "ds_write_b128 v181, v[92:95] offset:45056\n"
    "s_waitcnt lgkmcnt(0)\n"
    "s_barrier\n"
    "ds_read_b128 v[184:187], v182 offset:40960\n"
    "ds_read_b128 v[188:191], v182 offset:41984\n"
    "ds_read_b128 v[192:195], v182 offset:46080\n"
    "ds_read_b128 v[196:199], v182 offset:47104\n"
    "ds_read_b128 v[200:203], v182 offset:51200\n"
    "ds_read_b128 v[204:207], v182 offset:52224\n"
    "ds_read_b128 v[208:211], v182 offset:56320\n"
    "ds_read_b128 v[212:215], v182 offset:57344\n"
    "ds_read_b64 v[216:217], v183 offset:45056\n"
    "ds_read_b64 v[218:219], v183 offset:50176\n"
    "ds_read_b64 v[220:221], v183 offset:55296\n"
    "ds_read_b64 v[222:223], v183 offset:60416\n"
    "s_waitcnt lgkmcnt(6)\n"
    "v_pk_add_f32 v[184:185], v[184:185], v[192:193]\n"
    "v_pk_add_f32 v[186:187], v[186:187], v[194:195]\n"
    "v_pk_add_f32 v[188:189], v[188:189], v[196:197]\n"
    "v_pk_add_f32 v[190:191], v[190:191], v[198:199]\n"
    "v_pk_add_f32 v[184:185], v[184:185], v[200:201]\n"
    "v_pk_add_f32 v[186:187], v[186:187], v[202:203]\n"
    "v_pk_add_f32 v[188:189], v[188:189], v[204:205]\n"
    "v_pk_add_f32 v[190:191], v[190:191], v[206:207]\n"
    "s_waitcnt lgkmcnt(3)\n"
    "v_pk_add_f32 v[184:185], v[184:185], v[208:209]\n"
    "v_pk_add_f32 v[186:187], v[186:187], v[210:211]\n"
    "v_pk_add_f32 v[188:189], v[188:189], v[212:213]\n"
    "v_pk_add_f32 v[190:191], v[190:191], v[214:215]\n"
    "v_cvt_pk_bf16_f32 v224, v184, v185\n"
    "v_cvt_pk_bf16_f32 v225, v186, v187\n"
    "v_cvt_pk_bf16_f32 v226, v188, v189\n"
    "v_cvt_pk_bf16_f32 v227, v190, v191\n"
    "buffer_store_dwordx4 v[224:227], v10, s[12:15], s27 offen\n"
    "s_waitcnt lgkmcnt(2)\n"
    "v_pk_add_f32 v[216:217], v[216:217], v[218:219]\n"
    "s_waitcnt lgkmcnt(1)\n"
    "v_pk_add_f32 v[216:217], v[216:217], v[220:221]\n"
    "s_waitcnt lgkmcnt(0)\n"
    "v_pk_add_f32 v[216:217], v[216:217], v[222:223]\n"
    "v_cvt_pk_bf16_f32 v228, v216, v217\n"
    "buffer_store_dword v228, v11, s[12:15], s28 offen offset:128\n"
    "s_endpgm\n"
    "g2s_3_%=:\n"
    "s_waitcnt vmcnt(0) lgkmcnt(0)\n"
    "s_nop 7\n"
    "s_cmp_eq_u32 s30, 0\n"
    "s_cbranch_scc1 g2r_4_%=\n"
    "s_sub_u32 s30, s30, 1\n"
    "s_sleep 2\n"
    "buffer_load_dwordx4 v[184:187], v98, s[4:7], 0 offen sc1\n"
    "buffer_load_dwordx4 v[188:191], v98, s[4:7], 0 offen offset:2048 sc1\n"
    "buffer_load_dwordx4 v[192:195], v98, s[4:7], s45 offen sc1\n"
    "s_waitcnt vmcnt(0)\n"
    "v_xor_b32 v196, s29, v185\n"
    "v_xor_b32 v197, s29, v187\n"
    "v_xor_b32 v198, s29, v189\n"
    "v_xor_b32 v199, s29, v191\n"
    "v_or3_b32 v196, v196, v197, v198\n"
    "v_xor_b32 v197, s20, v193\n"
    "v_xor_b32 v198, s20, v195\n"
    "v_or3_b32 v196, v196, v197, v198\n"
    "v_or_b32 v196, v196, v199\n"
    "v_cmp_ne_u32 vcc, 0, v196\n"
    "s_cmp_lg_u64 vcc, 0\n"
    "s_cbranch_scc1 g2s_3_%=\n"
    "s_nop 3\n"
    "s_branch g2r_4_%=\n"
    "pfws_5_%=:\n"
    "s_waitcnt vmcnt(0) lgkmcnt(0)\n"
    "s_nop 7\n"
    "s_cmp_eq_u32 s30, 0\n"
    "s_cbranch_scc1 pfwr_6_%=\n"
    "s_sub_u32 s30, s30, 1\n"
    "buffer_load_dword v7, v168, s[4:7], 0 offen sc1\n"
    "s_waitcnt vmcnt(0)\n"
    "v_cmp_ne_u32 vcc, s29, v7\n"
    "s_cmp_lg_u64 vcc, 0\n"
    "s_cbranch_scc1 pfws_5_%=\n"
    "s_nop 3\n"
    "s_branch pfwr_6_%=\n"
    "pf2s_7_%=:\n"
    "s_waitcnt vmcnt(0) lgkmcnt(0)\n"
    "s_nop 7\n"
    "s_cmp_eq_u32 s30, 0\n"
    "s_cbranch_scc1 pf2r_8_%=\n"
    "s_sub_u32 s30, s30, 1\n"
    "s_mov_b32 s32, 32768\n"
    "buffer_load_dwordx4 v[220:223], v0, s[4:7], s32 offen sc1\n"
    "s_waitcnt vmcnt(0)\n"
    "v_xor_b32 v220, s29, v220\n"
    "v_xor_b32 v221, s29, v221\n"
    "v_xor_b32 v222, s29, v222\n"
    "v_xor_b32 v223, s29, v223\n"
    "v_or3_b32 v220, v220, v221, v222\n"
    "v_or_b32 v220, v220, v223\n"
    "v_cmp_ne_u32 vcc, 0, v220\n"
    "s_cmp_lg_u64 vcc, 0\n"
    "s_cbranch_scc1 pf2s_7_%=\n"
    "s_nop 3\n"
    "s_branch pf2r_8_%=\n"
    "slabs_9_%=:\n"
    "s_waitcnt vmcnt(0) lgkmcnt(0)\n"
    "s_nop 7\n"
    "s_cmp_eq_u32 s30, 0\n"
    "s_cbranch_scc1 slabr_10_%=\n"
    "s_sub_u32 s30, s30, 1\n"
    "buffer_load_dwordx2 v[16:17], v81, s[4:7], s73 offen offset:0 sc1\n"
    "buffer_load_dwordx2 v[18:19], v81, s[4:7], s73 offen offset:2048 sc1\n"
    "s_add_u32 s33, s73, 4096\n"
    "buffer_load_dwordx2 v[20:21], v81, s[4:7], s33 offen offset:0 sc1\n"
    "buffer_load_dwordx2 v[22:23], v81, s[4:7], s33 offen offset:2048 sc1\n"
    "s_add_u32 s33, s73, 8192\n"
    "buffer_load_dwordx2 v[24:25], v81, s[4:7], s33 offen offset:0 sc1\n"
    "buffer_load_dwordx2 v[26:27], v81, s[4:7], s33 offen offset:2048 sc1\n"
    "s_add_u32 s33, s73, 12288\n"
    "buffer_load_dwordx2 v[28:29], v81, s[4:7], s33 offen offset:0 sc1\n"
    "buffer_load_dwordx2 v[30:31], v81, s[4:7], s33 offen offset:2048 sc1\n"
    "s_add_u32 s33, s73, 16384\n"
    "buffer_load_dwordx2 v[32:33], v81, s[4:7], s33 offen offset:0 sc1\n"
    "buffer_load_dwordx2 v[34:35], v81, s[4:7], s33 offen offset:2048 sc1\n"
    "s_add_u32 s33, s73, 20480\n"
    "buffer_load_dwordx2 v[36:37], v81, s[4:7], s33 offen offset:0 sc1\n"
    "buffer_load_dwordx2 v[38:39], v81, s[4:7], s33 offen offset:2048 sc1\n"
    "s_add_u32 s33, s73, 24576\n"
    "buffer_load_dwordx2 v[40:41], v81, s[4:7], s33 offen offset:0 sc1\n"
    "buffer_load_dwordx2 v[42:43], v81, s[4:7], s33 offen offset:2048 sc1\n"
    "s_add_u32 s33, s73, 28672\n"
    "buffer_load_dwordx2 v[44:45], v81, s[4:7], s33 offen offset:0 sc1\n"
    "buffer_load_dwordx2 v[46:47], v81, s[4:7], s33 offen offset:2048 sc1\n"
    "s_waitcnt vmcnt(0)\n"
    "v_mfma_f32_16x16x32_bf16 v[48:51], v[108:111], v[16:19], 0\n"
    "v_mfma_f32_16x16x32_bf16 v[48:51], v[108:111], v[20:23], v[48:51]\n"
    "v_mfma_f32_16x16x32_bf16 v[48:51], v[108:111], v[24:27], v[48:51]\n"
    "v_mfma_f32_16x16x32_bf16 v[48:51], v[108:111], v[28:31], v[48:51]\n"
    "v_mfma_f32_16x16x32_bf16 v[48:51], v[108:111], v[32:35], v[48:51]\n"
    "v_mfma_f32_16x16x32_bf16 v[48:51], v[108:111], v[36:39], v[48:51]\n"
    "v_mfma_f32_16x16x32_bf16 v[48:51], v[108:111], v[40:43], v[48:51]\n"
    "v_mfma_f32_16x16x32_bf16 v[48:51], v[108:111], v[44:47], v[48:51]\n"
    "s_nop 7\n"
    "v_cmp_u_f32 vcc, v48, v48\n"
    "s_cmp_lg_u64 vcc, 0\n"
    "s_cbranch_scc1 slabs_9_%=\n"
    "s_nop 3\n"
    "s_branch slabr_10_%=\n"
    "o0s_11_%=:\n"
    "s_waitcnt vmcnt(0) lgkmcnt(0)\n"
    "s_nop 7\n"
    "s_cmp_eq_u32 s30, 0\n"
    "s_cbranch_scc1 o0r_12_%=\n"
    "s_sub_u32 s30, s30, 1\n"
    "buffer_load_dwordx4 v[96:99], v0, s[4:7], s84 offen offset:0 sc1\n"
    "buffer_load_dwordx4 v[100:103], v0, s[4:7], s84 offen offset:1024 sc1\n"
    "buffer_load_dwordx4 v[104:107], v0, s[4:7], s84 offen offset:2048 sc1\n"
    "buffer_load_dwordx4 v[108:111], v0, s[4:7], s84 offen offset:3072 sc1\n"
    "buffer_load_dwordx4 v[112:115], v0, s[4:7], s85 offen offset:0 sc1\n"
    "buffer_load_dwordx4 v[116:119], v0, s[4:7], s85 offen offset:1024 sc1\n"
    "buffer_load_dwordx4 v[120:123], v0, s[4:7], s85 offen offset:2048 sc1\n"
    "buffer_load_dwordx4 v[124:127], v0, s[4:7], s85 offen offset:3072 sc1\n"
    "buffer_load_dwordx4 v[128:131], v0, s[4:7], s86 offen offset:0 sc1\n"
    "buffer_load_dwordx4 v[132:135], v0, s[4:7], s86 offen offset:1024 sc1\n"
    "buffer_load_dwordx4 v[136:139], v0, s[4:7], s86 offen offset:2048 sc1\n"
    "buffer_load_dwordx4 v[140:143], v0, s[4:7], s86 offen offset:3072 sc1\n"
    "buffer_load_dwordx4 v[144:147], v0, s[4:7], s87 offen offset:0 sc1\n"
    "buffer_load_dwordx4 v[148:151], v0, s[4:7], s87 offen offset:1024 sc1\n"
    "buffer_load_dwordx4 v[152:155], v0, s[4:7], s87 offen offset:2048 sc1\n"
    "buffer_load_dwordx4 v[156:159], v0, s[4:7], s87 offen offset:3072 sc1\n"
    "buffer_load_dwordx4 v[160:163], v0, s[4:7], s88 offen offset:0 sc1\n"
    "buffer_load_dwordx4 v[164:167], v0, s[4:7], s88 offen offset:1024 sc1\n"
    "buffer_load_dwordx4 v[168:171], v0, s[4:7], s88 offen offset:2048 sc1\n"
    "buffer_load_dwordx4 v[172:175], v0, s[4:7], s88 offen offset:3072 sc1\n"
    "s_waitcnt vmcnt(0)\n"
    "v_max3_u32 v7, v108, v100, v104\n"
    "v_max_u32 v7, v7, v96\n"
    "v_cmp_eq_u32 vcc, -1, v7\n"
    "s_cmp_lg_u64 vcc, 0\n"
    "s_cbranch_scc1 o0s_11_%=\n"
    "s_nop 3\n"
    "s_branch o0r_12_%=\n"
    "o1s_13_%=:\n"
    "s_waitcnt vmcnt(0) lgkmcnt(0)\n"
    "s_nop 7\n"
    "s_cmp_eq_u32 s30, 0\n"
    "s_cbranch_scc1 o1r_14_%=\n"
    "s_sub_u32 s30, s30, 1\n"
    "buffer_load_dwordx4 v[112:115], v0, s[4:7], s85 offen offset:0 sc1\n"
    "buffer_load_dwordx4 v[116:119], v0, s[4:7], s85 offen offset:1024 sc1\n"
    "buffer_load_dwordx4 v[120:123], v0, s[4:7], s85 offen offset:2048 sc1\n"
    "buffer_load_dwordx4 v[124:127], v0, s[4:7], s85 offen offset:3072 sc1\n"
    "buffer_load_dwordx4 v[128:131], v0, s[4:7], s86 offen offset:0 sc1\n"
    "buffer_load_dwordx4 v[132:135], v0, s[4:7], s86 offen offset:1024 sc1\n"
    "buffer_load_dwordx4 v[136:139], v0, s[4:7], s86 offen offset:2048 sc1\n"
    "buffer_load_dwordx4 v[140:143], v0, s[4:7], s86 offen offset:3072 sc1\n"
    "buffer_load_dwordx4 v[144:147], v0, s[4:7], s87 offen offset:0 sc1\n"
    "buffer_load_dwordx4 v[148:151], v0, s[4:7], s87 offen offset:1024 sc1\n"
    "buffer_load_dwordx4 v[152:155], v0, s[4:7], s87 offen offset:2048 sc1\n"
    "buffer_load_dwordx4 v[156:159], v0, s[4:7], s87 offen offset:3072 sc1\n"
    "buffer_load_dwordx4 v[160:163], v0, s[4:7], s88 offen offset:0 sc1\n"
    "buffer_load_dwordx4 v[164:167], v0, s[4:7], s88 offen offset:1024 sc1\n"
    "buffer_load_dwordx4 v[168:171], v0, s[4:7], s88 offen offset:2048 sc1\n"
    "buffer_load_dwordx4 v[172:175], v0, s[4:7], s88 offen offset:3072 sc1\n"
    "s_waitcnt vmcnt(0)\n"
    "v_max3_u32 v7, v124, v116, v120\n"
    "v_max_u32 v7, v7, v112\n"
    "v_cmp_eq_u32 vcc, -1, v7\n"
    "s_cmp_lg_u64 vcc, 0\n"
    "s_cbranch_scc1 o1s_13_%=\n"
    "s_nop 3\n"
    "s_branch o1r_14_%=\n"
    "o2s_15_%=:\n"
    "s_waitcnt vmcnt(0) lgkmcnt(0)\n"
    "s_nop 7\n"
    "s_cmp_eq_u32 s30, 0\n"
    "s_cbranch_scc1 o2r_16_%=\n"
    "s_sub_u32 s30, s30, 1\n"
    "buffer_load_dwordx4 v[128:131], v0, s[4:7], s86 offen offset:0 sc1\n"
    "buffer_load_dwordx4 v[132:135], v0, s[4:7], s86 offen offset:1024 sc1\n"
    "buffer_load_dwordx4 v[136:139], v0, s[4:7], s86 offen offset:2048 sc1\n"
    "buffer_load_dwordx4 v[140:143], v0, s[4:7], s86 offen offset:3072 sc1\n"
    "buffer_load_dwordx4 v[144:147], v0, s[4:7], s87 offen offset:0 sc1\n"
    "buffer_load_dwordx4 v[148:151], v0, s[4:7], s87 offen offset:1024 sc1\n"
    "buffer_load_dwordx4 v[152:155], v0, s[4:7], s87 offen offset:2048 sc1\n"
    "buffer_load_dwordx4 v[156:159], v0, s[4:7], s87 offen offset:3072 sc1\n"
    "buffer_load_dwordx4 v[160:163], v0, s[4:7], s88 offen offset:0 sc1\n"
    "buffer_load_dwordx4 v[164:167], v0, s[4:7], s88 offen offset:1024 sc1\n"
    "buffer_load_dwordx4 v[168:171], v0, s[4:7], s88 offen offset:2048 sc1\n"
    "buffer_load_dwordx4 v[172:175], v0, s[4:7], s88 offen offset:3072 sc1\n"
    "s_waitcnt vmcnt(0)\n"
    "v_max3_u32 v7, v140, v132, v136\n"
    "v_max_u32 v7, v7, v128\n"
    "v_cmp_eq_u32 vcc, -1, v7\n"
    "s_cmp_lg_u64 vcc, 0\n"
    "s_cbranch_scc1 o2s_15_%=\n"
    "s_nop 3\n"
    "s_branch o2r_16_%=\n"
    "o3s_17_%=:\n"
    "s_waitcnt vmcnt(0) lgkmcnt(0)\n"
    "s_nop 7\n"
    "s_cmp_eq_u32 s30, 0\n"
    "s_cbranch_scc1 o3r_18_%=\n"
    "s_sub_u32 s30, s30, 1\n"
    "buffer_load_dwordx4 v[144:147], v0, s[4:7], s87 offen offset:0 sc1\n"
    "buffer_load_dwordx4 v[148:151], v0, s[4:7], s87 offen offset:1024 sc1\n"
    "buffer_load_dwordx4 v[152:155], v0, s[4:7], s87 offen offset:2048 sc1\n"
    "buffer_load_dwordx4 v[156:159], v0, s[4:7], s87 offen offset:3072 sc1\n"
    "buffer_load_dwordx4 v[160:163], v0, s[4:7], s88 offen offset:0 sc1\n"
    "buffer_load_dwordx4 v[164:167], v0, s[4:7], s88 offen offset:1024 sc1\n"
    "buffer_load_dwordx4 v[168:171], v0, s[4:7], s88 offen offset:2048 sc1\n"
    "buffer_load_dwordx4 v[172:175], v0, s[4:7], s88 offen offset:3072 sc1\n"
    "s_waitcnt vmcnt(0)\n"
    "v_max3_u32 v7, v156, v148, v152\n"
    "v_max_u32 v7, v7, v144\n"
    "v_cmp_eq_u32 vcc, -1, v7\n"
    "s_cmp_lg_u64 vcc, 0\n"
    "s_cbranch_scc1 o3s_17_%=\n"
    "s_nop 3\n"
    "s_branch o3r_18_%=\n"
    "o4s_19_%=:\n"
    "s_waitcnt vmcnt(0) lgkmcnt(0)\n"
    "s_nop 7\n"
    "s_cmp_eq_u32 s30, 0\n"
    "s_cbranch_scc1 o4r_20_%=\n"
    "s_sub_u32 s30, s30, 1\n"
    "buffer_load_dwordx4 v[160:163], v0, s[4:7], s88 offen offset:0 sc1\n"
    "buffer_load_dwordx4 v[164:167], v0, s[4:7], s88 offen offset:1024 sc1\n"
    "buffer_load_dwordx4 v[168:171], v0, s[4:7], s88 offen offset:2048 sc1\n"
    "buffer_load_dwordx4 v[172:175], v0, s[4:7], s88 offen offset:3072 sc1\n"
    "s_waitcnt vmcnt(0)\n"
    "v_max3_u32 v7, v172, v164, v168\n"
    "v_max_u32 v7, v7, v160\n"
    "v_cmp_eq_u32 vcc, -1, v7\n"
    "s_cmp_lg_u64 vcc, 0\n"
    "s_cbranch_scc1 o4s_19_%=\n"
    "s_nop 3\n"
    "s_branch o4r_20_%=\n"
    "h0s_21_%=:\n"
    "s_waitcnt vmcnt(0) lgkmcnt(0)\n"
    "s_nop 7\n"
    "s_cmp_eq_u32 s30, 0\n"
    "s_cbranch_scc1 h0r_22_%=\n"
    "s_sub_u32 s30, s30, 1\n"
    "buffer_load_dwordx4 v[96:99], v0, s[4:7], s36 offen offset:0 sc1\n"
    "buffer_load_dwordx4 v[100:103], v0, s[4:7], s36 offen offset:1024 sc1\n"
    "buffer_load_dwordx4 v[104:107], v0, s[4:7], s36 offen offset:2048 sc1\n"
    "buffer_load_dwordx4 v[108:111], v0, s[4:7], s36 offen offset:3072 sc1\n"
    "buffer_load_dwordx4 v[112:115], v0, s[4:7], s37 offen offset:0 sc1\n"
    "buffer_load_dwordx4 v[116:119], v0, s[4:7], s37 offen offset:1024 sc1\n"
    "buffer_load_dwordx4 v[120:123], v0, s[4:7], s37 offen offset:2048 sc1\n"
    "buffer_load_dwordx4 v[124:127], v0, s[4:7], s37 offen offset:3072 sc1\n"
    "buffer_load_dwordx4 v[128:131], v0, s[4:7], s38 offen offset:0 sc1\n"
    "buffer_load_dwordx4 v[132:135], v0, s[4:7], s38 offen offset:1024 sc1\n"
    "buffer_load_dwordx4 v[136:139], v0, s[4:7], s38 offen offset:2048 sc1\n"
    "buffer_load_dwordx4 v[140:143], v0, s[4:7], s38 offen offset:3072 sc1\n"
    "s_waitcnt vmcnt(0)\n"
    "v_max3_u32 v7, v108, v100, v104\n"
    "v_max_u32 v7, v7, v96\n"
    "v_cmp_eq_u32 vcc, -1, v7\n"
    "s_cmp_lg_u64 vcc, 0\n"
    "s_cbranch_scc1 h0s_21_%=\n"
    "s_nop 3\n"
    "s_branch h0r_22_%=\n"
    "h1s_23_%=:\n"
    "s_waitcnt vmcnt(0) lgkmcnt(0)\n"
    "s_nop 7\n"
    "s_cmp_eq_u32 s30, 0\n"
    "s_cbranch_scc1 h1r_24_%=\n"
    "s_sub_u32 s30, s30, 1\n"
    "buffer_load_dwordx4 v[112:115], v0, s[4:7], s37 offen offset:0 sc1\n"
    "buffer_load_dwordx4 v[116:119], v0, s[4:7], s37 offen offset:1024 sc1\n"
    "buffer_load_dwordx4 v[120:123], v0, s[4:7], s37 offen offset:2048 sc1\n"
    "buffer_load_dwordx4 v[124:127], v0, s[4:7], s37 offen offset:3072 sc1\n"
    "buffer_load_dwordx4 v[128:131], v0, s[4:7], s38 offen offset:0 sc1\n"
    "buffer_load_dwordx4 v[132:135], v0, s[4:7], s38 offen offset:1024 sc1\n"
    "buffer_load_dwordx4 v[136:139], v0, s[4:7], s38 offen offset:2048 sc1\n"
    "buffer_load_dwordx4 v[140:143], v0, s[4:7], s38 offen offset:3072 sc1\n"
    "buffer_load_dwordx4 v[144:147], v0, s[4:7], s39 offen offset:0 sc1\n"
    "buffer_load_dwordx4 v[148:151], v0, s[4:7], s39 offen offset:1024 sc1\n"
    "buffer_load_dwordx4 v[152:155], v0, s[4:7], s39 offen offset:2048 sc1\n"
    "buffer_load_dwordx4 v[156:159], v0, s[4:7], s39 offen offset:3072 sc1\n"
    "buffer_load_dwordx4 v[160:163], v0, s[4:7], s40 offen offset:0 sc1\n"
    "buffer_load_dwordx4 v[164:167], v0, s[4:7], s40 offen offset:1024 sc1\n"
    "buffer_load_dwordx4 v[168:171], v0, s[4:7], s40 offen offset:2048 sc1\n"
    "buffer_load_dwordx4 v[172:175], v0, s[4:7], s40 offen offset:3072 sc1\n"
    "s_waitcnt vmcnt(0)\n"
    "v_max3_u32 v7, v124, v116, v120\n"
    "v_max_u32 v7, v7, v112\n"
    "v_cmp_eq_u32 vcc, -1, v7\n"
    "s_cmp_lg_u64 vcc, 0\n"
    "s_cbranch_scc1 h1s_23_%=\n"
    "s_nop 3\n"
    "s_branch h1r_24_%=\n"
    "h2s_25_%=:\n"
    "s_waitcnt vmcnt(0) lgkmcnt(0)\n"
    "s_nop 7\n"
    "s_cmp_eq_u32 s30, 0\n"
    "s_cbranch_scc1 h2r_26_%=\n"
    "s_sub_u32 s30, s30, 1\n"
    "buffer_load_dwordx4 v[128:131], v0, s[4:7], s38 offen offset:0 sc1\n"
    "buffer_load_dwordx4 v[132:135], v0, s[4:7], s38 offen offset:1024 sc1\n"
    "buffer_load_dwordx4 v[136:139], v0, s[4:7], s38 offen offset:2048 sc1\n"
    "buffer_load_dwordx4 v[140:143], v0, s[4:7], s38 offen offset:3072 sc1\n"
    "buffer_load_dwordx4 v[144:147], v0, s[4:7], s39 offen offset:0 sc1\n"
    "buffer_load_dwordx4 v[148:151], v0, s[4:7], s39 offen offset:1024 sc1\n"
    "buffer_load_dwordx4 v[152:155], v0, s[4:7], s39 offen offset:2048 sc1\n"
    "buffer_load_dwordx4 v[156:159], v0, s[4:7], s39 offen offset:3072 sc1\n"
    "buffer_load_dwordx4 v[160:163], v0, s[4:7], s40 offen offset:0 sc1\n"
    "buffer_load_dwordx4 v[164:167], v0, s[4:7], s40 offen offset:1024 sc1\n"
    "buffer_load_dwordx4 v[168:171], v0, s[4:7], s40 offen offset:2048 sc1\n"
    "buffer_load_dwordx4 v[172:175], v0, s[4:7], s40 offen offset:3072 sc1\n"
    "s_waitcnt vmcnt(0)\n"
    "v_max3_u32 v7, v140, v132, v136\n"
    "v_max_u32 v7, v7, v128\n"
    "v_cmp_eq_u32 vcc, -1, v7\n"
    "s_cmp_lg_u64 vcc, 0\n"
    "s_cbranch_scc1 h2s_25_%=\n"
    "s_nop 3\n"
    "s_branch h2r_26_%=\n"
    "h3s_27_%=:\n"
    "s_waitcnt vmcnt(0) lgkmcnt(0)\n"
    "s_nop 7\n"
    "s_cmp_eq_u32 s30, 0\n"
    "s_cbranch_scc1 h3r_28_%=\n"
    "s_sub_u32 s30, s30, 1\n"
    "buffer_load_dwordx4 v[144:147], v0, s[4:7], s39 offen offset:0 sc1\n"
    "buffer_load_dwordx4 v[148:151], v0, s[4:7], s39 offen offset:1024 sc1\n"
    "buffer_load_dwordx4 v[152:155], v0, s[4:7], s39 offen offset:2048 sc1\n"
    "buffer_load_dwordx4 v[156:159], v0, s[4:7], s39 offen offset:3072 sc1\n"
    "buffer_load_dwordx4 v[160:163], v0, s[4:7], s40 offen offset:0 sc1\n"
    "buffer_load_dwordx4 v[164:167], v0, s[4:7], s40 offen offset:1024 sc1\n"
    "buffer_load_dwordx4 v[168:171], v0, s[4:7], s40 offen offset:2048 sc1\n"
    "buffer_load_dwordx4 v[172:175], v0, s[4:7], s40 offen offset:3072 sc1\n"
    "s_waitcnt vmcnt(0)\n"
    "v_max3_u32 v7, v156, v148, v152\n"
    "v_max_u32 v7, v7, v144\n"
    "v_cmp_eq_u32 vcc, -1, v7\n"
    "s_cmp_lg_u64 vcc, 0\n"
    "s_cbranch_scc1 h3s_27_%=\n"
    "s_nop 3\n"
    "s_branch h3r_28_%=\n"
    "h4s_29_%=:\n"
    "s_waitcnt vmcnt(0) lgkmcnt(0)\n"
    "s_nop 7\n"
    "s_cmp_eq_u32 s30, 0\n"
    "s_cbranch_scc1 h4r_30_%=\n"
    "s_sub_u32 s30, s30, 1\n"
    "buffer_load_dwordx4 v[160:163], v0, s[4:7], s40 offen offset:0 sc1\n"
    "buffer_load_dwordx4 v[164:167], v0, s[4:7], s40 offen offset:1024 sc1\n"
    "buffer_load_dwordx4 v[168:171], v0, s[4:7], s40 offen offset:2048 sc1\n"
    "buffer_load_dwordx4 v[172:175], v0, s[4:7], s40 offen offset:3072 sc1\n"
    "s_waitcnt vmcnt(0)\n"
    "v_max3_u32 v7, v172, v164, v168\n"
    "v_max_u32 v7, v7, v160\n"
    "v_cmp_eq_u32 vcc, -1, v7\n"
    "s_cmp_lg_u64 vcc, 0\n"
    "s_cbranch_scc1 h4s_29_%=\n"
    "s_nop 3\n"
    "s_branch h4r_30_%=\n"
    "h5s_31_%=:\n"
    "s_waitcnt vmcnt(0) lgkmcnt(0)\n"
    "s_nop 7\n"
    "s_cmp_eq_u32 s30, 0\n"
    "s_cbranch_scc1 h5r_32_%=\n"
    "s_sub_u32 s30, s30, 1\n"
    "buffer_load_dwordx4 v[96:99], v88, s[4:7], s36 offen offset:0 sc1\n"
    "buffer_load_dwordx4 v[100:103], v88, s[4:7], s36 offen offset:1024 sc1\n"
    "buffer_load_dwordx4 v[104:107], v88, s[4:7], s36 offen offset:2048 sc1\n"
    "buffer_load_dwordx4 v[108:111], v88, s[4:7], s36 offen offset:3072 sc1\n"
    "buffer_load_dwordx4 v[112:115], v88, s[4:7], s37 offen offset:0 sc1\n"
    "buffer_load_dwordx4 v[116:119], v88, s[4:7], s37 offen offset:1024 sc1\n"
    "buffer_load_dwordx4 v[120:123], v88, s[4:7], s37 offen offset:2048 sc1\n"
    "buffer_load_dwordx4 v[124:127], v88, s[4:7], s37 offen offset:3072 sc1\n"
    "buffer_load_dwordx4 v[128:131], v88, s[4:7], s38 offen offset:0 sc1\n"
    "buffer_load_dwordx4 v[132:135], v88, s[4:7], s38 offen offset:1024 sc1\n"
    "buffer_load_dwordx4 v[136:139], v88, s[4:7], s38 offen offset:2048 sc1\n"
    "buffer_load_dwordx4 v[140:143], v88, s[4:7], s38 offen offset:3072 sc1\n"
    "buffer_load_dwordx4 v[144:147], v88, s[4:7], s39 offen offset:0 sc1\n"
    "buffer_load_dwordx4 v[148:151], v88, s[4:7], s39 offen offset:1024 sc1\n"
    "buffer_load_dwordx4 v[152:155], v88, s[4:7], s39 offen offset:2048 sc1\n"
    "buffer_load_dwordx4 v[156:159], v88, s[4:7], s39 offen offset:3072 sc1\n"
    "buffer_load_dwordx4 v[160:163], v88, s[4:7], s40 offen offset:0 sc1\n"
    "buffer_load_dwordx4 v[164:167], v88, s[4:7], s40 offen offset:1024 sc1\n"
    "buffer_load_dwordx4 v[168:171], v88, s[4:7], s40 offen offset:2048 sc1\n"
    "buffer_load_dwordx4 v[172:175], v88, s[4:7], s40 offen offset:3072 sc1\n"
    "s_waitcnt vmcnt(0)\n"
    "v_max3_u32 v7, v108, v100, v104\n"
    "v_max_u32 v7, v7, v96\n"
    "v_cmp_eq_u32 vcc, -1, v7\n"
    "s_cmp_lg_u64 vcc, 0\n"
    "s_cbranch_scc1 h5s_31_%=\n"
    "s_nop 3\n"
    "s_branch h5r_32_%=\n"
    "h6s_33_%=:\n"
    "s_waitcnt vmcnt(0) lgkmcnt(0)\n"
    "s_nop 7\n"
    "s_cmp_eq_u32 s30, 0\n"
    "s_cbranch_scc1 h6r_34_%=\n"
    "s_sub_u32 s30, s30, 1\n"
    "buffer_load_dwordx4 v[112:115], v88, s[4:7], s37 offen offset:0 sc1\n"
    "buffer_load_dwordx4 v[116:119], v88, s[4:7], s37 offen offset:1024 sc1\n"
    "buffer_load_dwordx4 v[120:123], v88, s[4:7], s37 offen offset:2048 sc1\n"
    "buffer_load_dwordx4 v[124:127], v88, s[4:7], s37 offen offset:3072 sc1\n"
    "buffer_load_dwordx4 v[128:131], v88, s[4:7], s38 offen offset:0 sc1\n"
    "buffer_load_dwordx4 v[132:135], v88, s[4:7], s38 offen offset:1024 sc1\n"
    "buffer_load_dwordx4 v[136:139], v88, s[4:7], s38 offen offset:2048 sc1\n"
    "buffer_load_dwordx4 v[140:143], v88, s[4:7], s38 offen offset:3072 sc1\n"
    "buffer_load_dwordx4 v[144:147], v88, s[4:7], s39 offen offset:0 sc1\n"
    "buffer_load_dwordx4 v[148:151], v88, s[4:7], s39 offen offset:1024 sc1\n"
    "buffer_load_dwordx4 v[152:155], v88, s[4:7], s39 offen offset:2048 sc1\n"
    "buffer_load_dwordx4 v[156:159], v88, s[4:7], s39 offen offset:3072 sc1\n"
    "buffer_load_dwordx4 v[160:163], v88, s[4:7], s40 offen offset:0 sc1\n"
    "buffer_load_dwordx4 v[164:167], v88, s[4:7], s40 offen offset:1024 sc1\n"
    "buffer_load_dwordx4 v[168:171], v88, s[4:7], s40 offen offset:2048 sc1\n"
    "buffer_load_dwordx4 v[172:175], v88, s[4:7], s40 offen offset:3072 sc1\n"
    "s_waitcnt vmcnt(0)\n"
    "v_max3_u32 v7, v124, v116, v120\n"
    "v_max_u32 v7, v7, v112\n"
    "v_cmp_eq_u32 vcc, -1, v7\n"
    "s_cmp_lg_u64 vcc, 0\n"
    "s_cbranch_scc1 h6s_33_%=\n"
    "s_nop 3\n"
    "s_branch h6r_34_%=\n"
    "h7s_35_%=:\n"
    "s_waitcnt vmcnt(0) lgkmcnt(0)\n"
    "s_nop 7\n"
    "s_cmp_eq_u32 s30, 0\n"
    "s_cbranch_scc1 h7r_36_%=\n"
    "s_sub_u32 s30, s30, 1\n"
    "buffer_load_dwordx4 v[128:131], v88, s[4:7], s38 offen offset:0 sc1\n"
    "buffer_load_dwordx4 v[132:135], v88, s[4:7], s38 offen offset:1024 sc1\n"
    "buffer_load_dwordx4 v[136:139], v88, s[4:7], s38 offen offset:2048 sc1\n"
    "buffer_load_dwordx4 v[140:143], v88, s[4:7], s38 offen offset:3072 sc1\n"
    "buffer_load_dwordx4 v[144:147], v88, s[4:7], s39 offen offset:0 sc1\n"
    "buffer_load_dwordx4 v[148:151], v88, s[4:7], s39 offen offset:1024 sc1\n"
    "buffer_load_dwordx4 v[152:155], v88, s[4:7], s39 offen offset:2048 sc1\n"
    "buffer_load_dwordx4 v[156:159], v88, s[4:7], s39 offen offset:3072 sc1\n"
    "buffer_load_dwordx4 v[160:163], v88, s[4:7], s40 offen offset:0 sc1\n"
    "buffer_load_dwordx4 v[164:167], v88, s[4:7], s40 offen offset:1024 sc1\n"
    "buffer_load_dwordx4 v[168:171], v88, s[4:7], s40 offen offset:2048 sc1\n"
    "buffer_load_dwordx4 v[172:175], v88, s[4:7], s40 offen offset:3072 sc1\n"
    "s_waitcnt vmcnt(0)\n"
    "v_max3_u32 v7, v140, v132, v136\n"
    "v_max_u32 v7, v7, v128\n"
    "v_cmp_eq_u32 vcc, -1, v7\n"
    "s_cmp_lg_u64 vcc, 0\n"
    "s_cbranch_scc1 h7s_35_%=\n"
    "s_nop 3\n"
    "s_branch h7r_36_%=\n"
    "h8s_37_%=:\n"
    "s_waitcnt vmcnt(0) lgkmcnt(0)\n"
    "s_nop 7\n"
    "s_cmp_eq_u32 s30, 0\n"
    "s_cbranch_scc1 h8r_38_%=\n"
    "s_sub_u32 s30, s30, 1\n"
    "buffer_load_dwordx4 v[144:147], v88, s[4:7], s39 offen offset:0 sc1\n"
    "buffer_load_dwordx4 v[148:151], v88, s[4:7], s39 offen offset:1024 sc1\n"
    "buffer_load_dwordx4 v[152:155], v88, s[4:7], s39 offen offset:2048 sc1\n"
    "buffer_load_dwordx4 v[156:159], v88, s[4:7], s39 offen offset:3072 sc1\n"
    "buffer_load_dwordx4 v[160:163], v88, s[4:7], s40 offen offset:0 sc1\n"
    "buffer_load_dwordx4 v[164:167], v88, s[4:7], s40 offen offset:1024 sc1\n"
    "buffer_load_dwordx4 v[168:171], v88, s[4:7], s40 offen offset:2048 sc1\n"
    "buffer_load_dwordx4 v[172:175], v88, s[4:7], s40 offen offset:3072 sc1\n"
    "s_waitcnt vmcnt(0)\n"
    "v_max3_u32 v7, v156, v148, v152\n"
    "v_max_u32 v7, v7, v144\n"
    "v_cmp_eq_u32 vcc, -1, v7\n"
    "s_cmp_lg_u64 vcc, 0\n"
    "s_cbranch_scc1 h8s_37_%=\n"
    "s_nop 3\n"
    "s_branch h8r_38_%=\n"
    "h9s_39_%=:\n"
    "s_waitcnt vmcnt(0) lgkmcnt(0)\n"
    "s_nop 7\n"
    "s_cmp_eq_u32 s30, 0\n"
    "s_cbranch_scc1 h9r_40_%=\n"
    "s_sub_u32 s30, s30, 1\n"
    "buffer_load_dwordx4 v[160:163], v88, s[4:7], s40 offen offset:0 sc1\n"
    "buffer_load_dwordx4 v[164:167], v88, s[4:7], s40 offen offset:1024 sc1\n"
    "buffer_load_dwordx4 v[168:171], v88, s[4:7], s40 offen offset:2048 sc1\n"
    "buffer_load_dwordx4 v[172:175], v88, s[4:7], s40 offen offset:3072 sc1\n"
    "s_waitcnt vmcnt(0)\n"
    "v_max3_u32 v7, v172, v164, v168\n"
    "v_max_u32 v7, v7, v160\n"
    "v_cmp_eq_u32 vcc, -1, v7\n"
    "s_cmp_lg_u64 vcc, 0\n"
    "s_cbranch_scc1 h9s_39_%=\n"
    "s_nop 3\n"
    "s_branch h9r_40_%=\n"
    "sts_41_%=:\n"
    "s_waitcnt vmcnt(0) lgkmcnt(0)\n"
    "s_nop 7\n"
    "s_cmp_eq_u32 s30, 0\n"
    "s_cbranch_scc1 str_42_%=\n"
    "s_sub_u32 s30, s30, 1\n"
    "buffer_load_dwordx4 v[80:83], v78, s[4:7], s41 offen sc1\n"
    "buffer_load_dwordx4 v[84:87], v89, s[4:7], s41 offen sc1\n"
    "s_waitcnt vmcnt(0)\n"
    "v_add_f32 v80, v80, v82\n"
    "v_add_f32 v81, v81, v83\n"
    "v_add_f32 v84, v84, v86\n"
    "v_add_f32 v85, v85, v87\n"
    "v_cmp_u_f32 vcc, v80, v84\n"
    "s_cmp_lg_u64 vcc, 0\n"
    "s_cbranch_scc1 sts_41_%=\n"
    "s_nop 3\n"
    "s_branch str_42_%=\n"
    "gfs_43_%=:\n"
    "s_waitcnt vmcnt(0) lgkmcnt(0)\n"
    "s_nop 7\n"
    "s_cmp_eq_u32 s30, 0\n"
    "s_cbranch_scc1 gfr_44_%=\n"
    "s_sub_u32 s30, s30, 1\n"
    "s_mov_b32 s32, 32768\n"
    "buffer_load_dwordx4 v[92:95], v0, s[4:7], s32 offen offset:1024 sc1\n"
    "s_waitcnt vmcnt(0)\n"
    "v_xor_b32 v92, s29, v92\n"
    "v_xor_b32 v93, s29, v93\n"
    "v_xor_b32 v94, s29, v94\n"
    "v_xor_b32 v95, s29, v95\n"
    "v_or3_b32 v92, v92, v93, v94\n"
    "v_or_b32 v92, v92, v95\n"
    "v_cmp_ne_u32 vcc, 0, v92\n"
    "s_cmp_lg_u64 vcc, 0\n"
    "s_cbranch_scc1 gfs_43_%=\n"
    "s_nop 3\n"
    "s_branch gfr_44_%=\n"
    "f0s_45_%=:\n"
    "s_waitcnt vmcnt(0) lgkmcnt(0)\n"
    "s_nop 7\n"
    "s_cmp_eq_u32 s30, 0\n"
    "s_cbranch_scc1 f0r_46_%=\n"
    "s_sub_u32 s30, s30, 1\n"
    "buffer_load_dwordx4 v[96:99], v176, s[4:7], s52 offen sc1\n"
    "buffer_load_dwordx4 v[100:103], v176, s[4:7], s53 offen sc1\n"
    "buffer_load_dwordx4 v[104:107], v176, s[4:7], s55 offen sc1\n"
    "buffer_load_dwordx4 v[108:111], v176, s[4:7], s56 offen sc1\n"
    "buffer_load_dwordx4 v[112:115], v176, s[4:7], s58 offen sc1\n"
    "buffer_load_dwordx4 v[116:119], v176, s[4:7], s59 offen sc1\n"
    "buffer_load_dwordx4 v[120:123], v176, s[4:7], s61 offen sc1\n"
    "buffer_load_dwordx4 v[124:127], v176, s[4:7], s62 offen sc1\n"
    "buffer_load_dwordx4 v[128:131], v177, s[4:7], s52 offen sc1\n"
    "buffer_load_dwordx4 v[132:135], v177, s[4:7], s53 offen sc1\n"
    "buffer_load_dwordx4 v[136:139], v177, s[4:7], s55 offen sc1\n"
    "buffer_load_dwordx4 v[140:143], v177, s[4:7], s56 offen sc1\n"
    "s_waitcnt vmcnt(0)\n"
    "v_max3_u32 v7, v108, v100, v104\n"
    "v_max_u32 v7, v7, v96\n"
    "v_cmp_eq_u32 vcc, -1, v7\n"
    "s_cmp_lg_u64 vcc, 0\n"
    "s_cbranch_scc1 f0s_45_%=\n"
    "s_nop 3\n"
    "s_branch f0r_46_%=\n"
    "f1s_47_%=:\n"
    "s_waitcnt vmcnt(0) lgkmcnt(0)\n"
    "s_nop 7\n"
    "s_cmp_eq_u32 s30, 0\n"
    "s_cbranch_scc1 f1r_48_%=\n"
    "s_sub_u32 s30, s30, 1\n"
    "buffer_load_dwordx4 v[112:115], v176, s[4:7], s58 offen sc1\n"
    "buffer_load_dwordx4 v[116:119], v176, s[4:7], s59 offen sc1\n"
    "buffer_load_dwordx4 v[120:123], v176, s[4:7], s61 offen sc1\n"
    "buffer_load_dwordx4 v[124:127], v176, s[4:7], s62 offen sc1\n"
    "buffer_load_dwordx4 v[128:131], v177, s[4:7], s52 offen sc1\n"
    "buffer_load_dwordx4 v[132:135], v177, s[4:7], s53 offen sc1\n"
    "buffer_load_dwordx4 v[136:139], v177, s[4:7], s55 offen sc1\n"
    "buffer_load_dwordx4 v[140:143], v177, s[4:7], s56 offen sc1\n"
    "buffer_load_dwordx4 v[144:147], v177, s[4:7], s58 offen sc1\n"
    "buffer_load_dwordx4 v[148:151], v177, s[4:7], s59 offen sc1\n"
    "buffer_load_dwordx4 v[152:155], v177, s[4:7], s61 offen sc1\n"
    "buffer_load_dwordx4 v[156:159], v177, s[4:7], s62 offen sc1\n"
    "s_waitcnt vmcnt(0)\n"
    "v_max3_u32 v7, v124, v116, v120\n"
    "v_max_u32 v7, v7, v112\n"
    "v_cmp_eq_u32 vcc, -1, v7\n"
    "s_cmp_lg_u64 vcc, 0\n"
    "s_cbranch_scc1 f1s_47_%=\n"
    "s_nop 3\n"
    "s_branch f1r_48_%=\n"
    "f2s_49_%=:\n"
    "s_waitcnt vmcnt(0) lgkmcnt(0)\n"
    "s_nop 7\n"
    "s_cmp_eq_u32 s30, 0\n"
    "s_cbranch_scc1 f2r_50_%=\n"
    "s_sub_u32 s30, s30, 1\n"
    "buffer_load_dwordx4 v[128:131], v177, s[4:7], s52 offen sc1\n"
    "buffer_load_dwordx4 v[132:135], v177, s[4:7], s53 offen sc1\n"
    "buffer_load_dwordx4 v[136:139], v177, s[4:7], s55 offen sc1\n"
    "buffer_load_dwordx4 v[140:143], v177, s[4:7], s56 offen sc1\n"
    "buffer_load_dwordx4 v[144:147], v177, s[4:7], s58 offen sc1\n"
    "buffer_load_dwordx4 v[148:151], v177, s[4:7], s59 offen sc1\n"
    "buffer_load_dwordx4 v[152:155], v177, s[4:7], s61 offen sc1\n"
    "buffer_load_dwordx4 v[156:159], v177, s[4:7], s62 offen sc1\n"
    "buffer_load_dwordx2 v[160:161], v14, s[4:7], s44 offen offset:1024 sc1\n"
    "buffer_load_dwordx4 v[164:167], v176, s[4:7], s54 offen sc1\n"
    "buffer_load_dwordx4 v[168:171], v176, s[4:7], s57 offen sc1\n"
    "buffer_load_dwordx4 v[172:175], v176, s[4:7], s60 offen sc1\n"
    "s_waitcnt vmcnt(0)\n"
    "v_max3_u32 v7, v140, v132, v136\n"
    "v_max_u32 v7, v7, v128\n"
    "v_cmp_eq_u32 vcc, -1, v7\n"
    "s_cmp_lg_u64 vcc, 0\n"
    "s_cbranch_scc1 f2s_49_%=\n"
    "s_nop 3\n"
    "s_branch f2r_50_%=\n"
    "f3s_51_%=:\n"
    "s_waitcnt vmcnt(0) lgkmcnt(0)\n"
    "s_nop 7\n"
    "s_cmp_eq_u32 s30, 0\n"
    "s_cbranch_scc1 f3r_52_%=\n"
    "s_sub_u32 s30, s30, 1\n"
    "buffer_load_dwordx4 v[144:147], v177, s[4:7], s58 offen sc1\n"
    "buffer_load_dwordx4 v[148:151], v177, s[4:7], s59 offen sc1\n"
    "buffer_load_dwordx4 v[152:155], v177, s[4:7], s61 offen sc1\n"
    "buffer_load_dwordx4 v[156:159], v177, s[4:7], s62 offen sc1\n"
    "buffer_load_dwordx2 v[160:161], v14, s[4:7], s44 offen offset:1024 sc1\n"
    "buffer_load_dwordx4 v[164:167], v176, s[4:7], s54 offen sc1\n"
    "buffer_load_dwordx4 v[168:171], v176, s[4:7], s57 offen sc1\n"
    "buffer_load_dwordx4 v[172:175], v176, s[4:7], s60 offen sc1\n"
    "buffer_load_dwordx4 v[96:99], v176, s[4:7], s63 offen sc1\n"
    "buffer_load_dwordx4 v[100:103], v176, s[4:7], s64 offen sc1\n"
    "buffer_load_dwordx2 v[104:105], v14, s[4:7], s45 offen offset:1024 sc1\n"
    "buffer_load_dwordx4 v[108:111], v177, s[4:7], s54 offen sc1\n"
    "s_waitcnt vmcnt(0)\n"
    "v_max3_u32 v7, v156, v148, v152\n"
    "v_max_u32 v7, v7, v144\n"
    "v_cmp_eq_u32 vcc, -1, v7\n"
    "s_cmp_lg_u64 vcc, 0\n"
    "s_cbranch_scc1 f3s_51_%=\n"
    "s_nop 3\n"
    "s_branch f3r_52_%=\n"
    "f4s_53_%=:\n"
    "s_waitcnt vmcnt(0) lgkmcnt(0)\n"
    "s_nop 7\n"
    "s_cmp_eq_u32 s30, 0\n"
    "s_cbranch_scc1 f4r_54_%=\n"
    "s_sub_u32 s30, s30, 1\n"
    "buffer_load_dwordx2 v[160:161], v14, s[4:7], s44 offen offset:1024 sc1\n"
    "buffer_load_dwordx4 v[164:167], v176, s[4:7], s54 offen sc1\n"
    "buffer_load_dwordx4 v[168:171], v176, s[4:7], s57 offen sc1\n"
    "buffer_load_dwordx4 v[172:175], v176, s[4:7], s60 offen sc1\n"
    "buffer_load_dwordx4 v[96:99], v176, s[4:7], s63 offen sc1\n"
    "buffer_load_dwordx4 v[100:103], v176, s[4:7], s64 offen sc1\n"
    "buffer_load_dwordx2 v[104:105], v14, s[4:7], s45 offen offset:1024 sc1\n"
    "buffer_load_dwordx4 v[108:111], v177, s[4:7], s54 offen sc1\n"
    "buffer_load_dwordx4 v[112:115], v177, s[4:7], s57 offen sc1\n"
    "buffer_load_dwordx4 v[116:119], v177, s[4:7], s60 offen sc1\n"
    "buffer_load_dwordx4 v[120:123], v177, s[4:7], s63 offen sc1\n"
    "buffer_load_dwordx4 v[124:127], v177, s[4:7], s64 offen sc1\n"
    "s_waitcnt vmcnt(0)\n"
    "v_max3_u32 v7, v172, v164, v168\n"
    "v_max_u32 v7, v7, v160\n"
    "v_cmp_eq_u32 vcc, -1, v7\n"
    "s_cmp_lg_u64 vcc, 0\n"
    "s_cbranch_scc1 f4s_53_%=\n"
    "s_nop 3\n"
    "s_branch f4r_54_%=\n"
    "f5s_55_%=:\n"
    "s_waitcnt vmcnt(0) lgkmcnt(0)\n"
    "s_nop 7\n"
    "s_cmp_eq_u32 s30, 0\n"
    "s_cbranch_scc1 f5r_56_%=\n"
    "s_sub_u32 s30, s30, 1\n"
    "buffer_load_dwordx4 v[96:99], v176, s[4:7], s63 offen sc1\n"
    "buffer_load_dwordx4 v[100:103], v176, s[4:7], s64 offen sc1\n"
    "buffer_load_dwordx2 v[104:105], v14, s[4:7], s45 offen offset:1024 sc1\n"
    "buffer_load_dwordx4 v[108:111], v177, s[4:7], s54 offen sc1\n"
    "buffer_load_dwordx4 v[112:115], v177, s[4:7], s57 offen sc1\n"
    "buffer_load_dwordx4 v[116:119], v177, s[4:7], s60 offen sc1\n"
    "buffer_load_dwordx4 v[120:123], v177, s[4:7], s63 offen sc1\n"
    "buffer_load_dwordx4 v[124:127], v177, s[4:7], s64 offen sc1\n"
    "buffer_load_dwordx4 v[128:131], v178, s[4:7], s52 offen sc1\n"
    "buffer_load_dwordx4 v[132:135], v178, s[4:7], s53 offen sc1\n"
    "buffer_load_dwordx4 v[136:139], v178, s[4:7], s55 offen sc1\n"
    "buffer_load_dwordx4 v[140:143], v178, s[4:7], s56 offen sc1\n"
    "s_waitcnt vmcnt(0)\n"
    "v_max3_u32 v7, v108, v100, v104\n"
    "v_max_u32 v7, v7, v96\n"
    "v_cmp_eq_u32 vcc, -1, v7\n"
    "s_cmp_lg_u64 vcc, 0\n"
    "s_cbranch_scc1 f5s_55_%=\n"
    "s_nop 3\n"
    "s_branch f5r_56_%=\n"
    "f6s_57_%=:\n"
    "s_waitcnt vmcnt(0) lgkmcnt(0)\n"
    "s_nop 7\n"
    "s_cmp_eq_u32 s30, 0\n"
    "s_cbranch_scc1 f6r_58_%=\n"
    "s_sub_u32 s30, s30, 1\n"
    "buffer_load_dwordx4 v[112:115], v177, s[4:7], s57 offen sc1\n"
    "buffer_load_dwordx4 v[116:119], v177, s[4:7], s60 offen sc1\n"
    "buffer_load_dwordx4 v[120:123], v177, s[4:7], s63 offen sc1\n"
    "buffer_load_dwordx4 v[124:127], v177, s[4:7], s64 offen sc1\n"
    "buffer_load_dwordx4 v[128:131], v178, s[4:7], s52 offen sc1\n"
    "buffer_load_dwordx4 v[132:135], v178, s[4:7], s53 offen sc1\n"
    "buffer_load_dwordx4 v[136:139], v178, s[4:7], s55 offen sc1\n"
    "buffer_load_dwordx4 v[140:143], v178, s[4:7], s56 offen sc1\n"
    "buffer_load_dwordx4 v[144:147], v178, s[4:7], s58 offen sc1\n"
    "buffer_load_dwordx4 v[148:151], v178, s[4:7], s59 offen sc1\n"
    "buffer_load_dwordx4 v[152:155], v178, s[4:7], s61 offen sc1\n"
    "buffer_load_dwordx4 v[156:159], v178, s[4:7], s62 offen sc1\n"
    "s_waitcnt vmcnt(0)\n"
    "v_max3_u32 v7, v124, v116, v120\n"
    "v_max_u32 v7, v7, v112\n"
    "v_cmp_eq_u32 vcc, -1, v7\n"
    "s_cmp_lg_u64 vcc, 0\n"
    "s_cbranch_scc1 f6s_57_%=\n"
    "s_nop 3\n"
    "s_branch f6r_58_%=\n"
    "f7s_59_%=:\n"
    "s_waitcnt vmcnt(0) lgkmcnt(0)\n"
    "s_nop 7\n"
    "s_cmp_eq_u32 s30, 0\n"
    "s_cbranch_scc1 f7r_60_%=\n"
    "s_sub_u32 s30, s30, 1\n"
    "buffer_load_dwordx4 v[128:131], v178, s[4:7], s52 offen sc1\n"
    "buffer_load_dwordx4 v[132:135], v178, s[4:7], s53 offen sc1\n"
    "buffer_load_dwordx4 v[136:139], v178, s[4:7], s55 offen sc1\n"
    "buffer_load_dwordx4 v[140:143], v178, s[4:7], s56 offen sc1\n"
    "buffer_load_dwordx4 v[144:147], v178, s[4:7], s58 offen sc1\n"
    "buffer_load_dwordx4 v[148:151], v178, s[4:7], s59 offen sc1\n"
    "buffer_load_dwordx4 v[152:155], v178, s[4:7], s61 offen sc1\n"
    "buffer_load_dwordx4 v[156:159], v178, s[4:7], s62 offen sc1\n"
    "buffer_load_dwordx4 v[160:163], v179, s[4:7], s52 offen sc1\n"
    "buffer_load_dwordx4 v[164:167], v179, s[4:7], s53 offen sc1\n"
    "buffer_load_dwordx4 v[168:171], v179, s[4:7], s55 offen sc1\n"
    "buffer_load_dwordx4 v[172:175], v179, s[4:7], s56 offen sc1\n"
    "s_waitcnt vmcnt(0)\n"
    "v_max3_u32 v7, v140, v132, v136\n"
    "v_max_u32 v7, v7, v128\n"
    "v_cmp_eq_u32 vcc, -1, v7\n"
    "s_cmp_lg_u64 vcc, 0\n"
    "s_cbranch_scc1 f7s_59_%=\n"
    "s_nop 3\n"
    "s_branch f7r_60_%=\n"
    "f8s_61_%=:\n"
    "s_waitcnt vmcnt(0) lgkmcnt(0)\n"
    "s_nop 7\n"
    "s_cmp_eq_u32 s30, 0\n"
    "s_cbranch_scc1 f8r_62_%=\n"
    "s_sub_u32 s30, s30, 1\n"
    "buffer_load_dwordx4 v[144:147], v178, s[4:7], s58 offen sc1\n"
    "buffer_load_dwordx4 v[148:151], v178, s[4:7], s59 offen sc1\n"
    "buffer_load_dwordx4 v[152:155], v178, s[4:7], s61 offen sc1\n"
    "buffer_load_dwordx4 v[156:159], v178, s[4:7], s62 offen sc1\n"
    "buffer_load_dwordx4 v[160:163], v179, s[4:7], s52 offen sc1\n"
    "buffer_load_dwordx4 v[164:167], v179, s[4:7], s53 offen sc1\n"
    "buffer_load_dwordx4 v[168:171], v179, s[4:7], s55 offen sc1\n"
    "buffer_load_dwordx4 v[172:175], v179, s[4:7], s56 offen sc1\n"
    "buffer_load_dwordx4 v[96:99], v179, s[4:7], s58 offen sc1\n"
    "buffer_load_dwordx4 v[100:103], v179, s[4:7], s59 offen sc1\n"
    "buffer_load_dwordx4 v[104:107], v179, s[4:7], s61 offen sc1\n"
    "buffer_load_dwordx4 v[108:111], v179, s[4:7], s62 offen sc1\n"
    "s_waitcnt vmcnt(0)\n"
    "v_max3_u32 v7, v156, v148, v152\n"
    "v_max_u32 v7, v7, v144\n"
    "v_cmp_eq_u32 vcc, -1, v7\n"
    "s_cmp_lg_u64 vcc, 0\n"
    "s_cbranch_scc1 f8s_61_%=\n"
    "s_nop 3\n"
    "s_branch f8r_62_%=\n"
    "f9s_63_%=:\n"
    "s_waitcnt vmcnt(0) lgkmcnt(0)\n"
    "s_nop 7\n"
    "s_cmp_eq_u32 s30, 0\n"
    "s_cbranch_scc1 f9r_64_%=\n"
    "s_sub_u32 s30, s30, 1\n"
    "buffer_load_dwordx4 v[160:163], v179, s[4:7], s52 offen sc1\n"
    "buffer_load_dwordx4 v[164:167], v179, s[4:7], s53 offen sc1\n"
    "buffer_load_dwordx4 v[168:171], v179, s[4:7], s55 offen sc1\n"
    "buffer_load_dwordx4 v[172:175], v179, s[4:7], s56 offen sc1\n"
    "buffer_load_dwordx4 v[96:99], v179, s[4:7], s58 offen sc1\n"
    "buffer_load_dwordx4 v[100:103], v179, s[4:7], s59 offen sc1\n"
    "buffer_load_dwordx4 v[104:107], v179, s[4:7], s61 offen sc1\n"
    "buffer_load_dwordx4 v[108:111], v179, s[4:7], s62 offen sc1\n"
    "buffer_load_dwordx2 v[112:113], v14, s[4:7], s25 offen offset:1024 sc1\n"
    "buffer_load_dwordx4 v[116:119], v178, s[4:7], s54 offen sc1\n"
    "buffer_load_dwordx4 v[120:123], v178, s[4:7], s57 offen sc1\n"
    "buffer_load_dwordx4 v[124:127], v178, s[4:7], s60 offen sc1\n"
    "s_waitcnt vmcnt(0)\n"
    "v_max3_u32 v7, v172, v164, v168\n"
    "v_max_u32 v7, v7, v160\n"
    "v_cmp_eq_u32 vcc, -1, v7\n"
    "s_cmp_lg_u64 vcc, 0\n"
    "s_cbranch_scc1 f9s_63_%=\n"
    "s_nop 3\n"
    "s_branch f9r_64_%=\n"
    "f10s_65_%=:\n"
    "s_waitcnt vmcnt(0) lgkmcnt(0)\n"
    "s_nop 7\n"
    "s_cmp_eq_u32 s30, 0\n"
    "s_cbranch_scc1 f10r_66_%=\n"
    "s_sub_u32 s30, s30, 1\n"
    "buffer_load_dwordx4 v[96:99], v179, s[4:7], s58 offen sc1\n"
    "buffer_load_dwordx4 v[100:103], v179, s[4:7], s59 offen sc1\n"
    "buffer_load_dwordx4 v[104:107], v179, s[4:7], s61 offen sc1\n"
    "buffer_load_dwordx4 v[108:111], v179, s[4:7], s62 offen sc1\n"
    "buffer_load_dwordx2 v[112:113], v14, s[4:7], s25 offen offset:1024 sc1\n"
    "buffer_load_dwordx4 v[116:119], v178, s[4:7], s54 offen sc1\n"
    "buffer_load_dwordx4 v[120:123], v178, s[4:7], s57 offen sc1\n"
    "buffer_load_dwordx4 v[124:127], v178, s[4:7], s60 offen sc1\n"
    "buffer_load_dwordx4 v[128:131], v178, s[4:7], s63 offen sc1\n"
    "buffer_load_dwordx4 v[132:135], v178, s[4:7], s64 offen sc1\n"
    "buffer_load_dwordx2 v[136:137], v14, s[4:7], s26 offen offset:1024 sc1\n"
    "buffer_load_dwordx4 v[140:143], v179, s[4:7], s54 offen sc1\n"
    "s_waitcnt vmcnt(0)\n"
    "v_max3_u32 v7, v108, v100, v104\n"
    "v_max_u32 v7, v7, v96\n"
    "v_cmp_eq_u32 vcc, -1, v7\n"
    "s_cmp_lg_u64 vcc, 0\n"
    "s_cbranch_scc1 f10s_65_%=\n"
    "s_nop 3\n"
    "s_branch f10r_66_%=\n"
    "f11s_67_%=:\n"
    "s_waitcnt vmcnt(0) lgkmcnt(0)\n"
    "s_nop 7\n"
    "s_cmp_eq_u32 s30, 0\n"
    "s_cbranch_scc1 f11r_68_%=\n"
    "s_sub_u32 s30, s30, 1\n"
    "buffer_load_dwordx2 v[112:113], v14, s[4:7], s25 offen offset:1024 sc1\n"
    "buffer_load_dwordx4 v[116:119], v178, s[4:7], s54 offen sc1\n"
    "buffer_load_dwordx4 v[120:123], v178, s[4:7], s57 offen sc1\n"
    "buffer_load_dwordx4 v[124:127], v178, s[4:7], s60 offen sc1\n"
    "buffer_load_dwordx4 v[128:131], v178, s[4:7], s63 offen sc1\n"
    "buffer_load_dwordx4 v[132:135], v178, s[4:7], s64 offen sc1\n"
    "buffer_load_dwordx2 v[136:137], v14, s[4:7], s26 offen offset:1024 sc1\n"
    "buffer_load_dwordx4 v[140:143], v179, s[4:7], s54 offen sc1\n"
    "buffer_load_dwordx4 v[144:147], v179, s[4:7], s57 offen sc1\n"
    "buffer_load_dwordx4 v[148:151], v179, s[4:7], s60 offen sc1\n"
    "buffer_load_dwordx4 v[152:155], v179, s[4:7], s63 offen sc1\n"
    "buffer_load_dwordx4 v[156:159], v179, s[4:7], s64 offen sc1\n"
    "s_waitcnt vmcnt(0)\n"
    "v_max3_u32 v7, v124, v116, v120\n"
    "v_max_u32 v7, v7, v112\n"
    "v_cmp_eq_u32 vcc, -1, v7\n"
    "s_cmp_lg_u64 vcc, 0\n"
    "s_cbranch_scc1 f11s_67_%=\n"
    "s_nop 3\n"
    "s_branch f11r_68_%=\n"
    "f12s_69_%=:\n"
    "s_waitcnt vmcnt(0) lgkmcnt(0)\n"
    "s_nop 7\n"
    "s_cmp_eq_u32 s30, 0\n"
    "s_cbranch_scc1 f12r_70_%=\n"
    "s_sub_u32 s30, s30, 1\n"
    "buffer_load_dwordx4 v[128:131], v178, s[4:7], s63 offen sc1\n"
    "buffer_load_dwordx4 v[132:135], v178, s[4:7], s64 offen sc1\n"
    "buffer_load_dwordx2 v[136:137], v14, s[4:7], s26 offen offset:1024 sc1\n"
    "buffer_load_dwordx4 v[140:143], v179, s[4:7], s54 offen sc1\n"
    "buffer_load_dwordx4 v[144:147], v179, s[4:7], s57 offen sc1\n"
    "buffer_load_dwordx4 v[148:151], v179, s[4:7], s60 offen sc1\n"
    "buffer_load_dwordx4 v[152:155], v179, s[4:7], s63 offen sc1\n"
    "buffer_load_dwordx4 v[156:159], v179, s[4:7], s64 offen sc1\n"
    "s_waitcnt vmcnt(0)\n"
    "v_max3_u32 v7, v140, v132, v136\n"
    "v_max_u32 v7, v7, v128\n"
    "v_cmp_eq_u32 vcc, -1, v7\n"
    "s_cmp_lg_u64 vcc, 0\n"
    "s_cbranch_scc1 f12s_69_%=\n"
    "s_nop 3\n"
    "s_branch f12r_70_%=\n"
    "f13s_71_%=:\n"
    "s_waitcnt vmcnt(0) lgkmcnt(0)\n"
    "s_nop 7\n"
    "s_cmp_eq_u32 s30, 0\n"
    "s_cbranch_scc1 f13r_72_%=\n"
    "s_sub_u32 s30, s30, 1\n"
    "buffer_load_dwordx4 v[144:147], v179, s[4:7], s57 offen sc1\n"
    "buffer_load_dwordx4 v[148:151], v179, s[4:7], s60 offen sc1\n"
    "buffer_load_dwordx4 v[152:155], v179, s[4:7], s63 offen sc1\n"
    "buffer_load_dwordx4 v[156:159], v179, s[4:7], s64 offen sc1\n"
    "s_waitcnt vmcnt(0)\n"
    "v_max3_u32 v7, v156, v148, v152\n"
    "v_max_u32 v7, v7, v144\n"
    "v_cmp_eq_u32 vcc, -1, v7\n"
    "s_cmp_lg_u64 vcc, 0\n"
    "s_cbranch_scc1 f13s_71_%=\n"
    "s_nop 3\n"
    "s_branch f13r_72_%=\n"
    "L_end_%=:\n"
    ".pushsection .rodata\n"
    ".p2align 7\n"
    "T_st_%=:\n"
    ".set tb_,0\n"
    ".rept 256\n"
    ".set txc_,(tb_&7)\n"
    ".set trl_,(tb_>>6)\n"
    ".set tR_,((4*(txc_>>1))+trl_)\n"
    ".set tj8_,((tb_>>3)&7)\n"
    ".set te_,(txc_&1)\n"
    ".set tp_,((8*te_)+tj8_)\n"
    ".set thf_,(tj8_>>2)\n"
    ".set tct_,((4*te_)+(tj8_&3))\n"
    ".set tri_,((2*trl_)+thf_)\n"
    ".set tci_,(tj8_&3)\n"
    ".set trot_,((10*(tj8_&7))>>3)\n"
    ".set tkv_,(tb_<200)\n"
    ".set tkb_,((tkv_&(tb_/20))+((~tkv_)&(2*((tb_/10)-20))))\n"
    ".set tst_,((tkv_&(10+(tb_%%20)))+((~tkv_)&(tb_%%10)))\n"
    ".set tm1_,(tb_<108)\n"
    ".set tm2_,((tb_>=108)&((tb_<200)+(tb_>=240)))\n"
    ".set tr3_,(thf_*0)\n"
    ".set tr4_,((tj8_&1)*0)\n"
    ".set tr5_,0\n"
    ".set tfs_,(tst_%%10)\n"
    ".set tfp_,((((tfs_*8)+4)/5)+((tst_/10)-1))\n"
    ".set tw_,0\n"
    ".rept 4\n"
    ".set trt3_,((((2*tR_)+thf_)*4)+tw_)\n"
    ".set toc_,((tm1_&((20*(tb_/27))+(5*tw_)))+((~tm1_)&((2*(((tb_<200)&(tb_-108))+((~(tb_<200))&(tb_-148))))+(tw_>>1))))\n"
    ".set td2_,((tm2_&((6209536+((toc_/54)*71680))+((((((((toc_%%54)%%14)<12)&((12*((toc_%%54)/14))+((toc_%%54)%%14)))+((~(((toc_%%54)%%14)<12))&(((48+(2*((toc_%%54)/14)))+((toc_%%54)%%14))-12)))>>2)*5120)+(((((((toc_%%54)%%14)<12)&((12*((toc_%%54)/14))+((toc_%%54)%%14)))+((~(((toc_%%54)%%14)<12))&(((48+(2*((toc_%%54)/14)))+((toc_%%54)%%14))-12)))&3)*256))))+((~tm2_)&1073741824))\n"
    ".set twi_,((4*tb_)+tw_)\n"
    ".long ((((tb_<250)&(tw_<(2+tkv_)))&(98304+((((tst_*10)+tkb_)+tw_)*512)))+((~((tb_<250)&(tw_<(2+tkv_))))&1073741824)),((((tkv_&(((tfp_*5)>>3)==tfs_))&(((4*tkb_)+tw_)<32))&((8503296+(tfp_*32768))+(((4*tkb_)+tw_)*1024)))+((~((tkv_&(((tfp_*5)>>3)==tfs_))&(((4*tkb_)+tw_)<32)))&1073741824)),(((tw_==0)&(65536+(tb_*32)))+((~(tw_==0))&1073741824)),(((tw_==0)&(32768+(tb_*4)))+((~(tw_==0))&1073741824))\n"
    ".long ((9027584+(tb_*10240))+(((tw_<2)&(3072*tw_))+((~(tw_<2))&((2048*tw_)+2048)))),(((tw_<2)&(((9027584+(tb_*10240))+(3072*tw_))+2048))+((~(tw_<2))&1073741824)),(11649024+(((4*tb_)+tw_)*4096)),(20037632+(((4*tb_)+tw_)*2048))\n"
    ".long (2981888+((te_*409600)+(((4*(tb_>>3))+tw_)*3200))),(((tw_<2)&4096)+((~(tw_<2))&1073741824)),((tm1_&24)+((~tm1_)&32)),((((tm1_&(tb_%%27))+((~tm1_)&(27+(tw_&1))))*2048)+(((1023-(4*tb_))-tw_)*384))\n"
    ".long (8503296+(((16*tp_)+tR_)*2048)),(tw_*1024),(((tm1_&(tw_==0))&(3944448+((tb_/27)*256)))+((~(tm1_&(tw_==0)))&1073741824)),0\n"
    ".long (((524288+(tp_*153600))+(((((5*tw_)+0)+(2*trot_))%%20)*1024))-0),(((524288+(tp_*153600))+(((((5*tw_)+1)+(2*trot_))%%20)*1024))-1024),(((524288+(tp_*153600))+(((((5*tw_)+2)+(2*trot_))%%20)*1024))-2048),(((524288+(tp_*153600))+(((((5*tw_)+3)+(2*trot_))%%20)*1024))-3072)\n"
    ".long (((524288+(tp_*153600))+(((((5*tw_)+4)+(2*trot_))%%20)*1024))-0),((((tw_+0)<10)&(((524288+(tp_*153600))+143360)+((((tw_+0)+trot_)%%10)*1024)))+((~((tw_+0)<10))&1073741824)),((((tw_+4)<10)&(((524288+(tp_*153600))+143360)+((((tw_+4)+trot_)%%10)*1024)))+((~((tw_+4)<10))&1073741824)),((((tw_+8)<10)&(((524288+(tp_*153600))+143360)+((((tw_+8)+trot_)%%10)*1024)))+((~((tw_+8)<10))&1073741824))\n"
    ".long ((tm1_&(((toc_+0)*27648)+((tb_%%27)*128)))+((~tm1_)&((tm2_&((toc_*10240)+(((5*(tw_&1))+0)*128)))+((~tm2_)&1073741824)))),((tm1_&(((toc_+1)*27648)+((tb_%%27)*128)))+((~tm1_)&((tm2_&((toc_*10240)+(((5*(tw_&1))+1)*128)))+((~tm2_)&1073741824)))),((tm1_&(((toc_+2)*27648)+((tb_%%27)*128)))+((~tm1_)&((tm2_&((toc_*10240)+(((5*(tw_&1))+2)*128)))+((~tm2_)&1073741824)))),((tm1_&(((toc_+3)*27648)+((tb_%%27)*128)))+((~tm1_)&((tm2_&((toc_*10240)+(((5*(tw_&1))+3)*128)))+((~tm2_)&1073741824))))\n"
    ".long ((tm1_&(((toc_+4)*27648)+((tb_%%27)*128)))+((~tm1_)&((tm2_&((toc_*10240)+(((5*(tw_&1))+4)*128)))+((~tm2_)&1073741824)))),((tkv_&(((twi_/10)*10240)+((twi_%%10)*128)))+((~tkv_)&1073741824)),((tkv_&(2981888+((((((((twi_/10)%%5)<4)&((4*((twi_/10)/5))+((twi_/10)%%5)))+((~(((twi_/10)%%5)<4))&(((64+(1*((twi_/10)/5)))+((twi_/10)%%5))-4)))>>2)*5120)+(((((((twi_/10)%%5)<4)&((4*((twi_/10)/5))+((twi_/10)%%5)))+((~(((twi_/10)%%5)<4))&(((64+(1*((twi_/10)/5)))+((twi_/10)%%5))-4)))&3)*256))))+((~tkv_)&1073741824)),(((((29+((twi_%%10)/5))*2048)+(((twi_%%5)>>2)*1024))+(((twi_%%5)&3)*4))+((1023-twi_)*384))\n"
    ".long ((tm1_&(3801088+((((((((toc_+0)%%10)<8)&((8*((toc_+0)/10))+((toc_+0)%%10)))+((~(((toc_+0)%%10)<8))&(((64+(2*((toc_+0)/10)))+((toc_+0)%%10))-8)))>>2)*7168)+(((((((toc_+0)%%10)<8)&((8*((toc_+0)/10))+((toc_+0)%%10)))+((~(((toc_+0)%%10)<8))&(((64+(2*((toc_+0)/10)))+((toc_+0)%%10))-8)))&3)*256))))+((~tm1_)&td2_)),((tm1_&(3801088+((((((((toc_+1)%%10)<8)&((8*((toc_+1)/10))+((toc_+1)%%10)))+((~(((toc_+1)%%10)<8))&(((64+(2*((toc_+1)/10)))+((toc_+1)%%10))-8)))>>2)*7168)+(((((((toc_+1)%%10)<8)&((8*((toc_+1)/10))+((toc_+1)%%10)))+((~(((toc_+1)%%10)<8))&(((64+(2*((toc_+1)/10)))+((toc_+1)%%10))-8)))&3)*256))))+((~tm1_)&td2_)),((tm1_&(3801088+((((((((toc_+2)%%10)<8)&((8*((toc_+2)/10))+((toc_+2)%%10)))+((~(((toc_+2)%%10)<8))&(((64+(2*((toc_+2)/10)))+((toc_+2)%%10))-8)))>>2)*7168)+(((((((toc_+2)%%10)<8)&((8*((toc_+2)/10))+((toc_+2)%%10)))+((~(((toc_+2)%%10)<8))&(((64+(2*((toc_+2)/10)))+((toc_+2)%%10))-8)))&3)*256))))+((~tm1_)&td2_)),((tm1_&(3801088+((((((((toc_+3)%%10)<8)&((8*((toc_+3)/10))+((toc_+3)%%10)))+((~(((toc_+3)%%10)<8))&(((64+(2*((toc_+3)/10)))+((toc_+3)%%10))-8)))>>2)*7168)+(((((((toc_+3)%%10)<8)&((8*((toc_+3)/10))+((toc_+3)%%10)))+((~(((toc_+3)%%10)<8))&(((64+(2*((toc_+3)/10)))+((toc_+3)%%10))-8)))&3)*256))))+((~tm1_)&td2_))\n"
    ".long ((tm1_&(3801088+((((((((toc_+4)%%10)<8)&((8*((toc_+4)/10))+((toc_+4)%%10)))+((~(((toc_+4)%%10)<8))&(((64+(2*((toc_+4)/10)))+((toc_+4)%%10))-8)))>>2)*7168)+(((((((toc_+4)%%10)<8)&((8*((toc_+4)/10))+((toc_+4)%%10)))+((~(((toc_+4)%%10)<8))&(((64+(2*((toc_+4)/10)))+((toc_+4)%%10))-8)))&3)*256))))+((~tm1_)&td2_)),(8503296+((16*tp_)*2048)),((trt3_*20480)+(tct_*160)),(((9027584+(tR_*163840))+(tw_*40960))+(tp_*1024))\n"
    ".long ((((9027584+(tR_*163840))+(tw_*40960))+16384)+(tp_*256)),(((2981888+(tct_*102400))+(((0+tr3_)%%5)*20480))+(tw_*5120)),(((2981888+(tct_*102400))+(((1+tr3_)%%5)*20480))+(tw_*5120)),(((2981888+(tct_*102400))+(((2+tr3_)%%5)*20480))+(tw_*5120))\n"
    ".long (((2981888+(tct_*102400))+(((3+tr3_)%%5)*20480))+(tw_*5120)),(((2981888+(tct_*102400))+(((4+tr3_)%%5)*20480))+(tw_*5120)),(tw_*5120),(tw_*3072)\n"
    ".long (((9027584+(tR_*163840))+((trt3_&7)*20480))+(((0+tr3_)%%5)*4096)),(((9027584+(tR_*163840))+((trt3_&7)*20480))+(((1+tr3_)%%5)*4096)),(((9027584+(tR_*163840))+((trt3_&7)*20480))+(((2+tr3_)%%5)*4096)),(((9027584+(tR_*163840))+((trt3_&7)*20480))+(((3+tr3_)%%5)*4096))\n"
    ".long (((9027584+(tR_*163840))+((trt3_&7)*20480))+(((4+tr3_)%%5)*4096)),(((11649024+(tR_*172032))+((trt3_&7)*21504))+(tct_*2048)),((((11649024+(tR_*172032))+((trt3_&7)*21504))+16384)+(tct_*512)),((((11649024+(tR_*172032))+((trt3_&7)*21504))+20480)+(tct_*8))\n"
    ".long ((3801088+(tp_*150528))+(((((0+tr4_)%%5)*4)+tw_)*7168)),((3801088+(tp_*150528))+(((((1+tr4_)%%5)*4)+tw_)*7168)),((3801088+(tp_*150528))+(((((2+tr4_)%%5)*4)+tw_)*7168)),((3801088+(tp_*150528))+(((((3+tr4_)%%5)*4)+tw_)*7168))\n"
    ".long ((3801088+(tp_*150528))+(((((4+tr4_)%%5)*4)+tw_)*7168)),(((6209536+(tct_*286720))+(tw_*71680))+(tr5_*5120)),(((6209536+(tct_*286720))+(tw_*71680))+61440),((((tp_&3)==3)&1073741824)+((~((tp_&3)==3))&4096))\n"
    ".long (((11649024+(tR_*172032))+(tw_*43008))+(((0+tr4_)%%5)*4096)),(((11649024+(tR_*172032))+(tw_*43008))+(((1+tr4_)%%5)*4096)),(((11649024+(tR_*172032))+(tw_*43008))+(((2+tr4_)%%5)*4096)),(((11649024+(tR_*172032))+(tw_*43008))+(((3+tr4_)%%5)*4096))\n"
    ".long (((11649024+(tR_*172032))+(tw_*43008))+(((4+tr4_)%%5)*4096)),(((11649024+(tR_*172032))+(tw_*43008))+20480),(((14401536+(((4*tR_)+tw_)*114688))+((tp_>>2)*14336))+((tp_&3)*3072)),((((tp_&3)==3)&1073741824)+((~((tp_&3)==3))&((((14401536+(((4*tR_)+tw_)*114688))+((tp_>>2)*14336))+12288)+((tp_&3)*512))))\n"
    ".long ((14401536+(((trt3_&126)^0)*57344))+(tw_*14336)),((14401536+(((trt3_&126)^2)*57344))+(tw_*14336)),(((tw_==0)&(33792+(tb_*4)))+((~(tw_==0))&1073741824)),(((tw_&1)^1)<<16)\n"
    ".long ((((tb_+tw_)==0)&0)+((~((tb_+tw_)==0))&1073741824)),(((((2*tw_)+0)<(7+((tp_&3)==3)))&(((3801088+(tp_*150528))+143360)+(tw_*2048)))+((~(((2*tw_)+0)<(7+((tp_&3)==3))))&1073741824)),(((((2*tw_)+1)<(7+((tp_&3)==3)))&(((3801088+(tp_*150528))+143360)+(tw_*2048)))+((~(((2*tw_)+1)<(7+((tp_&3)==3))))&1073741824)),0\n"
    ".long (((0+tr5_)%%12)*1024),(((1+tr5_)%%12)*1024),(((2+tr5_)%%12)*1024),(((3+tr5_)%%12)*1024)\n"
    ".long (((4+tr5_)%%12)*1024),(((5+tr5_)%%12)*1024),(((6+tr5_)%%12)*1024),(((7+tr5_)%%12)*1024)\n"
    ".long (((8+tr5_)%%12)*1024),(((9+tr5_)%%12)*1024),(((10+tr5_)%%12)*1024),(((11+tr5_)%%12)*1024)\n"
    ".long 12288,((((tw_>>1)^0)*20480)+(tw_*5120)),((((tw_>>1)^1)*20480)+(tw_*5120)),(((tw_>>1)*20480)+((tw_&1)*2048))\n"
    ".set tw_,tw_+1\n"
    ".endr\n"
    ".set tb_,tb_+1\n"
    ".endr\n"
    ".p2align 7\n"
    "T_lt_%=:\n"
    ".set tw_,0\n"
    ".rept 4\n"
    ".set tl_,0\n"
    ".rept 64\n"
    ".set ti_,(tl_&15)\n"
    ".set tg_,(tl_>>4)\n"
    ".set tt_,((64*tw_)+tl_)\n"
    ".set tqd_,(tt_/21)\n"
    ".set tsl_,(tt_%%21)\n"
    ".set thd_,(1&(tqd_>4))\n"
    ".set tq0_,((4*((528>>(tw_*4))&15))+tg_)\n"
    ".set tv0_,(((16*((4096>>(tw_*4))&15))+ti_)-7)\n"
    ".set ts0_,(((tq0_<5)&tv0_)+((~(tq0_<5))&(((tv0_==0)&0)+((~(tv0_==0))&(((tv0_>20)&(tv0_-20))+((~(tv0_>20))&(-1)))))))\n"
    ".set tq1_,((4*((8481>>(tw_*4))&15))+tg_)\n"
    ".set tv1_,(((16*((4386>>(tw_*4))&15))+ti_)-7)\n"
    ".set ts1_,(((tq1_<5)&tv1_)+((~(tq1_<5))&(((tv1_==0)&0)+((~(tv1_==0))&(((tv1_>20)&(tv1_-20))+((~(tv1_>20))&(-1)))))))\n"
    ".long (159744+(((tqd_<10)&(((((tsl_==0)&(40+thd_))+((~(tsl_==0))&(((20*thd_)+tsl_)-1)))*96)+((((tqd_<8)&((2*(tqd_&3))+(tqd_>>2)))+((~(tqd_<8))&tqd_))*8)))+((~(tqd_<10))&4032))),(((tt_<210)&(tt_*8))+((~(tt_<210))&1073741824)),(((((ts0_>=0)&(ts0_<21))&(tq0_<10))&(((tq0_*21)+ts0_)*8))+((~(((ts0_>=0)&(ts0_<21))&(tq0_<10)))&1073741824)),(((((ts1_>=0)&(ts1_<21))&(tq1_<10))&(((tq1_*21)+ts1_)*8))+((~(((ts1_>=0)&(ts1_<21))&(tq1_<10)))&1073741824))\n"
    ".set tl_,tl_+1\n"
    ".endr\n"
    ".set tl_,0\n"
    ".rept 64\n"
    ".set ti_,(tl_&15)\n"
    ".set tg_,(tl_>>4)\n"
    ".set tt_,((64*tw_)+tl_)\n"
    ".set tqd_,(tt_/21)\n"
    ".set tsl_,(tt_%%21)\n"
    ".set thd_,(1&(tqd_>4))\n"
    ".set tq0_,((4*((528>>(tw_*4))&15))+tg_)\n"
    ".set tv0_,(((16*((4096>>(tw_*4))&15))+ti_)-7)\n"
    ".set ts0_,(((tq0_<5)&tv0_)+((~(tq0_<5))&(((tv0_==0)&0)+((~(tv0_==0))&(((tv0_>20)&(tv0_-20))+((~(tv0_>20))&(-1)))))))\n"
    ".set tq1_,((4*((8481>>(tw_*4))&15))+tg_)\n"
    ".set tv1_,(((16*((4386>>(tw_*4))&15))+ti_)-7)\n"
    ".set ts1_,(((tq1_<5)&tv1_)+((~(tq1_<5))&(((tv1_==0)&0)+((~(tv1_==0))&(((tv1_>20)&(tv1_-20))+((~(tv1_>20))&(-1)))))))\n"
    ".long ((159744+(((8*(ti_>>2))+(ti_&3))*96))+(tg_*16)),((159744+(((8*(ti_>>2))+(ti_&3))*96))+(tg_*8)),((159744+((32+(ti_&3))*96))+(tg_*16)),((159744+((32+(ti_&3))*96))+(tg_*8))\n"
    ".set tl_,tl_+1\n"
    ".endr\n"
    ".set tl_,0\n"
    ".rept 64\n"
    ".set ti_,(tl_&15)\n"
    ".set tg_,(tl_>>4)\n"
    ".set tt_,((64*tw_)+tl_)\n"
    ".set tqd_,(tt_/21)\n"
    ".set tsl_,(tt_%%21)\n"
    ".set thd_,(1&(tqd_>4))\n"
    ".set tq0_,((4*((528>>(tw_*4))&15))+tg_)\n"
    ".set tv0_,(((16*((4096>>(tw_*4))&15))+ti_)-7)\n"
    ".set ts0_,(((tq0_<5)&tv0_)+((~(tq0_<5))&(((tv0_==0)&0)+((~(tv0_==0))&(((tv0_>20)&(tv0_-20))+((~(tv0_>20))&(-1)))))))\n"
    ".set tq1_,((4*((8481>>(tw_*4))&15))+tg_)\n"
    ".set tv1_,(((16*((4386>>(tw_*4))&15))+ti_)-7)\n"
    ".set ts1_,(((tq1_<5)&tv1_)+((~(tq1_<5))&(((tv1_==0)&0)+((~(tv1_==0))&(((tv1_>20)&(tv1_-20))+((~(tv1_>20))&(-1)))))))\n"
    ".long ((159744+((40+(1&(((ti_>>2)+(ti_&1))>2)))*96))+(tg_*16)),((159744+((40+(1&(((ti_>>2)+(ti_&1))>2)))*96))+(tg_*8)),(((tg_==3)&(ti_*16))+((~(tg_==3))&1073741824)),((143360+(tg_*128))+((tl_&7)*16))\n"
    ".set tl_,tl_+1\n"
    ".endr\n"
    ".set tl_,0\n"
    ".rept 64\n"
    ".set ti_,(tl_&15)\n"
    ".set tg_,(tl_>>4)\n"
    ".set tt_,((64*tw_)+tl_)\n"
    ".set tqd_,(tt_/21)\n"
    ".set tsl_,(tt_%%21)\n"
    ".set thd_,(1&(tqd_>4))\n"
    ".set tq0_,((4*((528>>(tw_*4))&15))+tg_)\n"
    ".set tv0_,(((16*((4096>>(tw_*4))&15))+ti_)-7)\n"
    ".set ts0_,(((tq0_<5)&tv0_)+((~(tq0_<5))&(((tv0_==0)&0)+((~(tv0_==0))&(((tv0_>20)&(tv0_-20))+((~(tv0_>20))&(-1)))))))\n"
    ".set tq1_,((4*((8481>>(tw_*4))&15))+tg_)\n"
    ".set tv1_,(((16*((4386>>(tw_*4))&15))+ti_)-7)\n"
    ".set ts1_,(((tq1_<5)&tv1_)+((~(tq1_<5))&(((tv1_==0)&0)+((~(tv1_==0))&(((tv1_>20)&(tv1_-20))+((~(tv1_>20))&(-1)))))))\n"
    ".long ((((528>>(tw_*4))&15)*4096)+(tl_*16)),((((13380>>(tw_*4))&15)*4096)+(tl_*16)),((((8481>>(tw_*4))&15)*4096)+(tl_*16)),((((13141>>(tw_*4))&15)*4096)+(tl_*16))\n"
    ".set tl_,tl_+1\n"
    ".endr\n"
    ".set tw_,tw_+1\n"
    ".endr\n"
    ".p2align 7\n"
    "T_bt_%=:\n"
    ".set tb_,0\n"
    ".rept 256\n"
    ".set txc_,(tb_&7)\n"
    ".set trl_,(tb_>>6)\n"
    ".set tR_,((4*(txc_>>1))+trl_)\n"
    ".set tj8_,((tb_>>3)&7)\n"
    ".set te_,(txc_&1)\n"
    ".set tp_,((8*te_)+tj8_)\n"
    ".set thf_,(tj8_>>2)\n"
    ".set tct_,((4*te_)+(tj8_&3))\n"
    ".set tri_,((2*trl_)+thf_)\n"
    ".set tci_,(tj8_&3)\n"
    ".set trot_,((10*(tj8_&7))>>3)\n"
    ".set tkv_,(tb_<200)\n"
    ".set tkb_,((tkv_&(tb_/20))+((~tkv_)&(2*((tb_/10)-20))))\n"
    ".set tst_,((tkv_&(10+(tb_%%20)))+((~tkv_)&(tb_%%10)))\n"
    ".set tm1_,(tb_<108)\n"
    ".set tm2_,((tb_>=108)&((tb_<200)+(tb_>=240)))\n"
    ".set tl_,0\n"
    ".rept 64\n"
    ".set tco_,((tst_*64)+tl_)\n"
    ".set tpp_,(tco_/40)\n"
    ".set tcc_,(tco_%%40)\n"
    ".set tsx_,(tcc_+(40*(tpp_>>5)))\n"
    ".set tsd_,((tkv_&256)+((~tkv_)&(((tcc_<32)&256)+((~(tcc_<32))&128))))\n"
    ".set tl59_,(((tl_<60)&tl_)+((~(tl_<60))&59))\n"
    ".set tcs_,(((600*(tl59_/20))+(2*tl59_))+(40*tp_))\n"
    ".long (((tb_<250)&(((((524288+((tkv_&0)+((~tkv_)&102400)))+((tpp_&15)*153600))+((tsx_>>4)*20480))+((tsx_&15)*16))+((8*tkb_)*tsd_)))+((~(tb_<250))&1073741824)),tsd_,((98304+((tcs_>>6)*5120))+((tcs_&63)*8)),(((tl_>>3)*((tm1_&3456)+((~tm1_)&1280)))+((tl_&7)*16))\n"
    ".set tl_,tl_+1\n"
    ".endr\n"
    ".set tb_,tb_+1\n"
    ".endr\n"
    ".p2align 7\n"
    "T_ct_%=:\n"
    ".set tc_,0\n"
    ".rept 31\n"
    ".set tl_,0\n"
    ".rept 64\n"
    ".set ti_,(tl_&15)\n"
    ".set tg_,(tl_>>4)\n"
    ".set tcb_,((4*tc_)+tg_)\n"
    ".set td1_,((((4*(tcb_/27))+((tcb_%%27)/7))*150528)+(((((tcb_%%27)%%7)==6)&((((tcb_%%27)%%7)*1024)+(ti_*16)))+((~(((tcb_%%27)%%7)==6))&(((((((tcb_%%27)%%7)&6)+(ti_>>3))*1024)+((((tcb_%%27)%%7)&1)*128))+((ti_&7)*16)))))\n"
    ".set tc0_,((4*((5*((tc_+1)&1))+0))+tg_)\n"
    ".set tc1_,((4*((5*((tc_+1)&1))+1))+tg_)\n"
    ".set tc2_,((4*((5*((tc_+1)&1))+2))+tg_)\n"
    ".set tc3_,((4*((5*((tc_+1)&1))+3))+tg_)\n"
    ".set tc4_,((4*((5*((tc_+1)&1))+4))+tg_)\n"
    ".long (((tc_<27)&td1_)+((~(tc_<27))&(((tc0_/5)*(((tc_<29)&286720)+((~(tc_<29))&102400)))+((((tc0_%%5)==4)&(((tc0_%%5)*1024)+(ti_*16)))+((~((tc0_%%5)==4))&((((((tc0_%%5)&6)+(ti_>>3))*1024)+(((tc0_%%5)&1)*128))+((ti_&7)*16))))))),(((tc_<27)&td1_)+((~(tc_<27))&(((tc1_/5)*(((tc_<29)&286720)+((~(tc_<29))&102400)))+((((tc1_%%5)==4)&(((tc1_%%5)*1024)+(ti_*16)))+((~((tc1_%%5)==4))&((((((tc1_%%5)&6)+(ti_>>3))*1024)+(((tc1_%%5)&1)*128))+((ti_&7)*16))))))),(((tc_<27)&td1_)+((~(tc_<27))&(((tc2_/5)*(((tc_<29)&286720)+((~(tc_<29))&102400)))+((((tc2_%%5)==4)&(((tc2_%%5)*1024)+(ti_*16)))+((~((tc2_%%5)==4))&((((((tc2_%%5)&6)+(ti_>>3))*1024)+(((tc2_%%5)&1)*128))+((ti_&7)*16))))))),(((tc_<27)&td1_)+((~(tc_<27))&(((tc3_/5)*(((tc_<29)&286720)+((~(tc_<29))&102400)))+((((tc3_%%5)==4)&(((tc3_%%5)*1024)+(ti_*16)))+((~((tc3_%%5)==4))&((((((tc3_%%5)&6)+(ti_>>3))*1024)+(((tc3_%%5)&1)*128))+((ti_&7)*16)))))))\n"
    ".set tl_,tl_+1\n"
    ".endr\n"
    ".set tl_,0\n"
    ".rept 64\n"
    ".set ti_,(tl_&15)\n"
    ".set tg_,(tl_>>4)\n"
    ".set tcb_,((4*tc_)+tg_)\n"
    ".set td1_,((((4*(tcb_/27))+((tcb_%%27)/7))*150528)+(((((tcb_%%27)%%7)==6)&((((tcb_%%27)%%7)*1024)+(ti_*16)))+((~(((tcb_%%27)%%7)==6))&(((((((tcb_%%27)%%7)&6)+(ti_>>3))*1024)+((((tcb_%%27)%%7)&1)*128))+((ti_&7)*16)))))\n"
    ".set tc0_,((4*((5*((tc_+1)&1))+0))+tg_)\n"
    ".set tc1_,((4*((5*((tc_+1)&1))+1))+tg_)\n"
    ".set tc2_,((4*((5*((tc_+1)&1))+2))+tg_)\n"
    ".set tc3_,((4*((5*((tc_+1)&1))+3))+tg_)\n"
    ".set tc4_,((4*((5*((tc_+1)&1))+4))+tg_)\n"
    ".long (((tc_<27)&td1_)+((~(tc_<27))&(((tc4_/5)*(((tc_<29)&286720)+((~(tc_<29))&102400)))+((((tc4_%%5)==4)&(((tc4_%%5)*1024)+(ti_*16)))+((~((tc4_%%5)==4))&((((((tc4_%%5)&6)+(ti_>>3))*1024)+(((tc4_%%5)&1)*128))+((ti_&7)*16))))))),0,0,0\n"
    ".set tl_,tl_+1\n"
    ".endr\n"
    ".set tc_,tc_+1\n"
    ".endr\n"
    "T_end_%=:\n"
    ".popsection\n"
    :: "s"(__builtin_amdgcn_kernarg_segment_ptr()), "s"(blockIdx.x), "s"(w)
    : "v0", "v1", "v2", "v3", "v4", "v5", "v6", "v7", "v8", "v9", "v10", "v11", "v12", "v13", "v14", "v15", "v16", "v17", "v18", "v19", "v20", "v21", "v22", "v23", "v24", "v25", "v26", "v27", "v28", "v29", "v30", "v31", "v32", "v33", "v34", "v35", "v36", "v37", "v38", "v39", "v40", "v41", "v42", "v43", "v44", "v45", "v46", "v47", "v48", "v49", "v50", "v51", "v52", "v53", "v54", "v55", "v56", "v57", "v58", "v59", "v60", "v61", "v62", "v63", "v64", "v65", "v66", "v67", "v68", "v69", "v70", "v71", "v72", "v73", "v74", "v75", "v76", "v77", "v78", "v79", "v80", "v81", "v82", "v83", "v84", "v85", "v86", "v87", "v88", "v89", "v90", "v91", "v92", "v93", "v94", "v95", "v96", "v97", "v98", "v99", "v100", "v101", "v102", "v103", "v104", "v105", "v106", "v107", "v108", "v109", "v110", "v111", "v112", "v113", "v114", "v115", "v116", "v117", "v118", "v119", "v120", "v121", "v122", "v123", "v124", "v125", "v126", "v127", "v128", "v129", "v130", "v131", "v132", "v133", "v134", "v135", "v136", "v137", "v138", "v139", "v140", "v141", "v142", "v143", "v144", "v145", "v146", "v147", "v148", "v149", "v150", "v151", "v152", "v153", "v154", "v155", "v156", "v157", "v158", "v159", "v160", "v161", "v162", "v163", "v164", "v165", "v166", "v167", "v168", "v169", "v170", "v171", "v172", "v173", "v174", "v175", "v176", "v177", "v178", "v179", "v180", "v181", "v182", "v183", "v184", "v185", "v186", "v187", "v188", "v189", "v190", "v191", "v192", "v193", "v194", "v195", "v196", "v197", "v198", "v199", "v200", "v201", "v202", "v203", "v204", "v205", "v206", "v207", "v208", "v209", "v210", "v211", "v212", "v213", "v214", "v215", "v216", "v217", "v218", "v219", "v220", "v221", "v222", "v223", "v224", "v225", "v226", "v227", "v228", "v229", "v230", "v231", "v232", "v233", "v234", "v235", "v236", "v237", "v238", "v239", "v240", "v241", "v242", "v243", "v244", "v245", "v246", "v247", "v248", "v249", "v250", "v251", "v252", "v253", "v254", "v255", "a0", "a1", "a2", "a3", "a4", "a5", "a6", "a7", "a8", "a9", "a10", "a11", "a12", "a13", "a14", "a15", "a16", "a17", "a18", "a19", "a20", "a21", "a22", "a23", "a24", "a25", "a26", "a27", "a28", "a29", "a30", "a31", "a32", "a33", "a34", "a35", "a36", "a37", "a38", "a39", "a40", "a41", "a42", "a43", "a44", "a45", "a46", "a47", "a48", "a49", "a50", "a51", "a52", "a53", "a54", "a55", "a56", "a57", "a58", "a59", "a60", "a61", "a62", "a63", "a64", "a65", "a66", "a67", "a68", "a69", "a70", "a71", "a72", "a73", "a74", "a75", "a76", "a77", "a78", "a79", "a80", "a81", "a82", "a83", "a84", "a85", "a86", "a87", "a88", "a89", "a90", "a91", "a92", "a93", "a94", "a95", "a96", "a97", "a98", "a99", "a100", "a101", "a102", "a103", "a104", "a105", "a106", "a107", "a108", "a109", "a110", "a111", "a112", "a113", "a114", "a115", "a116", "a117", "a118", "a119", "a120", "a121", "a122", "a123", "a124", "a125", "a126", "a127", "a128", "a129", "a130", "a131", "a132", "a133", "a134", "a135", "a136", "a137", "a138", "a139", "a140", "a141", "a142", "a143", "a144", "a145", "a146", "a147", "a148", "a149", "a150", "a151", "a152", "a153", "a154", "a155", "a156", "a157", "a158", "a159", "a160", "a161", "a162", "a163", "a164", "a165", "a166", "a167", "a168", "a169", "a170", "a171", "a172", "a173", "a174", "a175", "a176", "a177", "a178", "a179", "a180", "a181", "a182", "a183", "a184", "a185", "a186", "a187", "a188", "a189", "a190", "a191", "a192", "a193", "a194", "a195", "a196", "a197", "a198", "a199", "a200", "a201", "a202", "a203", "a204", "a205", "a206", "a207", "a208", "a209", "a210", "a211", "a212", "a213", "a214", "a215", "a216", "a217", "a218", "a219", "a220", "a221", "a222", "a223", "a224", "a225", "a226", "a227", "a228", "a229", "a230", "a231", "a232", "a233", "a234", "a235", "a236", "a237", "a238", "a239", "a240", "a241", "a242", "a243", "a244", "a245", "a246", "a247", "a248", "a249", "a250", "a251", "a252", "a253", "a254", "a255", "s4", "s5", "s6", "s7", "s8", "s9", "s10", "s11", "s12", "s13", "s14", "s15", "s16", "s17", "s18", "s19", "s20", "s21", "s22", "s23", "s24", "s25", "s26", "s27", "s28", "s29", "s30", "s31", "s33", "s34", "s35", "s36", "s37", "s38", "s39", "s40", "s41", "s42", "s43", "s44", "s45", "s46", "s47", "s48", "s49", "s50", "s51", "s52", "s53", "s54", "s55", "s56", "s57", "s58", "s59", "s60", "s61", "s62", "s63", "s64", "s65", "s66", "s67", "s68", "s69", "s70", "s71", "s72", "s73", "s74", "s75", "s76", "s77", "s78", "s79", "s80", "s81", "s82", "s83", "s84", "s85", "s86", "s87", "s88", "s89", "s90", "s91", "s92", "s93", "s94", "s95", "s96", "s97", "s98", "s99", "vcc", "memory");
}

extern "C" void kernel_launch(void* const* d_in, const int* in_sizes, int n_in, void* d_out, int out_size, void* d_ws, size_t ws_size, hipStream_t stream) {
  (void)hipFuncSetAttribute((const void*)fused_block, hipFuncAttributeMaxDynamicSharedMemorySize, 163840);
  fused_block<<<256, 256, 163840, stream>>>((const u16*)d_in[0], (const u16*)d_in[1], (const u16*)d_in[2], (const u16*)d_in[4], (const u16*)d_in[5], (u16*)d_out, (char*)d_ws);
}
```
